```python
import torch

def _softmax(x, axis=-1):
    return torch.softmax(x, dim=axis)

def _gelu(x):
    return torch.nn.functional.gelu(x, approximate='tanh')

def _silu(x):
    return torch.nn.functional.silu(x)

def _sigmoid(x):
    return torch.sigmoid(x)

def _layernorm(x, axis=-1, eps=1e-05):
    m = x.mean(dim=axis, keepdim=True)
    v = x.var(dim=axis, keepdim=True, unbiased=False)
    return (x - m) / torch.sqrt(v + eps)

def _scatter_add(tgt, idx, src, axis=0):
    return tgt.clone().index_add_(axis, idx, src)

def _segment_sum(src, seg, n):
    out = torch.zeros((n,) + src.shape[1:], dtype=src.dtype)
    return out.index_add_(0, seg, src)

def _pad(x, pw):
    flat = tuple((v for pair in reversed(pw) for v in pair))
    return torch.nn.functional.pad(x, flat)
kernel_name = 'parallel_sum__memory_aug_recurrent_mem_tr__dense_activation_squared_relu__prompt'

def setup_inputs():

    _w = lambda a: (lambda t: t.to(torch.bfloat16) if t.is_floating_point() else t)(torch.as_tensor(a))

    def _A_setup():
        B, S, Dm = (1, 2048, 448)
        H, Dh = (16, 256)
        M, seg = (8, 32)
        seg = min(seg, S)
        S = S // seg * seg
        return {'x': _w((lambda *s: torch.randn(*s, dtype=torch.bfloat16) * 0.05)(B, S, Dm)), 'mem0': _w((lambda *s: torch.randn(*s, dtype=torch.bfloat16) * 1.6)(B, M, Dm)), 'Wq': _w((lambda *s: torch.randn(*s, dtype=torch.bfloat16) * 0.8)(Dm, H, Dh) * Dm ** (-0.5)), 'Wk': _w((lambda *s: torch.randn(*s, dtype=torch.bfloat16) * 0.8)(Dm, H, Dh) * Dm ** (-0.5)), 'Wv': _w((lambda *s: torch.randn(*s, dtype=torch.bfloat16) * 1.6)(Dm, H, Dh) * Dm ** (-0.5)), 'Wo': _w((lambda *s: torch.randn(*s, dtype=torch.bfloat16) * 1.6)(H, Dh, Dm) * (H * Dh) ** (-0.5)), 'seg_len': seg}
    _A_in = _A_setup()

    def _B_setup():
        B, S, D, F = (1, 2048, 448, 1792)
        return {'x': _w((lambda *s: torch.randn(*s, dtype=torch.bfloat16) * 0.05)(B, S, D)), 'w1': _w((lambda *s: torch.randn(*s, dtype=torch.bfloat16) * 0.05)(D, F)), 'b1': _w((lambda *s: torch.randn(*s, dtype=torch.bfloat16) * 0.4)(F)), 'w2': _w((lambda *s: torch.randn(*s, dtype=torch.bfloat16) * 0.05)(F, D)), 'b2': _w((lambda *s: torch.randn(*s, dtype=torch.bfloat16) * 0.8)(D))}
    _B_in = _B_setup()
    return {'A_x': _A_in['x'], 'A_mem0': _A_in['mem0'], 'A_Wq': _A_in['Wq'], 'A_Wk': _A_in['Wk'], 'A_Wv': _A_in['Wv'], 'A_Wo': _A_in['Wo'], 'A_seg_len': _A_in['seg_len'], 'B_x': _B_in['x'], 'B_w1': _B_in['w1'], 'B_b1': _B_in['b1'], 'B_w2': _B_in['w2'], 'B_b2': _B_in['b2']}

def reference(A_x, A_mem0, A_Wq, A_Wk, A_Wv, A_Wo, A_seg_len, B_x, B_w1, B_b1, B_w2, B_b2):

    def _A_ref(x, mem0, Wq, Wk, Wv, Wo, seg_len):
        B, S, Dm = x.shape
        H, Dh = (Wq.shape[1], Wq.shape[2])
        M = mem0.shape[1]
        scale = Dh ** (-0.5)
        n_seg = S // seg_len
        L = M + seg_len
        idx = torch.arange(L)
        mask = (idx[:, None] < M) | (idx[:, None] >= idx[None, :])
        mem = mem0
        outs = []
        for s in range(n_seg):
            xs = x[:, s * seg_len:(s + 1) * seg_len, :].clone()
            h = torch.concatenate([mem, xs], axis=1)
            q = torch.einsum('btd,dhe->bhte', h, Wq)
            k = torch.einsum('btd,dhe->bhte', h, Wk)
            v = torch.einsum('btd,dhe->bhte', h, Wv)
            scores = torch.einsum('bhqe,bhke->bhqk', q, k) * scale
            scores = torch.where(mask[None, None], scores, -1000000000.0)
            p = _softmax(scores, axis=-1)
            o = torch.einsum('bhqk,bhke->bhqe', p, v)
            o = torch.einsum('bhte,hed->btd', o, Wo)
            mem = o[:, :M, :].clone()
            outs.append(o[:, M:, :])
        return torch.concatenate(outs, axis=1)

    def _B_ref(x, w1, b1, w2, b2):
        h = torch.einsum('bsd,df->bsf', x, w1) + b1
        zero = torch.zeros_like(h)
        r = torch.where(h > zero, h, zero)
        h = r * r
        out = torch.einsum('bsf,fd->bsd', h, w2) + b2
        return out
    x = A_x
    xn = _layernorm(x, axis=-1)
    return x + _A_ref(xn, A_mem0, A_Wq, A_Wk, A_Wv, A_Wo, A_seg_len) + _B_ref(xn, B_w1, B_b1, B_w2, B_b2)
```

```cpp
#include <hip/hip_runtime.h>
#include <hip/hip_bf16.h>

typedef unsigned u4 __attribute__((ext_vector_type(4)));
typedef unsigned u2 __attribute__((ext_vector_type(2)));
typedef float f4 __attribute__((ext_vector_type(4)));
typedef float f16v __attribute__((ext_vector_type(16)));
typedef short s4 __attribute__((ext_vector_type(4)));
typedef __bf16 bf8 __attribute__((ext_vector_type(8)));
typedef __bf16 bf2 __attribute__((ext_vector_type(2)));
typedef unsigned short us;
#define LAS __attribute__((address_space(3)))
#define DEVI __device__ __forceinline__

#define K_START 700
#define K_REC 950
#define K_PASS 900
#define K_VIS 5000
#define K_PERT 190
#define K_RINC 400

#define ASM_R \
    "s_nop 4\n" \
    "s_add_u32 s40, %[f], 0\n" \
    "s_sub_u32 s60, s40, 7\n" \
    "s_cmp_ge_u32 s40, 7\n" \
    "s_cselect_b32 s40, s60, s40\n" \
    "s_lshl_b32 s40, s40, 10\n" \
    "s_add_u32 s41, %[f], 1\n" \
    "s_sub_u32 s60, s41, 7\n" \
    "s_cmp_ge_u32 s41, 7\n" \
    "s_cselect_b32 s41, s60, s41\n" \
    "s_lshl_b32 s41, s41, 10\n" \
    "s_add_u32 s42, %[f], 2\n" \
    "s_sub_u32 s60, s42, 7\n" \
    "s_cmp_ge_u32 s42, 7\n" \
    "s_cselect_b32 s42, s60, s42\n" \
    "s_lshl_b32 s42, s42, 10\n" \
    "s_add_u32 s43, %[f], 3\n" \
    "s_sub_u32 s60, s43, 7\n" \
    "s_cmp_ge_u32 s43, 7\n" \
    "s_cselect_b32 s43, s60, s43\n" \
    "s_lshl_b32 s43, s43, 10\n" \
    "s_add_u32 s44, %[f], 4\n" \
    "s_sub_u32 s60, s44, 7\n" \
    "s_cmp_ge_u32 s44, 7\n" \
    "s_cselect_b32 s44, s60, s44\n" \
    "s_lshl_b32 s44, s44, 10\n" \
    "s_add_u32 s45, %[f], 5\n" \
    "s_sub_u32 s60, s45, 7\n" \
    "s_cmp_ge_u32 s45, 7\n" \
    "s_cselect_b32 s45, s60, s45\n" \
    "s_lshl_b32 s45, s45, 10\n" \
    "s_add_u32 s46, %[f], 6\n" \
    "s_sub_u32 s60, s46, 7\n" \
    "s_cmp_ge_u32 s46, 7\n" \
    "s_cselect_b32 s46, s60, s46\n" \
    "s_lshl_b32 s46, s46, 10\n" \
    "s_lshl_b32 s60, s40, 1\n" \
    "s_add_u32 s60, s60, %[woff]\n" \
    "buffer_load_dwordx4 a[0:3], %[l16], %[rsW], s60 offen\n" \
    "buffer_load_dwordx4 a[4:7], %[l16], %[rsW], s60 offen offset:1024\n" \
    "s_lshl_b32 s60, s41, 1\n" \
    "s_add_u32 s60, s60, %[woff]\n" \
    "buffer_load_dwordx4 a[8:11], %[l16], %[rsW], s60 offen\n" \
    "buffer_load_dwordx4 a[12:15], %[l16], %[rsW], s60 offen offset:1024\n" \
    "s_lshl_b32 s60, s42, 1\n" \
    "s_add_u32 s60, s60, %[woff]\n" \
    "buffer_load_dwordx4 a[16:19], %[l16], %[rsW], s60 offen\n" \
    "buffer_load_dwordx4 a[20:23], %[l16], %[rsW], s60 offen offset:1024\n" \
    "s_lshl_b32 s60, s43, 1\n" \
    "s_add_u32 s60, s60, %[woff]\n" \
    "buffer_load_dwordx4 a[24:27], %[l16], %[rsW], s60 offen\n" \
    "buffer_load_dwordx4 a[28:31], %[l16], %[rsW], s60 offen offset:1024\n" \
    "s_lshl_b32 s60, s44, 1\n" \
    "s_add_u32 s60, s60, %[woff]\n" \
    "buffer_load_dwordx4 a[32:35], %[l16], %[rsW], s60 offen\n" \
    "buffer_load_dwordx4 a[36:39], %[l16], %[rsW], s60 offen offset:1024\n" \
    "s_lshl_b32 s60, s45, 1\n" \
    "s_add_u32 s60, s60, %[woff]\n" \
    "buffer_load_dwordx4 a[40:43], %[l16], %[rsW], s60 offen\n" \
    "buffer_load_dwordx4 a[44:47], %[l16], %[rsW], s60 offen offset:1024\n" \
    "s_lshl_b32 s60, s46, 1\n" \
    "s_add_u32 s60, s60, %[woff]\n" \
    "buffer_load_dwordx4 a[48:51], %[l16], %[rsW], s60 offen\n" \
    "buffer_load_dwordx4 a[52:55], %[l16], %[rsW], s60 offen offset:1024\n" \
    "s_lshl_b32 s60, s40, 1\n" \
    "s_add_u32 s60, s60, %[woff]\n" \
    "s_add_u32 s60, s60, 14336\n" \
    "buffer_load_dwordx4 a[56:59], %[l16], %[rsW], s60 offen\n" \
    "buffer_load_dwordx4 a[60:63], %[l16], %[rsW], s60 offen offset:1024\n" \
    "s_lshl_b32 s60, s41, 1\n" \
    "s_add_u32 s60, s60, %[woff]\n" \
    "s_add_u32 s60, s60, 14336\n" \
    "buffer_load_dwordx4 a[64:67], %[l16], %[rsW], s60 offen\n" \
    "buffer_load_dwordx4 a[68:71], %[l16], %[rsW], s60 offen offset:1024\n" \
    "s_lshl_b32 s60, s42, 1\n" \
    "s_add_u32 s60, s60, %[woff]\n" \
    "s_add_u32 s60, s60, 14336\n" \
    "buffer_load_dwordx4 a[72:75], %[l16], %[rsW], s60 offen\n" \
    "buffer_load_dwordx4 a[76:79], %[l16], %[rsW], s60 offen offset:1024\n" \
    "s_lshl_b32 s60, s43, 1\n" \
    "s_add_u32 s60, s60, %[woff]\n" \
    "s_add_u32 s60, s60, 14336\n" \
    "buffer_load_dwordx4 a[80:83], %[l16], %[rsW], s60 offen\n" \
    "buffer_load_dwordx4 a[84:87], %[l16], %[rsW], s60 offen offset:1024\n" \
    "s_lshl_b32 s60, s44, 1\n" \
    "s_add_u32 s60, s60, %[woff]\n" \
    "s_add_u32 s60, s60, 14336\n" \
    "buffer_load_dwordx4 a[88:91], %[l16], %[rsW], s60 offen\n" \
    "buffer_load_dwordx4 a[92:95], %[l16], %[rsW], s60 offen offset:1024\n" \
    "s_lshl_b32 s60, s45, 1\n" \
    "s_add_u32 s60, s60, %[woff]\n" \
    "s_add_u32 s60, s60, 14336\n" \
    "buffer_load_dwordx4 a[96:99], %[l16], %[rsW], s60 offen\n" \
    "buffer_load_dwordx4 a[100:103], %[l16], %[rsW], s60 offen offset:1024\n" \
    "s_lshl_b32 s60, s46, 1\n" \
    "s_add_u32 s60, s60, %[woff]\n" \
    "s_add_u32 s60, s60, 14336\n" \
    "buffer_load_dwordx4 a[104:107], %[l16], %[rsW], s60 offen\n" \
    "buffer_load_dwordx4 a[108:111], %[l16], %[rsW], s60 offen offset:1024\n" \
    "s_lshl_b32 s60, s40, 1\n" \
    "s_add_u32 s60, s60, %[woff]\n" \
    "s_add_u32 s60, s60, 28672\n" \
    "buffer_load_dwordx4 a[112:115], %[l16], %[rsW], s60 offen\n" \
    "buffer_load_dwordx4 a[116:119], %[l16], %[rsW], s60 offen offset:1024\n" \
    "s_lshl_b32 s60, s41, 1\n" \
    "s_add_u32 s60, s60, %[woff]\n" \
    "s_add_u32 s60, s60, 28672\n" \
    "buffer_load_dwordx4 a[120:123], %[l16], %[rsW], s60 offen\n" \
    "buffer_load_dwordx4 a[124:127], %[l16], %[rsW], s60 offen offset:1024\n" \
    "s_lshl_b32 s60, s42, 1\n" \
    "s_add_u32 s60, s60, %[woff]\n" \
    "s_add_u32 s60, s60, 28672\n" \
    "buffer_load_dwordx4 a[128:131], %[l16], %[rsW], s60 offen\n" \
    "buffer_load_dwordx4 a[132:135], %[l16], %[rsW], s60 offen offset:1024\n" \
    "s_lshl_b32 s60, s43, 1\n" \
    "s_add_u32 s60, s60, %[woff]\n" \
    "s_add_u32 s60, s60, 28672\n" \
    "buffer_load_dwordx4 a[136:139], %[l16], %[rsW], s60 offen\n" \
    "buffer_load_dwordx4 a[140:143], %[l16], %[rsW], s60 offen offset:1024\n" \
    "s_lshl_b32 s60, s44, 1\n" \
    "s_add_u32 s60, s60, %[woff]\n" \
    "s_add_u32 s60, s60, 28672\n" \
    "buffer_load_dwordx4 a[144:147], %[l16], %[rsW], s60 offen\n" \
    "buffer_load_dwordx4 a[148:151], %[l16], %[rsW], s60 offen offset:1024\n" \
    "s_lshl_b32 s60, s45, 1\n" \
    "s_add_u32 s60, s60, %[woff]\n" \
    "s_add_u32 s60, s60, 28672\n" \
    "buffer_load_dwordx4 a[152:155], %[l16], %[rsW], s60 offen\n" \
    "buffer_load_dwordx4 a[156:159], %[l16], %[rsW], s60 offen offset:1024\n" \
    "s_lshl_b32 s60, s46, 1\n" \
    "s_add_u32 s60, s60, %[woff]\n" \
    "s_add_u32 s60, s60, 28672\n" \
    "buffer_load_dwordx4 a[160:163], %[l16], %[rsW], s60 offen\n" \
    "buffer_load_dwordx4 a[164:167], %[l16], %[rsW], s60 offen offset:1024\n" \
    "s_lshl_b32 s60, s40, 1\n" \
    "s_add_u32 s60, s60, %[woff]\n" \
    "s_add_u32 s60, s60, 43008\n" \
    "buffer_load_dwordx4 a[168:171], %[l16], %[rsW], s60 offen\n" \
    "buffer_load_dwordx4 a[172:175], %[l16], %[rsW], s60 offen offset:1024\n" \
    "s_lshl_b32 s60, s41, 1\n" \
    "s_add_u32 s60, s60, %[woff]\n" \
    "s_add_u32 s60, s60, 43008\n" \
    "buffer_load_dwordx4 a[176:179], %[l16], %[rsW], s60 offen\n" \
    "buffer_load_dwordx4 a[180:183], %[l16], %[rsW], s60 offen offset:1024\n" \
    "s_lshl_b32 s60, s42, 1\n" \
    "s_add_u32 s60, s60, %[woff]\n" \
    "s_add_u32 s60, s60, 43008\n" \
    "buffer_load_dwordx4 a[184:187], %[l16], %[rsW], s60 offen\n" \
    "buffer_load_dwordx4 a[188:191], %[l16], %[rsW], s60 offen offset:1024\n" \
    "s_lshl_b32 s60, s43, 1\n" \
    "s_add_u32 s60, s60, %[woff]\n" \
    "s_add_u32 s60, s60, 43008\n" \
    "buffer_load_dwordx4 a[192:195], %[l16], %[rsW], s60 offen\n" \
    "buffer_load_dwordx4 a[196:199], %[l16], %[rsW], s60 offen offset:1024\n" \
    "s_lshl_b32 s60, s44, 1\n" \
    "s_add_u32 s60, s60, %[woff]\n" \
    "s_add_u32 s60, s60, 43008\n" \
    "buffer_load_dwordx4 a[200:203], %[l16], %[rsW], s60 offen\n" \
    "buffer_load_dwordx4 a[204:207], %[l16], %[rsW], s60 offen offset:1024\n" \
    "s_lshl_b32 s60, s45, 1\n" \
    "s_add_u32 s60, s60, %[woff]\n" \
    "s_add_u32 s60, s60, 43008\n" \
    "buffer_load_dwordx4 a[208:211], %[l16], %[rsW], s60 offen\n" \
    "buffer_load_dwordx4 a[212:215], %[l16], %[rsW], s60 offen offset:1024\n" \
    "s_lshl_b32 s60, s46, 1\n" \
    "s_add_u32 s60, s60, %[woff]\n" \
    "s_add_u32 s60, s60, 43008\n" \
    "buffer_load_dwordx4 a[216:219], %[l16], %[rsW], s60 offen\n" \
    "buffer_load_dwordx4 a[220:223], %[l16], %[rsW], s60 offen offset:1024\n" \
    "s_mov_b32 s48, 0\n" \
    "s_mov_b32 s49, 3\n" \
    "s_sub_u32 s50, %[nw], 1\n" \
    "s_mov_b32 s51, %[xmoff]\n" \
    "s_mov_b32 s52, %[xqoff]\n" \
    "s_mov_b32 s59, 0\n" \
    "s_mov_b32 s61, 20000\n" \
    "s_add_u32 s60, s52, 0\n" \
    "buffer_load_dwordx4 v[128:131], %[l16], %[rsW], s60 offen\n" \
    "s_add_u32 s60, s52, 7168\n" \
    "buffer_load_dwordx4 v[132:135], %[l16], %[rsW], s60 offen\n" \
    "s_add_u32 s60, s52, 14336\n" \
    "buffer_load_dwordx4 v[136:139], %[l16], %[rsW], s60 offen\n" \
    "s_add_u32 s60, s52, 21504\n" \
    "buffer_load_dwordx4 v[140:143], %[l16], %[rsW], s60 offen\n" \
    "v_mov_b64 v[124:125], 0\n" \
    "v_mov_b64 v[126:127], 0\n" \
    "v_mov_b32 v109, 1\n" \
    "v_mov_b32 v121, 0x7fff0000\n" \
    "s_mov_b32 s67, %[selm]\n" \
    "s_mov_b32 s37, 950\n" \
    "s_and_b32 s88, s67, 1\n" \
    "s_lshl_b32 s88, s88, 13\n" \
    "s_and_b32 s89, s67, 2\n" \
    "s_lshl_b32 s89, s89, 12\n" \
    "s_add_u32 s89, s89, 0x200000\n" \
    "s_add_u32 s90, s89, 0x100000\n" \
    "s_add_u32 s40, s40, s88\n" \
    "s_add_u32 s41, s41, s88\n" \
    "s_add_u32 s42, s42, s88\n" \
    "s_add_u32 s43, s43, s88\n" \
    "s_add_u32 s44, s44, s88\n" \
    "s_add_u32 s45, s45, s88\n" \
    "s_add_u32 s46, s46, s88\n" \
    "s_add_u32 s47, s88, 16384\n" \
    "s_waitcnt vmcnt(0)\n" \
    "s_barrier\n" \
    "s_memrealtime s[54:55]\n" \
    "s_waitcnt lgkmcnt(0)\n" \
    "s_lshl_b32 s54, s54, 3\n" \
    "s_or_b32 s54, s54, 1\n" \
    "v_mov_b32 v240, s54\n" \
    "buffer_store_dword v240, %[rdyw], %[rsW], 0 offen sc1\n" \
    "s_mov_b32 s66, 0\n" \
    "Rini_%=:\n" \
    "buffer_load_dword v240, %[rdy0], %[rsW], 0 offen sc1\n" \
    "buffer_load_dword v242, %[rdy0b], %[rsW], 0 offen sc1\n" \
    "s_add_u32 s66, s66, 1\n" \
    "s_waitcnt vmcnt(0)\n" \
    "v_min_u32 v241, v240, v242\n" \
    "v_cmp_eq_u32 vcc, 0, v241\n" \
    "s_cbranch_vccz Rini_ok_%=\n" \
    "s_sleep 3\n" \
    "s_cmp_lt_u32 s66, s61\n" \
    "s_cbranch_scc1 Rini_%=\n" \
    "s_mov_b32 s61, 0\n" \
    "Rini_ok_%=:\n" \
    "v_sub_u32 v241, v242, v240\n" \
    "v_max_i32 v241, 0, v241\n" \
    "v_add_u32 v240, v240, v241\n" \
    "s_nop 1\n" \
    "v_readfirstlane_b32 s60, v240\n" \
    "s_nop 3\n" \
    "v_subrev_u32 v240, s60, v240\n" \
    "s_nop 1\n" \
    "v_max_i32_dpp v240, v240, v240 row_ror:8 row_mask:0xf bank_mask:0xf\n" \
    "s_nop 1\n" \
    "v_max_i32_dpp v240, v240, v240 row_ror:4 row_mask:0xf bank_mask:0xf\n" \
    "s_nop 1\n" \
    "v_max_i32_dpp v240, v240, v240 row_ror:2 row_mask:0xf bank_mask:0xf\n" \
    "s_nop 1\n" \
    "v_max_i32_dpp v240, v240, v240 row_ror:1 row_mask:0xf bank_mask:0xf\n" \
    "v_mov_b32 v243, v240\n" \
    "s_nop 1\n" \
    "v_permlane16_swap_b32 v240, v243\n" \
    "s_nop 1\n" \
    "v_max_i32 v240, v240, v243\n" \
    "v_mov_b32 v243, v240\n" \
    "s_nop 1\n" \
    "v_permlane32_swap_b32 v240, v243\n" \
    "s_nop 1\n" \
    "v_max_i32 v240, v240, v243\n" \
    "s_nop 1\n" \
    "v_readfirstlane_b32 s57, v240\n" \
    "s_nop 3\n" \
    "s_add_u32 s60, s60, s57\n" \
    "s_add_u32 s53, s60, 700\n" \
    "Rstep_%=:\n" \
    "s_mov_b32 s58, 0\n" \
    "s_mov_b32 s89, 0\n" \
    "Rtry_%=:\n" \
    "s_sub_u32 s68, s53, %[thr]\n" \
    "s_mov_b32 s69, s53\n" \
    "Rwc_%=:\n" \
    "s_memrealtime s[54:55]\n" \
    "s_waitcnt lgkmcnt(0)\n" \
    "s_lshl_b32 s54, s54, 3\n" \
    "s_sub_i32 s56, s69, s54\n" \
    "s_cmp_gt_i32 s56, 170\n" \
    "s_cbranch_scc1 Rwc_%=\n" \
    "s_cmp_lt_i32 s56, 125\n" \
    "s_cbranch_scc1 Rwo_%=\n" \
    "s_memrealtime s[70:71]\n" \
    "s_nop 1\n" \
    "s_memrealtime s[72:73]\n" \
    "s_nop 1\n" \
    "s_memrealtime s[74:75]\n" \
    "s_nop 1\n" \
    "s_memrealtime s[76:77]\n" \
    "s_nop 1\n" \
    "s_memrealtime s[78:79]\n" \
    "s_nop 1\n" \
    "s_memrealtime s[80:81]\n" \
    "s_nop 1\n" \
    "s_memrealtime s[82:83]\n" \
    "s_nop 1\n" \
    "s_memrealtime s[84:85]\n" \
    "s_nop 1\n" \
    "s_memrealtime s[86:87]\n" \
    "s_mov_b32 s56, 0\n" \
    "s_waitcnt lgkmcnt(0)\n" \
    "s_cmp_eq_u32 s70, s72\n" \
    "s_addc_u32 s56, s56, 0\n" \
    "s_cmp_eq_u32 s70, s74\n" \
    "s_addc_u32 s56, s56, 0\n" \
    "s_cmp_eq_u32 s70, s76\n" \
    "s_addc_u32 s56, s56, 0\n" \
    "s_cmp_eq_u32 s70, s78\n" \
    "s_addc_u32 s56, s56, 0\n" \
    "s_cmp_eq_u32 s70, s80\n" \
    "s_addc_u32 s56, s56, 0\n" \
    "s_cmp_eq_u32 s70, s82\n" \
    "s_addc_u32 s56, s56, 0\n" \
    "s_cmp_eq_u32 s70, s84\n" \
    "s_addc_u32 s56, s56, 0\n" \
    "s_cmp_eq_u32 s70, s86\n" \
    "s_addc_u32 s56, s56, 0\n" \
    "s_lshl_b32 s70, s70, 3\n" \
    "s_sub_i32 s70, s69, s70\n" \
    "s_mul_i32 s70, s70, 94\n" \
    "s_mul_i32 s56, s56, 384\n" \
    "s_add_i32 s70, s70, s56\n" \
    "s_sub_i32 s70, s70, 10688\n" \
    "s_ashr_i32 s70, s70, 8\n" \
    "s_max_i32 s70, s70, 0\n" \
    "s_min_i32 s70, s70, 40\n" \
    "s_lshl_b32 s70, s70, 2\n" \
    "s_getpc_b64 s[54:55]\n" \
    "Rwb_%=:\n" \
    "s_add_u32 s54, s54, Rwd_%=-Rwb_%=\n" \
    "s_addc_u32 s55, s55, 0\n" \
    "s_sub_u32 s54, s54, s70\n" \
    "s_subb_u32 s55, s55, 0\n" \
    "s_setpc_b64 s[54:55]\n" \
    "Rwo_%=:\n" \
    "s_sub_i32 s56, s56, 40\n" \
    "s_max_i32 s56, s56, 0\n" \
    "Rw_%=:\n" \
    "s_sub_u32 s56, s56, 9\n" \
    "s_cbranch_scc0 Rw_%=\n" \
    "s_branch Rwd_%=\n" \
    "s_nop 1\n" \
    "s_nop 1\n" \
    "s_nop 1\n" \
    "s_nop 1\n" \
    "s_nop 1\n" \
    "s_nop 1\n" \
    "s_nop 1\n" \
    "s_nop 1\n" \
    "s_nop 1\n" \
    "s_nop 1\n" \
    "s_nop 1\n" \
    "s_nop 1\n" \
    "s_nop 1\n" \
    "s_nop 1\n" \
    "s_nop 1\n" \
    "s_nop 1\n" \
    "s_nop 1\n" \
    "s_nop 1\n" \
    "s_nop 1\n" \
    "s_nop 1\n" \
    "s_nop 1\n" \
    "s_nop 1\n" \
    "s_nop 1\n" \
    "s_nop 1\n" \
    "s_nop 1\n" \
    "s_nop 1\n" \
    "s_nop 1\n" \
    "s_nop 1\n" \
    "s_nop 1\n" \
    "s_nop 1\n" \
    "s_nop 1\n" \
    "s_nop 1\n" \
    "s_nop 1\n" \
    "s_nop 1\n" \
    "s_nop 1\n" \
    "s_nop 1\n" \
    "s_nop 1\n" \
    "s_nop 1\n" \
    "s_nop 1\n" \
    "s_nop 1\n" \
    "Rwd_%=:\n" \
    "Ragain_%=:\n" \
    "s_memtime s[96:97]\n" \
    "s_memrealtime s[94:95]\n" \
    "buffer_load_dwordx4 v[32:35], %[l16], %[rsH], s40 offen sc1\n" \
    "v_mov_b64 v[60:61], 0\n" \
    "v_mov_b64 v[62:63], 0\n" \
    "v_mov_b64 v[64:65], 0\n" \
    "v_mov_b64 v[66:67], 0\n" \
    "v_mov_b64 v[68:69], 0\n" \
    "v_mov_b64 v[70:71], 0\n" \
    "v_mov_b64 v[72:73], 0\n" \
    "v_mov_b64 v[74:75], 0\n" \
    "s_nop 3\n" \
    "buffer_load_dwordx4 v[36:39], %[l16], %[rsH], s41 offen sc1\n" \
    "v_mov_b32 v123, 0\n" \
    "s_nop 10\n" \
    "buffer_load_dwordx4 v[40:43], %[l16], %[rsH], s42 offen sc1\n" \
    "s_nop 11\n" \
    "buffer_load_dwordx4 v[44:47], %[l16], %[rsH], s43 offen sc1\n" \
    "s_nop 11\n" \
    "buffer_load_dwordx4 v[48:51], %[l16], %[rsH], s44 offen sc1\n" \
    "s_nop 11\n" \
    "buffer_load_dwordx4 v[52:55], %[l16], %[rsH], s45 offen sc1\n" \
    "s_nop 11\n" \
    "buffer_load_dwordx4 v[56:59], %[l16], %[rsH], s46 offen sc1\n" \
    "s_waitcnt vmcnt(6)\n" \
    "v_smfmac_f32_16x16x64_bf16 v[60:63], v[32:35], a[0:7], %[idx]\n" \
    "v_max3_u32 v123, v123, v32, v33\n" \
    "v_smfmac_f32_16x16x64_bf16 v[64:67], v[32:35], a[56:63], %[idx]\n" \
    "v_max3_u32 v123, v123, v34, v35\n" \
    "v_smfmac_f32_16x16x64_bf16 v[68:71], v[32:35], a[112:119], %[idx]\n" \
    "v_smfmac_f32_16x16x64_bf16 v[72:75], v[32:35], a[168:175], %[idx]\n" \
    "s_waitcnt vmcnt(5)\n" \
    "v_smfmac_f32_16x16x64_bf16 v[60:63], v[36:39], a[8:15], %[idx]\n" \
    "v_max3_u32 v123, v123, v36, v37\n" \
    "v_smfmac_f32_16x16x64_bf16 v[64:67], v[36:39], a[64:71], %[idx]\n" \
    "v_max3_u32 v123, v123, v38, v39\n" \
    "v_smfmac_f32_16x16x64_bf16 v[68:71], v[36:39], a[120:127], %[idx]\n" \
    "v_smfmac_f32_16x16x64_bf16 v[72:75], v[36:39], a[176:183], %[idx]\n" \
    "s_waitcnt vmcnt(4)\n" \
    "v_smfmac_f32_16x16x64_bf16 v[60:63], v[40:43], a[16:23], %[idx]\n" \
    "v_max3_u32 v123, v123, v40, v41\n" \
    "v_smfmac_f32_16x16x64_bf16 v[64:67], v[40:43], a[72:79], %[idx]\n" \
    "v_max3_u32 v123, v123, v42, v43\n" \
    "v_smfmac_f32_16x16x64_bf16 v[68:71], v[40:43], a[128:135], %[idx]\n" \
    "v_smfmac_f32_16x16x64_bf16 v[72:75], v[40:43], a[184:191], %[idx]\n" \
    "s_waitcnt vmcnt(3)\n" \
    "v_smfmac_f32_16x16x64_bf16 v[60:63], v[44:47], a[24:31], %[idx]\n" \
    "v_max3_u32 v123, v123, v44, v45\n" \
    "v_smfmac_f32_16x16x64_bf16 v[64:67], v[44:47], a[80:87], %[idx]\n" \
    "v_max3_u32 v123, v123, v46, v47\n" \
    "v_smfmac_f32_16x16x64_bf16 v[68:71], v[44:47], a[136:143], %[idx]\n" \
    "v_smfmac_f32_16x16x64_bf16 v[72:75], v[44:47], a[192:199], %[idx]\n" \
    "s_waitcnt vmcnt(2)\n" \
    "v_smfmac_f32_16x16x64_bf16 v[60:63], v[48:51], a[32:39], %[idx]\n" \
    "v_max3_u32 v123, v123, v48, v49\n" \
    "v_smfmac_f32_16x16x64_bf16 v[64:67], v[48:51], a[88:95], %[idx]\n" \
    "v_max3_u32 v123, v123, v50, v51\n" \
    "v_smfmac_f32_16x16x64_bf16 v[68:71], v[48:51], a[144:151], %[idx]\n" \
    "v_smfmac_f32_16x16x64_bf16 v[72:75], v[48:51], a[200:207], %[idx]\n" \
    "s_waitcnt vmcnt(1)\n" \
    "v_smfmac_f32_16x16x64_bf16 v[60:63], v[52:55], a[40:47], %[idx]\n" \
    "v_max3_u32 v123, v123, v52, v53\n" \
    "v_smfmac_f32_16x16x64_bf16 v[64:67], v[52:55], a[96:103], %[idx]\n" \
    "v_max3_u32 v123, v123, v54, v55\n" \
    "v_smfmac_f32_16x16x64_bf16 v[68:71], v[52:55], a[152:159], %[idx]\n" \
    "v_smfmac_f32_16x16x64_bf16 v[72:75], v[52:55], a[208:215], %[idx]\n" \
    "s_waitcnt vmcnt(0)\n" \
    "v_smfmac_f32_16x16x64_bf16 v[60:63], v[56:59], a[48:55], %[idx]\n" \
    "v_max3_u32 v123, v123, v56, v57\n" \
    "v_smfmac_f32_16x16x64_bf16 v[68:71], v[56:59], a[160:167], %[idx]\n" \
    "v_max3_u32 v123, v123, v58, v59\n" \
    "v_smfmac_f32_16x16x64_bf16 v[64:67], v[56:59], a[104:111], %[idx]\n" \
    "v_cmp_eq_u32 vcc, -1, v123\n" \
    "v_smfmac_f32_16x16x64_bf16 v[72:75], v[56:59], a[216:223], %[idx]\n" \
    "s_cbranch_vccnz Rfail_%=\n" \
    "Rfail_go_%=:\n" \
    "v_cvt_pk_bf16_f32 v76, v60, v61\n" \
    "v_cvt_pk_bf16_f32 v77, v62, v63\n" \
    "v_cvt_pk_bf16_f32 v78, v68, v69\n" \
    "v_cvt_pk_bf16_f32 v79, v70, v71\n" \
    "v_cvt_pk_bf16_f32 v80, v64, v65\n" \
    "v_cvt_pk_bf16_f32 v81, v66, v67\n" \
    "v_mfma_f32_16x16x32_bf16 v[84:87], v[76:79], %[sel], 0\n" \
    "v_cvt_pk_bf16_f32 v82, v72, v73\n" \
    "v_cvt_pk_bf16_f32 v83, v74, v75\n" \
    "s_nop 1\n" \
    "v_mfma_f32_16x16x32_bf16 v[88:91], v[80:83], %[sel], 0\n" \
    "s_nop 2\n" \
    "v_cvt_pk_bf16_f32 v92, v84, v85\n" \
    "v_cvt_pk_bf16_f32 v93, v86, v87\n" \
    "s_nop 2\n" \
    "v_cvt_pk_bf16_f32 v94, v88, v89\n" \
    "v_cvt_pk_bf16_f32 v95, v90, v91\n" \
    "s_nop 1\n" \
    "v_mfma_f32_16x16x32_bf16 v[96:99], v[32:35], v[92:95], 0\n" \
    "s_nop 0\n" \
    "s_nop 6\n" \
    "v_permlane32_swap_b32 v96, v98\n" \
    "v_permlane32_swap_b32 v97, v99\n" \
    "s_nop 0\n" \
    "v_add_f32_dpp v100, v98, v96 row_ror:8 row_mask:0xf bank_mask:0x3\n" \
    "v_add_f32_dpp v100, v97, v99 row_ror:8 row_mask:0xf bank_mask:0xc\n" \
    "ds_write_b32 %[aS], v100\n" \
    "ds_add_rtn_u32 v108, %[cnt], %[one16]\n" \
    "ds_read_b32 v110, %[rdS] offset:0\n" \
    "ds_read_b32 v111, %[rdS] offset:256\n" \
    "ds_read_b32 v112, %[rdS] offset:512\n" \
    "ds_read_b32 v113, %[rdS] offset:768\n" \
    "s_waitcnt lgkmcnt(0)\n" \
    "v_add_f32 v110, v110, v111\n" \
    "v_add_f32 v112, v112, v113\n" \
    "v_cmp_eq_u32 vcc, s49, v108\n" \
    "v_add_f32 v116, v110, v112\n" \
    "v_add_f32 v117, v110, v112\n" \
    "s_cbranch_vccz Rnp_%=\n" \
    "v_cndmask_b32 v122, v121, %[pubS], vcc\n" \
    "v_permlane16_swap_b32 v116, v117\n" \
    "v_mov_b32 v118, v116\n" \
    "v_mov_b32 v119, v117\n" \
    "s_nop 0\n" \
    "v_permlane32_swap_b32 v116, v118\n" \
    "v_permlane32_swap_b32 v117, v119\n" \
    "buffer_store_dwordx4 v[116:119], v122, %[rsH], s47 offen sc1\n" \
    "s_memtime s[98:99]\n" \
    "buffer_load_dwordx2 v[240:241], %[stpo], %[rsH], s90 offen sc1\n" \
    "v_cndmask_b32 v122, v121, %[rdys], vcc\n" \
    "s_waitcnt lgkmcnt(0)\n" \
    "s_sub_u32 s98, s98, s96\n" \
    "s_mul_i32 s98, s98, 87\n" \
    "s_lshr_b32 s98, s98, 8\n" \
    "s_lshl_b32 s57, s94, 3\n" \
    "s_sub_i32 s57, s57, s53\n" \
    "s_max_i32 s57, s57, 0\n" \
    "s_add_u32 s57, s57, s53\n" \
    "s_add_u32 s57, s57, s98\n" \
    "s_and_b32 s57, s57, -2\n" \
    "v_mov_b32 v242, s57\n" \
    "s_cmp_lt_u32 s48, 63\n" \
    "s_cselect_b32 s60, 16384, 0\n" \
    "s_add_u32 s60, s60, s90\n" \
    "buffer_store_dword v242, v122, %[rsH], s60 offen sc1\n" \
    "s_branch Rnpj_%=\n" \
    "Rnp_%=:\n" \
    "buffer_load_dwordx2 v[240:241], %[stpo], %[rsH], s90 offen sc1\n" \
    "Rnpj_%=:\n" \
    "v_mfma_f32_16x16x32_bf16 v[144:147], v[128:131], v[92:95], 0\n" \
    "v_mfma_f32_16x16x32_bf16 v[148:151], v[132:135], v[92:95], 0\n" \
    "v_mfma_f32_16x16x32_bf16 v[152:155], v[136:139], v[92:95], 0\n" \
    "v_mfma_f32_16x16x32_bf16 v[156:159], v[140:143], v[92:95], 0\n" \
    "s_cmp_lt_u32 s48, 63\n" \
    "s_cselect_b32 s60, 28672, 0\n" \
    "s_add_u32 s52, s52, s60\n" \
    "s_nop 4\n" \
    "v_permlane32_swap_b32 v144, v146\n" \
    "v_permlane32_swap_b32 v145, v147\n" \
    "s_nop 0\n" \
    "v_add_f32_dpp v160, v146, v144 row_ror:8 row_mask:0xf bank_mask:0x3\n" \
    "v_add_f32_dpp v160, v145, v147 row_ror:8 row_mask:0xf bank_mask:0xc\n" \
    "v_permlane32_swap_b32 v148, v150\n" \
    "v_permlane32_swap_b32 v149, v151\n" \
    "s_nop 0\n" \
    "v_add_f32_dpp v161, v150, v148 row_ror:8 row_mask:0xf bank_mask:0x3\n" \
    "v_add_f32_dpp v161, v149, v151 row_ror:8 row_mask:0xf bank_mask:0xc\n" \
    "v_permlane32_swap_b32 v152, v154\n" \
    "v_permlane32_swap_b32 v153, v155\n" \
    "s_nop 0\n" \
    "v_add_f32_dpp v162, v154, v152 row_ror:8 row_mask:0xf bank_mask:0x3\n" \
    "v_add_f32_dpp v162, v153, v155 row_ror:8 row_mask:0xf bank_mask:0xc\n" \
    "v_permlane32_swap_b32 v156, v158\n" \
    "v_permlane32_swap_b32 v157, v159\n" \
    "s_nop 0\n" \
    "v_add_f32_dpp v163, v158, v156 row_ror:8 row_mask:0xf bank_mask:0x3\n" \
    "v_add_f32_dpp v163, v157, v159 row_ror:8 row_mask:0xf bank_mask:0xc\n" \
    "ds_write_b128 %[aX], v[160:163]\n" \
    "ds_add_rtn_u32 v108, %[cnt], v109 offset:256\n" \
    "ds_read_b128 v[164:167], %[l16] offset:2048\n" \
    "ds_read_b128 v[168:171], %[l16] offset:3072\n" \
    "ds_read_b128 v[172:175], %[l16] offset:4096\n" \
    "ds_read_b128 v[176:179], %[l16] offset:5120\n" \
    "s_waitcnt vmcnt(0)\n" \
    "v_subrev_u32 v237, s68, v240\n" \
    "v_cmp_eq_u32 s[38:39], -1, v240\n" \
    "v_cmp_lt_i32 vcc, 0, v237\n" \
    "s_or_b64 vcc, vcc, s[38:39]\n" \
    "s_cbranch_vccz Rsc_n_%=\n" \
    "s_mov_b32 s66, 0\n" \
    "Rsc_p_%=:\n" \
    "buffer_load_dwordx2 v[240:241], %[stpo], %[rsH], s90 offen sc1\n" \
    "s_add_u32 s66, s66, 1\n" \
    "s_waitcnt vmcnt(0)\n" \
    "v_cmp_eq_u32 vcc, -1, v240\n" \
    "s_cbranch_vccz Rsc_ok_%=\n" \
    "s_sleep 3\n" \
    "s_cmp_lt_u32 s66, s61\n" \
    "s_cbranch_scc1 Rsc_p_%=\n" \
    "s_mov_b32 s61, 0\n" \
    "Rsc_ok_%=:\n" \
    "v_readfirstlane_b32 s60, v240\n" \
    "s_nop 3\n" \
    "v_subrev_u32 v240, s60, v240\n" \
    "s_nop 1\n" \
    "v_max_i32_dpp v240, v240, v240 row_ror:8 row_mask:0xf bank_mask:0xf\n" \
    "s_nop 1\n" \
    "v_max_i32_dpp v240, v240, v240 row_ror:4 row_mask:0xf bank_mask:0xf\n" \
    "s_nop 1\n" \
    "v_max_i32_dpp v240, v240, v240 row_ror:2 row_mask:0xf bank_mask:0xf\n" \
    "s_nop 1\n" \
    "v_max_i32_dpp v240, v240, v240 row_ror:1 row_mask:0xf bank_mask:0xf\n" \
    "v_mov_b32 v243, v240\n" \
    "s_nop 1\n" \
    "v_permlane16_swap_b32 v240, v243\n" \
    "s_nop 1\n" \
    "v_max_i32 v240, v240, v243\n" \
    "v_mov_b32 v243, v240\n" \
    "s_nop 1\n" \
    "v_permlane32_swap_b32 v240, v243\n" \
    "s_nop 1\n" \
    "v_max_i32 v240, v240, v243\n" \
    "s_nop 1\n" \
    "v_readfirstlane_b32 s57, v240\n" \
    "s_nop 3\n" \
    "s_add_u32 s60, s60, s57\n" \
    "s_sub_u32 s57, s53, %[thr]\n" \
    "s_sub_i32 s57, s60, s57\n" \
    "s_cmp_gt_i32 s57, 0\n" \
    "s_cbranch_scc0 Rsc_k_%=\n" \
    "s_sub_i32 s57, s60, s53\n" \
    "s_add_u32 s60, s57, s37\n" \
    "s_cmp_le_i32 s57, -3200\n" \
    "s_cselect_b32 s60, 1825, s60\n" \
    "s_cmp_le_i32 s57, -4100\n" \
    "s_cselect_b32 s60, 920, s60\n" \
    "s_add_u32 s53, s53, s60\n" \
    "s_mov_b32 s89, 190\n" \
    "Rsc_k_%=:\n" \
    "Rsc_n_%=:\n" \
    "v_readlane_b32 s57, v241, 28\n" \
    "s_nop 3\n" \
    "s_and_b32 s57, s57, 0xffff\n" \
    "s_add_u32 s53, s53, s57\n" \
    "s_add_u32 s53, s53, s89\n" \
    "s_add_u32 s37, s37, 400\n" \
    "s_min_u32 s37, s37, 2550\n" \
    "s_cmp_eq_u32 s89, 0\n" \
    "s_cselect_b32 s37, 950, s37\n" \
    "s_add_u32 s60, s52, 0\n" \
    "buffer_load_dwordx4 v[128:131], %[l16], %[rsW], s60 offen\n" \
    "s_add_u32 s60, s52, 7168\n" \
    "buffer_load_dwordx4 v[132:135], %[l16], %[rsW], s60 offen\n" \
    "s_add_u32 s60, s52, 14336\n" \
    "buffer_load_dwordx4 v[136:139], %[l16], %[rsW], s60 offen\n" \
    "s_add_u32 s60, s52, 21504\n" \
    "buffer_load_dwordx4 v[140:143], %[l16], %[rsW], s60 offen\n" \
    "s_waitcnt lgkmcnt(0)\n" \
    "v_cmp_eq_u32 vcc, s50, v108\n" \
    "v_add_f32 v164, v164, v168\n" \
    "v_add_f32 v165, v165, v169\n" \
    "v_add_f32 v166, v166, v170\n" \
    "v_add_f32 v167, v167, v171\n" \
    "v_add_f32 v172, v172, v176\n" \
    "v_add_f32 v173, v173, v177\n" \
    "v_add_f32 v174, v174, v178\n" \
    "v_add_f32 v175, v175, v179\n" \
    "v_add_f32 v164, v164, v172\n" \
    "v_add_f32 v165, v165, v173\n" \
    "v_add_f32 v166, v166, v174\n" \
    "v_add_f32 v167, v167, v175\n" \
    "s_cbranch_vccz Rnx_%=\n" \
    "s_mov_b64 exec, vcc\n" \
    "buffer_store_dwordx4 v[164:167], %[l16], %[rsW], s51 offen sc1\n" \
    "s_mov_b64 exec, -1\n" \
    "Rnx_%=:\n" \
    "s_add_u32 s40, s40, 32768\n" \
    "s_add_u32 s41, s41, 32768\n" \
    "s_add_u32 s42, s42, 32768\n" \
    "s_add_u32 s43, s43, 32768\n" \
    "s_add_u32 s44, s44, 32768\n" \
    "s_add_u32 s45, s45, 32768\n" \
    "s_add_u32 s46, s46, 32768\n" \
    "s_add_u32 s47, s47, 32768\n" \
    "s_add_u32 s90, s90, 16384\n" \
    "s_add_u32 s49, s49, 4\n" \
    "s_add_u32 s50, s50, %[nw]\n" \
    "s_add_u32 s51, s51, 32768\n" \
    "s_add_u32 s48, s48, 1\n" \
    "s_cmp_lt_u32 s48, 64\n" \
    "s_cbranch_scc1 Rstep_%=\n" \
    "s_branch Rend_%=\n" \
    "Rfail_%=:\n" \
    "s_add_u32 s58, s58, 1\n" \
    "s_add_u32 s59, s59, 1\n" \
    "s_cmp_ge_u32 s58, s61\n" \
    "s_cbranch_scc1 Rfail_up_%=\n" \
    "s_cmp_le_u32 s58, 0\n" \
    "s_cbranch_scc1 Ragain_%=\n" \
    "s_mov_b32 s66, 0\n" \
    "Rfail_p_%=:\n" \
    "buffer_load_dwordx2 v[240:241], %[stpo], %[rsH], s90 offen sc1\n" \
    "s_add_u32 s66, s66, 1\n" \
    "s_waitcnt vmcnt(0)\n" \
    "v_cmp_eq_u32 vcc, -1, v240\n" \
    "s_cbranch_vccz Rfail_ok_%=\n" \
    "s_sleep 3\n" \
    "s_cmp_lt_u32 s66, s61\n" \
    "s_cbranch_scc1 Rfail_p_%=\n" \
    "s_mov_b32 s61, 0\n" \
    "Rfail_ok_%=:\n" \
    "v_readfirstlane_b32 s60, v240\n" \
    "s_nop 3\n" \
    "v_subrev_u32 v240, s60, v240\n" \
    "s_nop 1\n" \
    "v_max_i32_dpp v240, v240, v240 row_ror:8 row_mask:0xf bank_mask:0xf\n" \
    "s_nop 1\n" \
    "v_max_i32_dpp v240, v240, v240 row_ror:4 row_mask:0xf bank_mask:0xf\n" \
    "s_nop 1\n" \
    "v_max_i32_dpp v240, v240, v240 row_ror:2 row_mask:0xf bank_mask:0xf\n" \
    "s_nop 1\n" \
    "v_max_i32_dpp v240, v240, v240 row_ror:1 row_mask:0xf bank_mask:0xf\n" \
    "v_mov_b32 v243, v240\n" \
    "s_nop 1\n" \
    "v_permlane16_swap_b32 v240, v243\n" \
    "s_nop 1\n" \
    "v_max_i32 v240, v240, v243\n" \
    "v_mov_b32 v243, v240\n" \
    "s_nop 1\n" \
    "v_permlane32_swap_b32 v240, v243\n" \
    "s_nop 1\n" \
    "v_max_i32 v240, v240, v243\n" \
    "s_nop 1\n" \
    "v_readfirstlane_b32 s57, v240\n" \
    "s_nop 3\n" \
    "s_add_u32 s60, s60, s57\n" \
    "s_sub_u32 s57, s53, %[thr]\n" \
    "s_sub_i32 s57, s60, s57\n" \
    "s_cmp_gt_i32 s57, 0\n" \
    "s_cbranch_scc0 Rfail_k_%=\n" \
    "s_sub_i32 s57, s60, s53\n" \
    "s_add_u32 s60, s57, s37\n" \
    "s_cmp_le_i32 s57, -3200\n" \
    "s_cselect_b32 s60, 1825, s60\n" \
    "s_cmp_le_i32 s57, -4100\n" \
    "s_cselect_b32 s60, 920, s60\n" \
    "s_add_u32 s53, s53, s60\n" \
    "s_mov_b32 s89, 190\n" \
    "Rfail_k_%=:\n" \
    "s_branch Rtry_%=\n" \
    "Rfail_up_%=:\n" \
    "s_mov_b32 s61, 0\n" \
    "s_branch Rfail_go_%=\n" \
    "Rend_%=:\n" \
    "s_waitcnt vmcnt(0) lgkmcnt(0)\n"

#define ASM_X \
    "s_nop 4\n" \
    "s_mov_b32 s48, 0\n" \
    "s_mov_b32 s49, 3\n" \
    "s_add_u32 s60, %[kpoff], 0\n" \
    "s_lshr_b32 s42, s60, 13\n" \
    "s_lshl_b32 s42, s42, 14\n" \
    "s_and_b32 s60, s60, 8191\n" \
    "s_add_u32 s42, s42, s60\n" \
    "s_add_u32 s60, %[kpoff], 4096\n" \
    "s_lshr_b32 s43, s60, 13\n" \
    "s_lshl_b32 s43, s43, 14\n" \
    "s_and_b32 s60, s60, 8191\n" \
    "s_add_u32 s43, s43, s60\n" \
    "s_add_u32 s60, %[kpoff], 8192\n" \
    "s_lshr_b32 s44, s60, 13\n" \
    "s_lshl_b32 s44, s44, 14\n" \
    "s_and_b32 s60, s60, 8191\n" \
    "s_add_u32 s44, s44, s60\n" \
    "s_add_u32 s60, %[kpoff], 12288\n" \
    "s_lshr_b32 s45, s60, 13\n" \
    "s_lshl_b32 s45, s45, 14\n" \
    "s_and_b32 s60, s60, 8191\n" \
    "s_add_u32 s45, s45, s60\n" \
    "s_add_u32 s60, %[kpoff], 16384\n" \
    "s_lshr_b32 s50, s60, 13\n" \
    "s_lshl_b32 s50, s50, 14\n" \
    "s_and_b32 s60, s60, 8191\n" \
    "s_add_u32 s50, s50, s60\n" \
    "s_add_u32 s60, %[kpoff], 20480\n" \
    "s_lshr_b32 s51, s60, 13\n" \
    "s_lshl_b32 s51, s51, 14\n" \
    "s_and_b32 s60, s60, 8191\n" \
    "s_add_u32 s51, s51, s60\n" \
    "s_add_u32 s60, %[kpoff], 24576\n" \
    "s_lshr_b32 s91, s60, 13\n" \
    "s_lshl_b32 s91, s91, 14\n" \
    "s_and_b32 s60, s60, 8191\n" \
    "s_add_u32 s91, s91, s60\n" \
    "s_mov_b32 s59, 0\n" \
    "s_mov_b32 s61, 20000\n" \
    "s_mov_b32 s67, %[selm]\n" \
    "s_mov_b32 s37, 950\n" \
    "s_and_b32 s88, s67, 1\n" \
    "s_lshl_b32 s88, s88, 13\n" \
    "s_and_b32 s89, s67, 2\n" \
    "s_lshl_b32 s89, s89, 12\n" \
    "s_add_u32 s89, s89, 0x200000\n" \
    "s_add_u32 s90, s89, 0x100000\n" \
    "s_mov_b32 s40, s88\n" \
    "s_add_u32 s41, s88, 4096\n" \
    "s_add_u32 s47, s88, 16384\n" \
    "s_mov_b32 s46, s89\n" \
    "s_mov_b32 s64, 0\n" \
    "s_mov_b32 s65, 0\n" \
    "v_mov_b32 v121, 0x7fff0000\n" \
    "s_cmp_gt_u32 s64, s65\n" \
    "s_cbranch_scc1 Xf0_ok_%=\n" \
    "s_mov_b32 s66, 0\n" \
    "Xf0_poll_%=:\n" \
    "buffer_load_dword v240, %[flg], %[rsW], 0 offen sc1\n" \
    "s_waitcnt vmcnt(0)\n" \
    "s_nop 1\n" \
    "v_min_u32_dpp v240, v240, v240 row_ror:8 row_mask:0xf bank_mask:0xf\n" \
    "s_nop 1\n" \
    "v_min_u32_dpp v240, v240, v240 row_ror:4 row_mask:0xf bank_mask:0xf\n" \
    "s_nop 1\n" \
    "v_min_u32_dpp v240, v240, v240 row_ror:2 row_mask:0xf bank_mask:0xf\n" \
    "s_nop 1\n" \
    "v_min_u32_dpp v240, v240, v240 row_ror:1 row_mask:0xf bank_mask:0xf\n" \
    "s_nop 1\n" \
    "v_readfirstlane_b32 s64, v240\n" \
    "s_nop 3\n" \
    "s_cmp_gt_u32 s64, s65\n" \
    "s_cbranch_scc1 Xf0_ok_%=\n" \
    "s_sleep 4\n" \
    "s_add_u32 s66, s66, 1\n" \
    "s_cmp_lt_u32 s66, s61\n" \
    "s_cbranch_scc1 Xf0_poll_%=\n" \
    "s_mov_b32 s61, 0\n" \
    "Xf0_ok_%=:\n" \
    "s_lshr_b32 s52, s48, 1\n" \
    "s_lshr_b32 s60, s67, 16\n" \
    "s_xor_b32 s60, s60, s52\n" \
    "s_and_b32 s60, s60, 1\n" \
    "s_lshl_b32 s60, s60, 13\n" \
    "s_lshl_b32 s52, s48, 20\n" \
    "s_add_u32 s52, s52, s60\n" \
    "s_add_u32 s52, s52, %[kpb]\n" \
    "s_add_u32 s60, s52, s42\n" \
    "buffer_load_dwordx4 a[0:3], %[l16], %[rsW], s60 offen offset:0 sc1\n" \
    "buffer_load_dwordx4 a[4:7], %[l16], %[rsW], s60 offen offset:1024 sc1\n" \
    "buffer_load_dwordx4 a[8:11], %[l16], %[rsW], s60 offen offset:2048 sc1\n" \
    "buffer_load_dwordx4 a[12:15], %[l16], %[rsW], s60 offen offset:3072 sc1\n" \
    "s_add_u32 s60, s52, s43\n" \
    "buffer_load_dwordx4 a[16:19], %[l16], %[rsW], s60 offen offset:0 sc1\n" \
    "buffer_load_dwordx4 a[20:23], %[l16], %[rsW], s60 offen offset:1024 sc1\n" \
    "buffer_load_dwordx4 a[24:27], %[l16], %[rsW], s60 offen offset:2048 sc1\n" \
    "buffer_load_dwordx4 a[28:31], %[l16], %[rsW], s60 offen offset:3072 sc1\n" \
    "s_add_u32 s60, s52, s44\n" \
    "buffer_load_dwordx4 a[32:35], %[l16], %[rsW], s60 offen offset:0 sc1\n" \
    "buffer_load_dwordx4 a[36:39], %[l16], %[rsW], s60 offen offset:1024 sc1\n" \
    "buffer_load_dwordx4 a[40:43], %[l16], %[rsW], s60 offen offset:2048 sc1\n" \
    "buffer_load_dwordx4 a[44:47], %[l16], %[rsW], s60 offen offset:3072 sc1\n" \
    "s_add_u32 s60, s52, s45\n" \
    "buffer_load_dwordx4 a[48:51], %[l16], %[rsW], s60 offen offset:0 sc1\n" \
    "buffer_load_dwordx4 a[52:55], %[l16], %[rsW], s60 offen offset:1024 sc1\n" \
    "buffer_load_dwordx4 a[56:59], %[l16], %[rsW], s60 offen offset:2048 sc1\n" \
    "buffer_load_dwordx4 a[60:63], %[l16], %[rsW], s60 offen offset:3072 sc1\n" \
    "s_add_u32 s60, s52, s50\n" \
    "buffer_load_dwordx4 a[64:67], %[l16], %[rsW], s60 offen offset:0 sc1\n" \
    "buffer_load_dwordx4 a[68:71], %[l16], %[rsW], s60 offen offset:1024 sc1\n" \
    "buffer_load_dwordx4 a[72:75], %[l16], %[rsW], s60 offen offset:2048 sc1\n" \
    "buffer_load_dwordx4 a[76:79], %[l16], %[rsW], s60 offen offset:3072 sc1\n" \
    "s_add_u32 s60, s52, s51\n" \
    "buffer_load_dwordx4 a[80:83], %[l16], %[rsW], s60 offen offset:0 sc1\n" \
    "buffer_load_dwordx4 a[84:87], %[l16], %[rsW], s60 offen offset:1024 sc1\n" \
    "buffer_load_dwordx4 a[88:91], %[l16], %[rsW], s60 offen offset:2048 sc1\n" \
    "buffer_load_dwordx4 a[92:95], %[l16], %[rsW], s60 offen offset:3072 sc1\n" \
    "s_add_u32 s60, s52, s91\n" \
    "buffer_load_dwordx4 a[96:99], %[l16], %[rsW], s60 offen offset:0 sc1\n" \
    "buffer_load_dwordx4 a[100:103], %[l16], %[rsW], s60 offen offset:1024 sc1\n" \
    "buffer_load_dwordx4 a[104:107], %[l16], %[rsW], s60 offen offset:2048 sc1\n" \
    "buffer_load_dwordx4 a[108:111], %[l16], %[rsW], s60 offen offset:3072 sc1\n" \
    "s_add_u32 s60, s48, 3\n" \
    "s_cmp_gt_u32 s64, s60\n" \
    "s_cselect_b64 vcc, -1, 0\n" \
    "v_cndmask_b32 v238, %[flg], v121, vcc\n" \
    "buffer_load_dword v239, v238, %[rsW], 0 offen sc1\n" \
    "v_mov_b32 v100, 0\n" \
    "v_mov_b32 v109, 1\n" \
    "v_mov_b32 v121, 0x7fff0000\n" \
    "s_waitcnt vmcnt(0)\n" \
    "s_barrier\n" \
    "s_memrealtime s[54:55]\n" \
    "s_waitcnt lgkmcnt(0)\n" \
    "s_lshl_b32 s54, s54, 3\n" \
    "s_or_b32 s54, s54, 1\n" \
    "v_mov_b32 v240, s54\n" \
    "buffer_store_dword v240, %[rdyw], %[rsW], 0 offen sc1\n" \
    "s_mov_b32 s66, 0\n" \
    "Xini_%=:\n" \
    "buffer_load_dword v240, %[rdy0], %[rsW], 0 offen sc1\n" \
    "buffer_load_dword v242, %[rdy0b], %[rsW], 0 offen sc1\n" \
    "s_add_u32 s66, s66, 1\n" \
    "s_waitcnt vmcnt(0)\n" \
    "v_min_u32 v241, v240, v242\n" \
    "v_cmp_eq_u32 vcc, 0, v241\n" \
    "s_cbranch_vccz Xini_ok_%=\n" \
    "s_sleep 3\n" \
    "s_cmp_lt_u32 s66, s61\n" \
    "s_cbranch_scc1 Xini_%=\n" \
    "s_mov_b32 s61, 0\n" \
    "Xini_ok_%=:\n" \
    "v_sub_u32 v241, v242, v240\n" \
    "v_max_i32 v241, 0, v241\n" \
    "v_add_u32 v240, v240, v241\n" \
    "s_nop 1\n" \
    "v_readfirstlane_b32 s60, v240\n" \
    "s_nop 3\n" \
    "v_subrev_u32 v240, s60, v240\n" \
    "s_nop 1\n" \
    "v_max_i32_dpp v240, v240, v240 row_ror:8 row_mask:0xf bank_mask:0xf\n" \
    "s_nop 1\n" \
    "v_max_i32_dpp v240, v240, v240 row_ror:4 row_mask:0xf bank_mask:0xf\n" \
    "s_nop 1\n" \
    "v_max_i32_dpp v240, v240, v240 row_ror:2 row_mask:0xf bank_mask:0xf\n" \
    "s_nop 1\n" \
    "v_max_i32_dpp v240, v240, v240 row_ror:1 row_mask:0xf bank_mask:0xf\n" \
    "v_mov_b32 v243, v240\n" \
    "s_nop 1\n" \
    "v_permlane16_swap_b32 v240, v243\n" \
    "s_nop 1\n" \
    "v_max_i32 v240, v240, v243\n" \
    "v_mov_b32 v243, v240\n" \
    "s_nop 1\n" \
    "v_permlane32_swap_b32 v240, v243\n" \
    "s_nop 1\n" \
    "v_max_i32 v240, v240, v243\n" \
    "s_nop 1\n" \
    "v_readfirstlane_b32 s57, v240\n" \
    "s_nop 3\n" \
    "s_add_u32 s60, s60, s57\n" \
    "s_add_u32 s53, s60, 700\n" \
    "Xstep_%=:\n" \
    "s_mov_b32 s58, 0\n" \
    "s_mov_b32 s89, 0\n" \
    "Xtry_%=:\n" \
    "s_sub_u32 s68, s53, %[thr]\n" \
    "s_mov_b32 s69, s53\n" \
    "Xwc_%=:\n" \
    "s_memrealtime s[54:55]\n" \
    "s_waitcnt lgkmcnt(0)\n" \
    "s_lshl_b32 s54, s54, 3\n" \
    "s_sub_i32 s56, s69, s54\n" \
    "s_cmp_gt_i32 s56, 170\n" \
    "s_cbranch_scc1 Xwc_%=\n" \
    "s_cmp_lt_i32 s56, 125\n" \
    "s_cbranch_scc1 Xwo_%=\n" \
    "s_memrealtime s[70:71]\n" \
    "s_nop 1\n" \
    "s_memrealtime s[72:73]\n" \
    "s_nop 1\n" \
    "s_memrealtime s[74:75]\n" \
    "s_nop 1\n" \
    "s_memrealtime s[76:77]\n" \
    "s_nop 1\n" \
    "s_memrealtime s[78:79]\n" \
    "s_nop 1\n" \
    "s_memrealtime s[80:81]\n" \
    "s_nop 1\n" \
    "s_memrealtime s[82:83]\n" \
    "s_nop 1\n" \
    "s_memrealtime s[84:85]\n" \
    "s_nop 1\n" \
    "s_memrealtime s[86:87]\n" \
    "s_mov_b32 s56, 0\n" \
    "s_waitcnt lgkmcnt(0)\n" \
    "s_cmp_eq_u32 s70, s72\n" \
    "s_addc_u32 s56, s56, 0\n" \
    "s_cmp_eq_u32 s70, s74\n" \
    "s_addc_u32 s56, s56, 0\n" \
    "s_cmp_eq_u32 s70, s76\n" \
    "s_addc_u32 s56, s56, 0\n" \
    "s_cmp_eq_u32 s70, s78\n" \
    "s_addc_u32 s56, s56, 0\n" \
    "s_cmp_eq_u32 s70, s80\n" \
    "s_addc_u32 s56, s56, 0\n" \
    "s_cmp_eq_u32 s70, s82\n" \
    "s_addc_u32 s56, s56, 0\n" \
    "s_cmp_eq_u32 s70, s84\n" \
    "s_addc_u32 s56, s56, 0\n" \
    "s_cmp_eq_u32 s70, s86\n" \
    "s_addc_u32 s56, s56, 0\n" \
    "s_lshl_b32 s70, s70, 3\n" \
    "s_sub_i32 s70, s69, s70\n" \
    "s_mul_i32 s70, s70, 94\n" \
    "s_mul_i32 s56, s56, 384\n" \
    "s_add_i32 s70, s70, s56\n" \
    "s_sub_i32 s70, s70, 10688\n" \
    "s_ashr_i32 s70, s70, 8\n" \
    "s_max_i32 s70, s70, 0\n" \
    "s_min_i32 s70, s70, 40\n" \
    "s_lshl_b32 s70, s70, 2\n" \
    "s_getpc_b64 s[54:55]\n" \
    "Xwb_%=:\n" \
    "s_add_u32 s54, s54, Xwd_%=-Xwb_%=\n" \
    "s_addc_u32 s55, s55, 0\n" \
    "s_sub_u32 s54, s54, s70\n" \
    "s_subb_u32 s55, s55, 0\n" \
    "s_setpc_b64 s[54:55]\n" \
    "Xwo_%=:\n" \
    "s_sub_i32 s56, s56, 40\n" \
    "s_max_i32 s56, s56, 0\n" \
    "Xw_%=:\n" \
    "s_sub_u32 s56, s56, 9\n" \
    "s_cbranch_scc0 Xw_%=\n" \
    "s_branch Xwd_%=\n" \
    "s_nop 1\n" \
    "s_nop 1\n" \
    "s_nop 1\n" \
    "s_nop 1\n" \
    "s_nop 1\n" \
    "s_nop 1\n" \
    "s_nop 1\n" \
    "s_nop 1\n" \
    "s_nop 1\n" \
    "s_nop 1\n" \
    "s_nop 1\n" \
    "s_nop 1\n" \
    "s_nop 1\n" \
    "s_nop 1\n" \
    "s_nop 1\n" \
    "s_nop 1\n" \
    "s_nop 1\n" \
    "s_nop 1\n" \
    "s_nop 1\n" \
    "s_nop 1\n" \
    "s_nop 1\n" \
    "s_nop 1\n" \
    "s_nop 1\n" \
    "s_nop 1\n" \
    "s_nop 1\n" \
    "s_nop 1\n" \
    "s_nop 1\n" \
    "s_nop 1\n" \
    "s_nop 1\n" \
    "s_nop 1\n" \
    "s_nop 1\n" \
    "s_nop 1\n" \
    "s_nop 1\n" \
    "s_nop 1\n" \
    "s_nop 1\n" \
    "s_nop 1\n" \
    "s_nop 1\n" \
    "s_nop 1\n" \
    "s_nop 1\n" \
    "s_nop 1\n" \
    "Xwd_%=:\n" \
    "Xagain_%=:\n" \
    "s_memtime s[96:97]\n" \
    "s_memrealtime s[94:95]\n" \
    "buffer_load_dwordx4 v[32:35], %[l16], %[rsH], s40 offen sc1\n" \
    "v_mov_b32 v123, 0\n" \
    "s_nop 10\n" \
    "buffer_load_dwordx4 v[36:39], %[l16], %[rsH], s40 offen offset:1024 sc1\n" \
    "s_nop 11\n" \
    "buffer_load_dwordx4 v[40:43], %[l16], %[rsH], s40 offen offset:2048 sc1\n" \
    "s_nop 11\n" \
    "buffer_load_dwordx4 v[44:47], %[l16], %[rsH], s40 offen offset:3072 sc1\n" \
    "s_nop 11\n" \
    "buffer_load_dwordx4 v[48:51], %[l16], %[rsH], s41 offen sc1\n" \
    "s_nop 11\n" \
    "buffer_load_dwordx4 v[52:55], %[l16], %[rsH], s41 offen offset:1024 sc1\n" \
    "s_nop 11\n" \
    "buffer_load_dwordx4 v[56:59], %[l16], %[rsH], s41 offen offset:2048 sc1\n" \
    "s_waitcnt vmcnt(6)\n" \
    "v_mfma_f32_16x16x32_bf16 v[60:63], a[0:3], v[32:35], 0\n" \
    "v_max3_u32 v123, v123, v32, v33\n" \
    "v_mfma_f32_16x16x32_bf16 v[64:67], a[4:7], v[32:35], 0\n" \
    "v_max3_u32 v123, v123, v34, v35\n" \
    "v_mfma_f32_16x16x32_bf16 v[68:71], a[8:11], v[32:35], 0\n" \
    "v_mfma_f32_16x16x32_bf16 v[72:75], a[12:15], v[32:35], 0\n" \
    "s_waitcnt vmcnt(5)\n" \
    "v_mfma_f32_16x16x32_bf16 v[60:63], a[16:19], v[36:39], v[60:63]\n" \
    "v_max3_u32 v123, v123, v36, v37\n" \
    "v_mfma_f32_16x16x32_bf16 v[64:67], a[20:23], v[36:39], v[64:67]\n" \
    "v_max3_u32 v123, v123, v38, v39\n" \
    "v_mfma_f32_16x16x32_bf16 v[68:71], a[24:27], v[36:39], v[68:71]\n" \
    "v_mfma_f32_16x16x32_bf16 v[72:75], a[28:31], v[36:39], v[72:75]\n" \
    "s_waitcnt vmcnt(4)\n" \
    "v_mfma_f32_16x16x32_bf16 v[60:63], a[32:35], v[40:43], v[60:63]\n" \
    "v_max3_u32 v123, v123, v40, v41\n" \
    "v_mfma_f32_16x16x32_bf16 v[64:67], a[36:39], v[40:43], v[64:67]\n" \
    "v_max3_u32 v123, v123, v42, v43\n" \
    "v_mfma_f32_16x16x32_bf16 v[68:71], a[40:43], v[40:43], v[68:71]\n" \
    "v_mfma_f32_16x16x32_bf16 v[72:75], a[44:47], v[40:43], v[72:75]\n" \
    "s_waitcnt vmcnt(3)\n" \
    "v_mfma_f32_16x16x32_bf16 v[60:63], a[48:51], v[44:47], v[60:63]\n" \
    "v_max3_u32 v123, v123, v44, v45\n" \
    "v_mfma_f32_16x16x32_bf16 v[64:67], a[52:55], v[44:47], v[64:67]\n" \
    "v_max3_u32 v123, v123, v46, v47\n" \
    "v_mfma_f32_16x16x32_bf16 v[68:71], a[56:59], v[44:47], v[68:71]\n" \
    "v_mfma_f32_16x16x32_bf16 v[72:75], a[60:63], v[44:47], v[72:75]\n" \
    "s_waitcnt vmcnt(2)\n" \
    "v_mfma_f32_16x16x32_bf16 v[60:63], a[64:67], v[48:51], v[60:63]\n" \
    "v_max3_u32 v123, v123, v48, v49\n" \
    "v_mfma_f32_16x16x32_bf16 v[64:67], a[68:71], v[48:51], v[64:67]\n" \
    "v_max3_u32 v123, v123, v50, v51\n" \
    "v_mfma_f32_16x16x32_bf16 v[68:71], a[72:75], v[48:51], v[68:71]\n" \
    "v_mfma_f32_16x16x32_bf16 v[72:75], a[76:79], v[48:51], v[72:75]\n" \
    "s_waitcnt vmcnt(1)\n" \
    "v_mfma_f32_16x16x32_bf16 v[60:63], a[80:83], v[52:55], v[60:63]\n" \
    "v_max3_u32 v123, v123, v52, v53\n" \
    "v_mfma_f32_16x16x32_bf16 v[64:67], a[84:87], v[52:55], v[64:67]\n" \
    "v_max3_u32 v123, v123, v54, v55\n" \
    "v_mfma_f32_16x16x32_bf16 v[68:71], a[88:91], v[52:55], v[68:71]\n" \
    "v_mfma_f32_16x16x32_bf16 v[72:75], a[92:95], v[52:55], v[72:75]\n" \
    "s_waitcnt vmcnt(0)\n" \
    "v_mfma_f32_16x16x32_bf16 v[60:63], a[96:99], v[56:59], v[60:63]\n" \
    "v_max3_u32 v123, v123, v56, v57\n" \
    "v_mfma_f32_16x16x32_bf16 v[64:67], a[100:103], v[56:59], v[64:67]\n" \
    "v_max3_u32 v123, v123, v58, v59\n" \
    "v_mfma_f32_16x16x32_bf16 v[68:71], a[104:107], v[56:59], v[68:71]\n" \
    "v_cmp_eq_u32 vcc, -1, v123\n" \
    "v_mfma_f32_16x16x32_bf16 v[72:75], a[108:111], v[56:59], v[72:75]\n" \
    "s_cbranch_vccnz Xfail_%=\n" \
    "Xfail_go_%=:\n" \
    "s_nop 0\n" \
    "s_nop 4\n" \
    "v_add_f32_dpp v76, v68, v60 row_ror:8 row_mask:0xf bank_mask:0x3\n" \
    "v_add_f32_dpp v77, v69, v61 row_ror:8 row_mask:0xf bank_mask:0x3\n" \
    "v_add_f32_dpp v78, v70, v62 row_ror:8 row_mask:0xf bank_mask:0x3\n" \
    "v_add_f32_dpp v79, v71, v63 row_ror:8 row_mask:0xf bank_mask:0x3\n" \
    "v_add_f32_dpp v76, v64, v72 row_ror:8 row_mask:0xf bank_mask:0xc\n" \
    "v_add_f32_dpp v77, v65, v73 row_ror:8 row_mask:0xf bank_mask:0xc\n" \
    "v_add_f32_dpp v78, v66, v74 row_ror:8 row_mask:0xf bank_mask:0xc\n" \
    "v_add_f32_dpp v79, v67, v75 row_ror:8 row_mask:0xf bank_mask:0xc\n" \
    "v_exp_f32 v80, v76\n" \
    "v_exp_f32 v81, v77\n" \
    "v_exp_f32 v82, v78\n" \
    "v_exp_f32 v83, v79\n" \
    "v_max3_f32 v88, |v76|, |v77|, |v78|\n" \
    "v_cvt_pk_bf16_f32 v84, v80, v81\n" \
    "v_cvt_pk_bf16_f32 v85, v82, v83\n" \
    "v_max_f32 v88, v88, |v79|\n" \
    "v_cmp_lt_f32 vcc, 0x42480000, v88\n" \
    "v_mov_b32_dpp v86, v84 row_ror:8 row_mask:0xf bank_mask:0xf\n" \
    "v_mov_b32_dpp v87, v85 row_ror:8 row_mask:0xf bank_mask:0xf\n" \
    "s_cbranch_vccnz Xslow_%=\n" \
    "Xpub_%=:\n" \
    "buffer_store_dwordx4 v[84:87], %[pubE], %[rsH], s46 offen sc1\n" \
    "ds_write_b32 %[aS], v100\n" \
    "ds_add_rtn_u32 v108, %[cnt], %[one16]\n" \
    "ds_read_b32 v110, %[rdS] offset:0\n" \
    "ds_read_b32 v111, %[rdS] offset:256\n" \
    "ds_read_b32 v112, %[rdS] offset:512\n" \
    "ds_read_b32 v113, %[rdS] offset:768\n" \
    "s_waitcnt lgkmcnt(0)\n" \
    "v_add_f32 v110, v110, v111\n" \
    "v_add_f32 v112, v112, v113\n" \
    "v_cmp_eq_u32 vcc, s49, v108\n" \
    "v_add_f32 v116, v110, v112\n" \
    "v_add_f32 v117, v110, v112\n" \
    "s_cbranch_vccz Xnp_%=\n" \
    "v_cndmask_b32 v122, v121, %[pubS], vcc\n" \
    "v_permlane16_swap_b32 v116, v117\n" \
    "v_mov_b32 v118, v116\n" \
    "v_mov_b32 v119, v117\n" \
    "s_nop 0\n" \
    "v_permlane32_swap_b32 v116, v118\n" \
    "v_permlane32_swap_b32 v117, v119\n" \
    "buffer_store_dwordx4 v[116:119], v122, %[rsH], s47 offen sc1\n" \
    "s_memtime s[98:99]\n" \
    "v_cndmask_b32 v122, v121, %[rdys], vcc\n" \
    "s_waitcnt lgkmcnt(0)\n" \
    "s_sub_u32 s98, s98, s96\n" \
    "s_mul_i32 s98, s98, 87\n" \
    "s_lshr_b32 s98, s98, 8\n" \
    "s_lshl_b32 s57, s94, 3\n" \
    "s_sub_i32 s57, s57, s53\n" \
    "s_max_i32 s57, s57, 0\n" \
    "s_add_u32 s57, s57, s53\n" \
    "s_add_u32 s57, s57, s98\n" \
    "s_and_b32 s57, s57, -2\n" \
    "v_mov_b32 v242, s57\n" \
    "s_cmp_lt_u32 s48, 63\n" \
    "s_cselect_b32 s60, 16384, 0\n" \
    "s_add_u32 s60, s60, s90\n" \
    "buffer_store_dword v242, v122, %[rsH], s60 offen sc1\n" \
    "Xnp_%=:\n" \
    "s_add_u32 s48, s48, 1\n" \
    "v_mov_b32 v100, 0\n" \
    "s_cmp_ge_u32 s48, 63\n" \
    "s_cbranch_scc1 Xend_%=\n" \
    "s_mov_b32 s65, s48\n" \
    "v_min_u32_dpp v239, v239, v239 row_ror:8 row_mask:0xf bank_mask:0xf\n" \
    "s_nop 1\n" \
    "v_min_u32_dpp v239, v239, v239 row_ror:4 row_mask:0xf bank_mask:0xf\n" \
    "s_nop 1\n" \
    "v_min_u32_dpp v239, v239, v239 row_ror:2 row_mask:0xf bank_mask:0xf\n" \
    "s_nop 1\n" \
    "v_min_u32_dpp v239, v239, v239 row_ror:1 row_mask:0xf bank_mask:0xf\n" \
    "s_nop 1\n" \
    "v_readfirstlane_b32 s60, v239\n" \
    "s_nop 3\n" \
    "s_max_u32 s64, s64, s60\n" \
    "s_cmp_gt_u32 s64, s65\n" \
    "s_cbranch_scc1 Xf1_ok_%=\n" \
    "s_mov_b32 s66, 0\n" \
    "Xf1_poll_%=:\n" \
    "buffer_load_dword v240, %[flg], %[rsW], 0 offen sc1\n" \
    "s_waitcnt vmcnt(0)\n" \
    "s_nop 1\n" \
    "v_min_u32_dpp v240, v240, v240 row_ror:8 row_mask:0xf bank_mask:0xf\n" \
    "s_nop 1\n" \
    "v_min_u32_dpp v240, v240, v240 row_ror:4 row_mask:0xf bank_mask:0xf\n" \
    "s_nop 1\n" \
    "v_min_u32_dpp v240, v240, v240 row_ror:2 row_mask:0xf bank_mask:0xf\n" \
    "s_nop 1\n" \
    "v_min_u32_dpp v240, v240, v240 row_ror:1 row_mask:0xf bank_mask:0xf\n" \
    "s_nop 1\n" \
    "v_readfirstlane_b32 s64, v240\n" \
    "s_nop 3\n" \
    "s_cmp_gt_u32 s64, s65\n" \
    "s_cbranch_scc1 Xf1_ok_%=\n" \
    "s_sleep 4\n" \
    "s_add_u32 s66, s66, 1\n" \
    "s_cmp_lt_u32 s66, s61\n" \
    "s_cbranch_scc1 Xf1_poll_%=\n" \
    "s_mov_b32 s61, 0\n" \
    "Xf1_ok_%=:\n" \
    "s_mov_b32 s66, 0\n" \
    "Xall_%=:\n" \
    "ds_read_b32 v108, %[cnt]\n" \
    "s_add_u32 s66, s66, 1\n" \
    "s_waitcnt lgkmcnt(0)\n" \
    "v_readfirstlane_b32 s60, v108\n" \
    "s_cmp_ge_u32 s66, 64\n" \
    "s_cbranch_scc1 Xalk_%=\n" \
    "s_cmp_gt_u32 s60, s49\n" \
    "s_cbranch_scc0 Xall_%=\n" \
    "Xalk_%=:\n" \
    "s_sleep 1\n" \
    "buffer_load_dwordx2 v[240:241], %[stpo], %[rsH], s90 offen sc1\n" \
    "s_lshr_b32 s52, s48, 1\n" \
    "s_lshr_b32 s60, s67, 16\n" \
    "s_xor_b32 s60, s60, s52\n" \
    "s_and_b32 s60, s60, 1\n" \
    "s_lshl_b32 s60, s60, 13\n" \
    "s_lshl_b32 s52, s48, 20\n" \
    "s_add_u32 s52, s52, s60\n" \
    "s_add_u32 s52, s52, %[kpb]\n" \
    "s_add_u32 s60, s52, s42\n" \
    "buffer_load_dwordx4 a[0:3], %[l16], %[rsW], s60 offen offset:0 sc1\n" \
    "buffer_load_dwordx4 a[4:7], %[l16], %[rsW], s60 offen offset:1024 sc1\n" \
    "buffer_load_dwordx4 a[8:11], %[l16], %[rsW], s60 offen offset:2048 sc1\n" \
    "buffer_load_dwordx4 a[12:15], %[l16], %[rsW], s60 offen offset:3072 sc1\n" \
    "s_add_u32 s60, s52, s43\n" \
    "buffer_load_dwordx4 a[16:19], %[l16], %[rsW], s60 offen offset:0 sc1\n" \
    "buffer_load_dwordx4 a[20:23], %[l16], %[rsW], s60 offen offset:1024 sc1\n" \
    "buffer_load_dwordx4 a[24:27], %[l16], %[rsW], s60 offen offset:2048 sc1\n" \
    "buffer_load_dwordx4 a[28:31], %[l16], %[rsW], s60 offen offset:3072 sc1\n" \
    "s_add_u32 s60, s52, s44\n" \
    "buffer_load_dwordx4 a[32:35], %[l16], %[rsW], s60 offen offset:0 sc1\n" \
    "buffer_load_dwordx4 a[36:39], %[l16], %[rsW], s60 offen offset:1024 sc1\n" \
    "buffer_load_dwordx4 a[40:43], %[l16], %[rsW], s60 offen offset:2048 sc1\n" \
    "buffer_load_dwordx4 a[44:47], %[l16], %[rsW], s60 offen offset:3072 sc1\n" \
    "s_add_u32 s60, s52, s45\n" \
    "buffer_load_dwordx4 a[48:51], %[l16], %[rsW], s60 offen offset:0 sc1\n" \
    "buffer_load_dwordx4 a[52:55], %[l16], %[rsW], s60 offen offset:1024 sc1\n" \
    "buffer_load_dwordx4 a[56:59], %[l16], %[rsW], s60 offen offset:2048 sc1\n" \
    "buffer_load_dwordx4 a[60:63], %[l16], %[rsW], s60 offen offset:3072 sc1\n" \
    "s_add_u32 s60, s52, s50\n" \
    "buffer_load_dwordx4 a[64:67], %[l16], %[rsW], s60 offen offset:0 sc1\n" \
    "buffer_load_dwordx4 a[68:71], %[l16], %[rsW], s60 offen offset:1024 sc1\n" \
    "buffer_load_dwordx4 a[72:75], %[l16], %[rsW], s60 offen offset:2048 sc1\n" \
    "buffer_load_dwordx4 a[76:79], %[l16], %[rsW], s60 offen offset:3072 sc1\n" \
    "s_add_u32 s60, s52, s51\n" \
    "buffer_load_dwordx4 a[80:83], %[l16], %[rsW], s60 offen offset:0 sc1\n" \
    "buffer_load_dwordx4 a[84:87], %[l16], %[rsW], s60 offen offset:1024 sc1\n" \
    "buffer_load_dwordx4 a[88:91], %[l16], %[rsW], s60 offen offset:2048 sc1\n" \
    "buffer_load_dwordx4 a[92:95], %[l16], %[rsW], s60 offen offset:3072 sc1\n" \
    "s_add_u32 s60, s52, s91\n" \
    "buffer_load_dwordx4 a[96:99], %[l16], %[rsW], s60 offen offset:0 sc1\n" \
    "buffer_load_dwordx4 a[100:103], %[l16], %[rsW], s60 offen offset:1024 sc1\n" \
    "buffer_load_dwordx4 a[104:107], %[l16], %[rsW], s60 offen offset:2048 sc1\n" \
    "buffer_load_dwordx4 a[108:111], %[l16], %[rsW], s60 offen offset:3072 sc1\n" \
    "s_add_u32 s60, s48, 3\n" \
    "s_cmp_gt_u32 s64, s60\n" \
    "s_cselect_b64 vcc, -1, 0\n" \
    "v_cndmask_b32 v238, %[flg], v121, vcc\n" \
    "buffer_load_dword v239, v238, %[rsW], 0 offen sc1\n" \
    "s_waitcnt vmcnt(29)\n" \
    "v_subrev_u32 v237, s68, v240\n" \
    "v_cmp_eq_u32 s[38:39], -1, v240\n" \
    "v_cmp_lt_i32 vcc, 0, v237\n" \
    "s_or_b64 vcc, vcc, s[38:39]\n" \
    "s_cbranch_vccz Xsc_n_%=\n" \
    "s_mov_b32 s66, 0\n" \
    "Xsc_p_%=:\n" \
    "buffer_load_dwordx2 v[240:241], %[stpo], %[rsH], s90 offen sc1\n" \
    "s_add_u32 s66, s66, 1\n" \
    "s_waitcnt vmcnt(0)\n" \
    "v_cmp_eq_u32 vcc, -1, v240\n" \
    "s_cbranch_vccz Xsc_ok_%=\n" \
    "s_sleep 3\n" \
    "s_cmp_lt_u32 s66, s61\n" \
    "s_cbranch_scc1 Xsc_p_%=\n" \
    "s_mov_b32 s61, 0\n" \
    "Xsc_ok_%=:\n" \
    "v_readfirstlane_b32 s60, v240\n" \
    "s_nop 3\n" \
    "v_subrev_u32 v240, s60, v240\n" \
    "s_nop 1\n" \
    "v_max_i32_dpp v240, v240, v240 row_ror:8 row_mask:0xf bank_mask:0xf\n" \
    "s_nop 1\n" \
    "v_max_i32_dpp v240, v240, v240 row_ror:4 row_mask:0xf bank_mask:0xf\n" \
    "s_nop 1\n" \
    "v_max_i32_dpp v240, v240, v240 row_ror:2 row_mask:0xf bank_mask:0xf\n" \
    "s_nop 1\n" \
    "v_max_i32_dpp v240, v240, v240 row_ror:1 row_mask:0xf bank_mask:0xf\n" \
    "v_mov_b32 v243, v240\n" \
    "s_nop 1\n" \
    "v_permlane16_swap_b32 v240, v243\n" \
    "s_nop 1\n" \
    "v_max_i32 v240, v240, v243\n" \
    "v_mov_b32 v243, v240\n" \
    "s_nop 1\n" \
    "v_permlane32_swap_b32 v240, v243\n" \
    "s_nop 1\n" \
    "v_max_i32 v240, v240, v243\n" \
    "s_nop 1\n" \
    "v_readfirstlane_b32 s57, v240\n" \
    "s_nop 3\n" \
    "s_add_u32 s60, s60, s57\n" \
    "s_sub_u32 s57, s53, %[thr]\n" \
    "s_sub_i32 s57, s60, s57\n" \
    "s_cmp_gt_i32 s57, 0\n" \
    "s_cbranch_scc0 Xsc_k_%=\n" \
    "s_sub_i32 s57, s60, s53\n" \
    "s_add_u32 s60, s57, s37\n" \
    "s_cmp_le_i32 s57, -3200\n" \
    "s_cselect_b32 s60, 1825, s60\n" \
    "s_cmp_le_i32 s57, -4100\n" \
    "s_cselect_b32 s60, 920, s60\n" \
    "s_add_u32 s53, s53, s60\n" \
    "s_mov_b32 s89, 190\n" \
    "Xsc_k_%=:\n" \
    "Xsc_n_%=:\n" \
    "v_readlane_b32 s57, v241, 28\n" \
    "s_nop 3\n" \
    "s_and_b32 s57, s57, 0xffff\n" \
    "s_add_u32 s53, s53, s57\n" \
    "s_add_u32 s53, s53, s89\n" \
    "s_add_u32 s37, s37, 400\n" \
    "s_min_u32 s37, s37, 2550\n" \
    "s_cmp_eq_u32 s89, 0\n" \
    "s_cselect_b32 s37, 950, s37\n" \
    "s_add_u32 s90, s90, 16384\n" \
    "s_add_u32 s40, s40, 32768\n" \
    "s_add_u32 s41, s41, 32768\n" \
    "s_add_u32 s47, s47, 32768\n" \
    "s_add_u32 s46, s46, 16384\n" \
    "s_add_u32 s49, s49, 4\n" \
    "s_branch Xstep_%=\n" \
    "Xslow_%=:\n" \
    "v_max3_f32 v88, v76, v77, v78\n" \
    "v_max_f32 v88, v88, v79\n" \
    "s_nop 1\n" \
    "v_max_f32_dpp v88, v88, v88 row_ror:8 row_mask:0xf bank_mask:0xf\n" \
    "v_mov_b32 v89, v88\n" \
    "s_nop 1\n" \
    "v_permlane16_swap_b32 v88, v89\n" \
    "s_nop 1\n" \
    "v_max_f32 v88, v88, v89\n" \
    "v_mov_b32 v89, v88\n" \
    "s_nop 1\n" \
    "v_permlane32_swap_b32 v88, v89\n" \
    "s_nop 1\n" \
    "v_max_f32 v88, v88, v89\n" \
    "v_sub_f32 v80, v76, v88\n" \
    "v_sub_f32 v81, v77, v88\n" \
    "v_sub_f32 v82, v78, v88\n" \
    "v_sub_f32 v83, v79, v88\n" \
    "v_exp_f32 v80, v80\n" \
    "v_exp_f32 v81, v81\n" \
    "v_exp_f32 v82, v82\n" \
    "v_exp_f32 v83, v83\n" \
    "v_sub_f32 v100, 0, v88\n" \
    "v_cvt_pk_bf16_f32 v84, v80, v81\n" \
    "v_cvt_pk_bf16_f32 v85, v82, v83\n" \
    "s_nop 1\n" \
    "v_mov_b32_dpp v86, v84 row_ror:8 row_mask:0xf bank_mask:0xf\n" \
    "v_mov_b32_dpp v87, v85 row_ror:8 row_mask:0xf bank_mask:0xf\n" \
    "s_branch Xpub_%=\n" \
    "Xfail_%=:\n" \
    "s_add_u32 s58, s58, 1\n" \
    "s_add_u32 s59, s59, 1\n" \
    "s_cmp_ge_u32 s58, s61\n" \
    "s_cbranch_scc1 Xfail_up_%=\n" \
    "s_cmp_le_u32 s58, 0\n" \
    "s_cbranch_scc1 Xagain_%=\n" \
    "s_mov_b32 s66, 0\n" \
    "Xfail_p_%=:\n" \
    "buffer_load_dwordx2 v[240:241], %[stpo], %[rsH], s90 offen sc1\n" \
    "s_add_u32 s66, s66, 1\n" \
    "s_waitcnt vmcnt(0)\n" \
    "v_cmp_eq_u32 vcc, -1, v240\n" \
    "s_cbranch_vccz Xfail_ok_%=\n" \
    "s_sleep 3\n" \
    "s_cmp_lt_u32 s66, s61\n" \
    "s_cbranch_scc1 Xfail_p_%=\n" \
    "s_mov_b32 s61, 0\n" \
    "Xfail_ok_%=:\n" \
    "v_readfirstlane_b32 s60, v240\n" \
    "s_nop 3\n" \
    "v_subrev_u32 v240, s60, v240\n" \
    "s_nop 1\n" \
    "v_max_i32_dpp v240, v240, v240 row_ror:8 row_mask:0xf bank_mask:0xf\n" \
    "s_nop 1\n" \
    "v_max_i32_dpp v240, v240, v240 row_ror:4 row_mask:0xf bank_mask:0xf\n" \
    "s_nop 1\n" \
    "v_max_i32_dpp v240, v240, v240 row_ror:2 row_mask:0xf bank_mask:0xf\n" \
    "s_nop 1\n" \
    "v_max_i32_dpp v240, v240, v240 row_ror:1 row_mask:0xf bank_mask:0xf\n" \
    "v_mov_b32 v243, v240\n" \
    "s_nop 1\n" \
    "v_permlane16_swap_b32 v240, v243\n" \
    "s_nop 1\n" \
    "v_max_i32 v240, v240, v243\n" \
    "v_mov_b32 v243, v240\n" \
    "s_nop 1\n" \
    "v_permlane32_swap_b32 v240, v243\n" \
    "s_nop 1\n" \
    "v_max_i32 v240, v240, v243\n" \
    "s_nop 1\n" \
    "v_readfirstlane_b32 s57, v240\n" \
    "s_nop 3\n" \
    "s_add_u32 s60, s60, s57\n" \
    "s_sub_u32 s57, s53, %[thr]\n" \
    "s_sub_i32 s57, s60, s57\n" \
    "s_cmp_gt_i32 s57, 0\n" \
    "s_cbranch_scc0 Xfail_k_%=\n" \
    "s_sub_i32 s57, s60, s53\n" \
    "s_add_u32 s60, s57, s37\n" \
    "s_cmp_le_i32 s57, -3200\n" \
    "s_cselect_b32 s60, 1825, s60\n" \
    "s_cmp_le_i32 s57, -4100\n" \
    "s_cselect_b32 s60, 920, s60\n" \
    "s_add_u32 s53, s53, s60\n" \
    "s_mov_b32 s89, 190\n" \
    "Xfail_k_%=:\n" \
    "s_branch Xtry_%=\n" \
    "Xfail_up_%=:\n" \
    "s_mov_b32 s61, 0\n" \
    "s_branch Xfail_go_%=\n" \
    "Xend_%=:\n" \
    "s_waitcnt vmcnt(0) lgkmcnt(0)\n"

#define ASM_M \
    "s_nop 4\n" \
    "s_add_u32 s60, %[woff], 0\n" \
    "buffer_load_dwordx4 a[0:3], %[l16], %[rsW], s60 offen\n" \
    "buffer_load_dwordx4 a[4:7], %[l16], %[rsW], s60 offen offset:1024\n" \
    "s_add_u32 s60, %[woff], 2048\n" \
    "buffer_load_dwordx4 a[8:11], %[l16], %[rsW], s60 offen\n" \
    "buffer_load_dwordx4 a[12:15], %[l16], %[rsW], s60 offen offset:1024\n" \
    "s_add_u32 s60, %[woff], 4096\n" \
    "buffer_load_dwordx4 a[16:19], %[l16], %[rsW], s60 offen\n" \
    "buffer_load_dwordx4 a[20:23], %[l16], %[rsW], s60 offen offset:1024\n" \
    "s_add_u32 s60, %[woff], 6144\n" \
    "buffer_load_dwordx4 a[24:27], %[l16], %[rsW], s60 offen\n" \
    "buffer_load_dwordx4 a[28:31], %[l16], %[rsW], s60 offen offset:1024\n" \
    "s_add_u32 s60, %[woff], 8192\n" \
    "buffer_load_dwordx4 a[32:35], %[l16], %[rsW], s60 offen\n" \
    "buffer_load_dwordx4 a[36:39], %[l16], %[rsW], s60 offen offset:1024\n" \
    "s_add_u32 s60, %[woff], 10240\n" \
    "buffer_load_dwordx4 a[40:43], %[l16], %[rsW], s60 offen\n" \
    "buffer_load_dwordx4 a[44:47], %[l16], %[rsW], s60 offen offset:1024\n" \
    "s_add_u32 s60, %[woff], 12288\n" \
    "buffer_load_dwordx4 a[48:51], %[l16], %[rsW], s60 offen\n" \
    "buffer_load_dwordx4 a[52:55], %[l16], %[rsW], s60 offen offset:1024\n" \
    "s_add_u32 s60, %[woff], 401408\n" \
    "buffer_load_dwordx4 a[56:59], %[l16], %[rsW], s60 offen\n" \
    "buffer_load_dwordx4 a[60:63], %[l16], %[rsW], s60 offen offset:1024\n" \
    "s_add_u32 s60, %[woff], 403456\n" \
    "buffer_load_dwordx4 a[64:67], %[l16], %[rsW], s60 offen\n" \
    "buffer_load_dwordx4 a[68:71], %[l16], %[rsW], s60 offen offset:1024\n" \
    "s_add_u32 s60, %[woff], 405504\n" \
    "buffer_load_dwordx4 a[72:75], %[l16], %[rsW], s60 offen\n" \
    "buffer_load_dwordx4 a[76:79], %[l16], %[rsW], s60 offen offset:1024\n" \
    "s_add_u32 s60, %[woff], 407552\n" \
    "buffer_load_dwordx4 a[80:83], %[l16], %[rsW], s60 offen\n" \
    "buffer_load_dwordx4 a[84:87], %[l16], %[rsW], s60 offen offset:1024\n" \
    "s_add_u32 s60, %[woff], 409600\n" \
    "buffer_load_dwordx4 a[88:91], %[l16], %[rsW], s60 offen\n" \
    "buffer_load_dwordx4 a[92:95], %[l16], %[rsW], s60 offen offset:1024\n" \
    "s_add_u32 s60, %[woff], 411648\n" \
    "buffer_load_dwordx4 a[96:99], %[l16], %[rsW], s60 offen\n" \
    "buffer_load_dwordx4 a[100:103], %[l16], %[rsW], s60 offen offset:1024\n" \
    "s_add_u32 s60, %[woff], 413696\n" \
    "buffer_load_dwordx4 a[104:107], %[l16], %[rsW], s60 offen\n" \
    "buffer_load_dwordx4 a[108:111], %[l16], %[rsW], s60 offen offset:1024\n" \
    "s_add_u32 s60, %[woff], 802816\n" \
    "buffer_load_dwordx4 a[112:115], %[l16], %[rsW], s60 offen\n" \
    "buffer_load_dwordx4 a[116:119], %[l16], %[rsW], s60 offen offset:1024\n" \
    "s_add_u32 s60, %[woff], 804864\n" \
    "buffer_load_dwordx4 a[120:123], %[l16], %[rsW], s60 offen\n" \
    "buffer_load_dwordx4 a[124:127], %[l16], %[rsW], s60 offen offset:1024\n" \
    "s_add_u32 s60, %[woff], 806912\n" \
    "buffer_load_dwordx4 a[128:131], %[l16], %[rsW], s60 offen\n" \
    "buffer_load_dwordx4 a[132:135], %[l16], %[rsW], s60 offen offset:1024\n" \
    "s_add_u32 s60, %[woff], 808960\n" \
    "buffer_load_dwordx4 a[136:139], %[l16], %[rsW], s60 offen\n" \
    "buffer_load_dwordx4 a[140:143], %[l16], %[rsW], s60 offen offset:1024\n" \
    "s_add_u32 s60, %[woff], 811008\n" \
    "buffer_load_dwordx4 a[144:147], %[l16], %[rsW], s60 offen\n" \
    "buffer_load_dwordx4 a[148:151], %[l16], %[rsW], s60 offen offset:1024\n" \
    "s_add_u32 s60, %[woff], 813056\n" \
    "buffer_load_dwordx4 a[152:155], %[l16], %[rsW], s60 offen\n" \
    "buffer_load_dwordx4 a[156:159], %[l16], %[rsW], s60 offen offset:1024\n" \
    "s_add_u32 s60, %[woff], 815104\n" \
    "buffer_load_dwordx4 a[160:163], %[l16], %[rsW], s60 offen\n" \
    "buffer_load_dwordx4 a[164:167], %[l16], %[rsW], s60 offen offset:1024\n" \
    "s_add_u32 s60, %[woff], 1204224\n" \
    "buffer_load_dwordx4 a[168:171], %[l16], %[rsW], s60 offen\n" \
    "buffer_load_dwordx4 a[172:175], %[l16], %[rsW], s60 offen offset:1024\n" \
    "s_add_u32 s60, %[woff], 1206272\n" \
    "buffer_load_dwordx4 a[176:179], %[l16], %[rsW], s60 offen\n" \
    "buffer_load_dwordx4 a[180:183], %[l16], %[rsW], s60 offen offset:1024\n" \
    "s_add_u32 s60, %[woff], 1208320\n" \
    "buffer_load_dwordx4 a[184:187], %[l16], %[rsW], s60 offen\n" \
    "buffer_load_dwordx4 a[188:191], %[l16], %[rsW], s60 offen offset:1024\n" \
    "s_add_u32 s60, %[woff], 1210368\n" \
    "buffer_load_dwordx4 a[192:195], %[l16], %[rsW], s60 offen\n" \
    "buffer_load_dwordx4 a[196:199], %[l16], %[rsW], s60 offen offset:1024\n" \
    "s_add_u32 s60, %[woff], 1212416\n" \
    "buffer_load_dwordx4 a[200:203], %[l16], %[rsW], s60 offen\n" \
    "buffer_load_dwordx4 a[204:207], %[l16], %[rsW], s60 offen offset:1024\n" \
    "s_add_u32 s60, %[woff], 1214464\n" \
    "buffer_load_dwordx4 a[208:211], %[l16], %[rsW], s60 offen\n" \
    "buffer_load_dwordx4 a[212:215], %[l16], %[rsW], s60 offen offset:1024\n" \
    "s_add_u32 s60, %[woff], 1216512\n" \
    "buffer_load_dwordx4 a[216:219], %[l16], %[rsW], s60 offen\n" \
    "buffer_load_dwordx4 a[220:223], %[l16], %[rsW], s60 offen offset:1024\n" \
    "s_mov_b32 s48, 0\n" \
    "s_mov_b32 s59, 0\n" \
    "s_mov_b32 s61, 20000\n" \
    "s_mov_b32 s67, %[selm]\n" \
    "s_mov_b32 s37, 950\n" \
    "s_and_b32 s88, s67, 1\n" \
    "s_lshl_b32 s88, s88, 13\n" \
    "s_and_b32 s89, s67, 2\n" \
    "s_lshl_b32 s89, s89, 12\n" \
    "s_add_u32 s89, s89, 0x200000\n" \
    "s_add_u32 s90, s89, 0x100000\n" \
    "s_mov_b32 s40, s88\n" \
    "s_add_u32 s41, s88, 4096\n" \
    "s_mov_b32 s93, 2\n" \
    "s_mov_b32 s95, 0\n" \
    "s_bfe_u32 s94, s67, 0x10002\n" \
    "s_lshl_b32 s94, s94, 13\n" \
    "s_add_u32 s94, s94, 0x400000\n" \
    "s_add_u32 s51, s94, %[vmoff]\n" \
    "v_mov_b32 v84, -1\n" \
    "v_mov_b32 v85, -1\n" \
    "v_mov_b32 v86, -1\n" \
    "v_mov_b32 v87, -1\n" \
    "buffer_store_dwordx4 v[84:87], %[l16], %[rsH], s51 offen sc1\n" \
    "s_mov_b32 s45, s51\n" \
    "s_add_u32 s94, s94, 229376\n" \
    "s_add_u32 s92, s93, 1\n" \
    "s_lshr_b32 s60, s67, s92\n" \
    "s_and_b32 s60, s60, 1\n" \
    "s_lshl_b32 s60, s60, 13\n" \
    "s_lshl_b32 s91, s92, 21\n" \
    "s_add_u32 s60, s60, s91\n" \
    "s_add_u32 s95, s95, 1\n" \
    "s_cmp_eq_u32 s95, 9\n" \
    "s_cselect_b32 s94, s60, s94\n" \
    "s_cselect_b32 s93, s92, s93\n" \
    "s_cselect_b32 s95, 0, s95\n" \
    "s_add_u32 s51, s94, %[vmoff]\n" \
    "buffer_store_dwordx4 v[84:87], %[l16], %[rsH], s51 offen sc1\n" \
    "s_mov_b32 s46, s51\n" \
    "s_add_u32 s94, s94, 229376\n" \
    "s_add_u32 s92, s93, 1\n" \
    "s_lshr_b32 s60, s67, s92\n" \
    "s_and_b32 s60, s60, 1\n" \
    "s_lshl_b32 s60, s60, 13\n" \
    "s_lshl_b32 s91, s92, 21\n" \
    "s_add_u32 s60, s60, s91\n" \
    "s_add_u32 s95, s95, 1\n" \
    "s_cmp_eq_u32 s95, 9\n" \
    "s_cselect_b32 s94, s60, s94\n" \
    "s_cselect_b32 s93, s92, s93\n" \
    "s_cselect_b32 s95, 0, s95\n" \
    "s_add_u32 s51, s94, %[vmoff]\n" \
    "s_waitcnt vmcnt(0)\n" \
    "s_barrier\n" \
    "s_memrealtime s[54:55]\n" \
    "s_waitcnt lgkmcnt(0)\n" \
    "s_lshl_b32 s54, s54, 3\n" \
    "s_or_b32 s54, s54, 1\n" \
    "v_mov_b32 v240, s54\n" \
    "buffer_store_dword v240, %[rdyw], %[rsW], 0 offen sc1\n" \
    "s_mov_b32 s66, 0\n" \
    "Mini_%=:\n" \
    "buffer_load_dword v240, %[rdy0], %[rsW], 0 offen sc1\n" \
    "buffer_load_dword v242, %[rdy0b], %[rsW], 0 offen sc1\n" \
    "s_add_u32 s66, s66, 1\n" \
    "s_waitcnt vmcnt(0)\n" \
    "v_min_u32 v241, v240, v242\n" \
    "v_cmp_eq_u32 vcc, 0, v241\n" \
    "s_cbranch_vccz Mini_ok_%=\n" \
    "s_sleep 3\n" \
    "s_cmp_lt_u32 s66, s61\n" \
    "s_cbranch_scc1 Mini_%=\n" \
    "s_mov_b32 s61, 0\n" \
    "Mini_ok_%=:\n" \
    "v_sub_u32 v241, v242, v240\n" \
    "v_max_i32 v241, 0, v241\n" \
    "v_add_u32 v240, v240, v241\n" \
    "s_nop 1\n" \
    "v_readfirstlane_b32 s60, v240\n" \
    "s_nop 3\n" \
    "v_subrev_u32 v240, s60, v240\n" \
    "s_nop 1\n" \
    "v_max_i32_dpp v240, v240, v240 row_ror:8 row_mask:0xf bank_mask:0xf\n" \
    "s_nop 1\n" \
    "v_max_i32_dpp v240, v240, v240 row_ror:4 row_mask:0xf bank_mask:0xf\n" \
    "s_nop 1\n" \
    "v_max_i32_dpp v240, v240, v240 row_ror:2 row_mask:0xf bank_mask:0xf\n" \
    "s_nop 1\n" \
    "v_max_i32_dpp v240, v240, v240 row_ror:1 row_mask:0xf bank_mask:0xf\n" \
    "v_mov_b32 v243, v240\n" \
    "s_nop 1\n" \
    "v_permlane16_swap_b32 v240, v243\n" \
    "s_nop 1\n" \
    "v_max_i32 v240, v240, v243\n" \
    "v_mov_b32 v243, v240\n" \
    "s_nop 1\n" \
    "v_permlane32_swap_b32 v240, v243\n" \
    "s_nop 1\n" \
    "v_max_i32 v240, v240, v243\n" \
    "s_nop 1\n" \
    "v_readfirstlane_b32 s57, v240\n" \
    "s_nop 3\n" \
    "s_add_u32 s60, s60, s57\n" \
    "s_add_u32 s53, s60, 700\n" \
    "Mstep_%=:\n" \
    "s_mov_b32 s58, 0\n" \
    "s_mov_b32 s89, 0\n" \
    "Mtry_%=:\n" \
    "s_sub_u32 s68, s53, %[thr]\n" \
    "s_add_u32 s69, s53, %[d1]\n" \
    "Mwc_%=:\n" \
    "s_memrealtime s[54:55]\n" \
    "s_waitcnt lgkmcnt(0)\n" \
    "s_lshl_b32 s54, s54, 3\n" \
    "s_sub_i32 s56, s69, s54\n" \
    "s_cmp_gt_i32 s56, 170\n" \
    "s_cbranch_scc1 Mwc_%=\n" \
    "s_cmp_lt_i32 s56, 125\n" \
    "s_cbranch_scc1 Mwo_%=\n" \
    "s_memrealtime s[70:71]\n" \
    "s_nop 1\n" \
    "s_memrealtime s[72:73]\n" \
    "s_nop 1\n" \
    "s_memrealtime s[74:75]\n" \
    "s_nop 1\n" \
    "s_memrealtime s[76:77]\n" \
    "s_nop 1\n" \
    "s_memrealtime s[78:79]\n" \
    "s_nop 1\n" \
    "s_memrealtime s[80:81]\n" \
    "s_nop 1\n" \
    "s_memrealtime s[82:83]\n" \
    "s_nop 1\n" \
    "s_memrealtime s[84:85]\n" \
    "s_nop 1\n" \
    "s_memrealtime s[86:87]\n" \
    "s_mov_b32 s56, 0\n" \
    "s_waitcnt lgkmcnt(0)\n" \
    "s_cmp_eq_u32 s70, s72\n" \
    "s_addc_u32 s56, s56, 0\n" \
    "s_cmp_eq_u32 s70, s74\n" \
    "s_addc_u32 s56, s56, 0\n" \
    "s_cmp_eq_u32 s70, s76\n" \
    "s_addc_u32 s56, s56, 0\n" \
    "s_cmp_eq_u32 s70, s78\n" \
    "s_addc_u32 s56, s56, 0\n" \
    "s_cmp_eq_u32 s70, s80\n" \
    "s_addc_u32 s56, s56, 0\n" \
    "s_cmp_eq_u32 s70, s82\n" \
    "s_addc_u32 s56, s56, 0\n" \
    "s_cmp_eq_u32 s70, s84\n" \
    "s_addc_u32 s56, s56, 0\n" \
    "s_cmp_eq_u32 s70, s86\n" \
    "s_addc_u32 s56, s56, 0\n" \
    "s_lshl_b32 s70, s70, 3\n" \
    "s_sub_i32 s70, s69, s70\n" \
    "s_mul_i32 s70, s70, 94\n" \
    "s_mul_i32 s56, s56, 384\n" \
    "s_add_i32 s70, s70, s56\n" \
    "s_sub_i32 s70, s70, 10688\n" \
    "s_ashr_i32 s70, s70, 8\n" \
    "s_max_i32 s70, s70, 0\n" \
    "s_min_i32 s70, s70, 40\n" \
    "s_lshl_b32 s70, s70, 2\n" \
    "s_getpc_b64 s[54:55]\n" \
    "Mwb_%=:\n" \
    "s_add_u32 s54, s54, Mwd_%=-Mwb_%=\n" \
    "s_addc_u32 s55, s55, 0\n" \
    "s_sub_u32 s54, s54, s70\n" \
    "s_subb_u32 s55, s55, 0\n" \
    "s_setpc_b64 s[54:55]\n" \
    "Mwo_%=:\n" \
    "s_sub_i32 s56, s56, 40\n" \
    "s_max_i32 s56, s56, 0\n" \
    "Mw_%=:\n" \
    "s_sub_u32 s56, s56, 9\n" \
    "s_cbranch_scc0 Mw_%=\n" \
    "s_branch Mwd_%=\n" \
    "s_nop 1\n" \
    "s_nop 1\n" \
    "s_nop 1\n" \
    "s_nop 1\n" \
    "s_nop 1\n" \
    "s_nop 1\n" \
    "s_nop 1\n" \
    "s_nop 1\n" \
    "s_nop 1\n" \
    "s_nop 1\n" \
    "s_nop 1\n" \
    "s_nop 1\n" \
    "s_nop 1\n" \
    "s_nop 1\n" \
    "s_nop 1\n" \
    "s_nop 1\n" \
    "s_nop 1\n" \
    "s_nop 1\n" \
    "s_nop 1\n" \
    "s_nop 1\n" \
    "s_nop 1\n" \
    "s_nop 1\n" \
    "s_nop 1\n" \
    "s_nop 1\n" \
    "s_nop 1\n" \
    "s_nop 1\n" \
    "s_nop 1\n" \
    "s_nop 1\n" \
    "s_nop 1\n" \
    "s_nop 1\n" \
    "s_nop 1\n" \
    "s_nop 1\n" \
    "s_nop 1\n" \
    "s_nop 1\n" \
    "s_nop 1\n" \
    "s_nop 1\n" \
    "s_nop 1\n" \
    "s_nop 1\n" \
    "s_nop 1\n" \
    "s_nop 1\n" \
    "Mwd_%=:\n" \
    "Magain_%=:\n" \
    "buffer_load_dwordx4 v[32:35], %[l16], %[rsH], s40 offen sc1\n" \
    "v_mov_b64 v[60:61], 0\n" \
    "v_mov_b64 v[62:63], 0\n" \
    "v_mov_b64 v[64:65], 0\n" \
    "v_mov_b64 v[66:67], 0\n" \
    "v_mov_b64 v[68:69], 0\n" \
    "v_mov_b64 v[70:71], 0\n" \
    "v_mov_b64 v[72:73], 0\n" \
    "v_mov_b64 v[74:75], 0\n" \
    "s_nop 3\n" \
    "buffer_load_dwordx4 v[36:39], %[l16], %[rsH], s40 offen offset:1024 sc1\n" \
    "v_mov_b32 v123, 0\n" \
    "s_nop 10\n" \
    "buffer_load_dwordx4 v[40:43], %[l16], %[rsH], s40 offen offset:2048 sc1\n" \
    "s_nop 11\n" \
    "buffer_load_dwordx4 v[44:47], %[l16], %[rsH], s40 offen offset:3072 sc1\n" \
    "s_nop 11\n" \
    "buffer_load_dwordx4 v[48:51], %[l16], %[rsH], s41 offen sc1\n" \
    "s_nop 11\n" \
    "buffer_load_dwordx4 v[52:55], %[l16], %[rsH], s41 offen offset:1024 sc1\n" \
    "s_nop 11\n" \
    "buffer_load_dwordx4 v[56:59], %[l16], %[rsH], s41 offen offset:2048 sc1\n" \
    "s_waitcnt vmcnt(6)\n" \
    "v_smfmac_f32_16x16x64_bf16 v[60:63], v[32:35], a[0:7], %[idx]\n" \
    "v_max3_u32 v123, v123, v32, v33\n" \
    "v_smfmac_f32_16x16x64_bf16 v[64:67], v[32:35], a[56:63], %[idx]\n" \
    "v_max3_u32 v123, v123, v34, v35\n" \
    "v_smfmac_f32_16x16x64_bf16 v[68:71], v[32:35], a[112:119], %[idx]\n" \
    "v_smfmac_f32_16x16x64_bf16 v[72:75], v[32:35], a[168:175], %[idx]\n" \
    "s_waitcnt vmcnt(5)\n" \
    "v_smfmac_f32_16x16x64_bf16 v[60:63], v[36:39], a[8:15], %[idx]\n" \
    "v_max3_u32 v123, v123, v36, v37\n" \
    "v_smfmac_f32_16x16x64_bf16 v[64:67], v[36:39], a[64:71], %[idx]\n" \
    "v_max3_u32 v123, v123, v38, v39\n" \
    "v_smfmac_f32_16x16x64_bf16 v[68:71], v[36:39], a[120:127], %[idx]\n" \
    "v_smfmac_f32_16x16x64_bf16 v[72:75], v[36:39], a[176:183], %[idx]\n" \
    "s_waitcnt vmcnt(4)\n" \
    "v_smfmac_f32_16x16x64_bf16 v[60:63], v[40:43], a[16:23], %[idx]\n" \
    "v_max3_u32 v123, v123, v40, v41\n" \
    "v_smfmac_f32_16x16x64_bf16 v[64:67], v[40:43], a[72:79], %[idx]\n" \
    "v_max3_u32 v123, v123, v42, v43\n" \
    "v_smfmac_f32_16x16x64_bf16 v[68:71], v[40:43], a[128:135], %[idx]\n" \
    "v_smfmac_f32_16x16x64_bf16 v[72:75], v[40:43], a[184:191], %[idx]\n" \
    "s_waitcnt vmcnt(3)\n" \
    "v_smfmac_f32_16x16x64_bf16 v[60:63], v[44:47], a[24:31], %[idx]\n" \
    "v_max3_u32 v123, v123, v44, v45\n" \
    "v_smfmac_f32_16x16x64_bf16 v[64:67], v[44:47], a[80:87], %[idx]\n" \
    "v_max3_u32 v123, v123, v46, v47\n" \
    "v_smfmac_f32_16x16x64_bf16 v[68:71], v[44:47], a[136:143], %[idx]\n" \
    "v_smfmac_f32_16x16x64_bf16 v[72:75], v[44:47], a[192:199], %[idx]\n" \
    "s_waitcnt vmcnt(2)\n" \
    "v_smfmac_f32_16x16x64_bf16 v[60:63], v[48:51], a[32:39], %[idx]\n" \
    "v_max3_u32 v123, v123, v48, v49\n" \
    "v_smfmac_f32_16x16x64_bf16 v[64:67], v[48:51], a[88:95], %[idx]\n" \
    "v_max3_u32 v123, v123, v50, v51\n" \
    "v_smfmac_f32_16x16x64_bf16 v[68:71], v[48:51], a[144:151], %[idx]\n" \
    "v_smfmac_f32_16x16x64_bf16 v[72:75], v[48:51], a[200:207], %[idx]\n" \
    "s_waitcnt vmcnt(1)\n" \
    "v_smfmac_f32_16x16x64_bf16 v[60:63], v[52:55], a[40:47], %[idx]\n" \
    "v_max3_u32 v123, v123, v52, v53\n" \
    "v_smfmac_f32_16x16x64_bf16 v[64:67], v[52:55], a[96:103], %[idx]\n" \
    "v_max3_u32 v123, v123, v54, v55\n" \
    "v_smfmac_f32_16x16x64_bf16 v[68:71], v[52:55], a[152:159], %[idx]\n" \
    "v_smfmac_f32_16x16x64_bf16 v[72:75], v[52:55], a[208:215], %[idx]\n" \
    "s_waitcnt vmcnt(0)\n" \
    "v_smfmac_f32_16x16x64_bf16 v[60:63], v[56:59], a[48:55], %[idx]\n" \
    "v_max3_u32 v123, v123, v56, v57\n" \
    "v_smfmac_f32_16x16x64_bf16 v[64:67], v[56:59], a[104:111], %[idx]\n" \
    "v_max3_u32 v123, v123, v58, v59\n" \
    "v_smfmac_f32_16x16x64_bf16 v[68:71], v[56:59], a[160:167], %[idx]\n" \
    "v_cmp_eq_u32 vcc, -1, v123\n" \
    "v_smfmac_f32_16x16x64_bf16 v[72:75], v[56:59], a[216:223], %[idx]\n" \
    "s_cbranch_vccnz Mfail_%=\n" \
    "Mfail_go_%=:\n" \
    "v_permlane32_swap_b32 v60, v64\n" \
    "v_permlane32_swap_b32 v61, v65\n" \
    "v_permlane32_swap_b32 v62, v66\n" \
    "v_permlane32_swap_b32 v63, v67\n" \
    "v_pk_add_f32 v[60:61], v[60:61], v[64:65]\n" \
    "v_pk_add_f32 v[62:63], v[62:63], v[66:67]\n" \
    "s_nop 0\n" \
    "v_permlane32_swap_b32 v68, v72\n" \
    "v_permlane32_swap_b32 v69, v73\n" \
    "v_permlane32_swap_b32 v70, v74\n" \
    "v_permlane32_swap_b32 v71, v75\n" \
    "v_cvt_pk_bf16_f32 v76, v60, v61\n" \
    "v_cvt_pk_bf16_f32 v77, v62, v63\n" \
    "v_pk_add_f32 v[68:69], v[68:69], v[72:73]\n" \
    "v_pk_add_f32 v[70:71], v[70:71], v[74:75]\n" \
    "v_cvt_pk_bf16_f32 v78, v68, v69\n" \
    "v_cvt_pk_bf16_f32 v79, v70, v71\n" \
    "buffer_store_dwordx4 v[76:79], %[l16], %[rsH], s45 offen sc1\n" \
    "s_sleep 3\n" \
    "buffer_load_dwordx2 v[240:241], %[stpo], %[rsH], s90 offen sc1\n" \
    "s_cmp_lt_u32 s48, 62\n" \
    "s_cbranch_scc0 Mnf_%=\n" \
    "buffer_store_dwordx4 v[84:87], %[l16], %[rsH], s51 offen sc1\n" \
    "Mnf_%=:\n" \
    "s_mov_b32 s45, s46\n" \
    "s_mov_b32 s46, s51\n" \
    "s_waitcnt vmcnt(0)\n" \
    "v_subrev_u32 v237, s68, v240\n" \
    "v_cmp_eq_u32 s[38:39], -1, v240\n" \
    "v_cmp_lt_i32 vcc, 0, v237\n" \
    "s_or_b64 vcc, vcc, s[38:39]\n" \
    "s_cbranch_vccz Msc_n_%=\n" \
    "s_mov_b32 s66, 0\n" \
    "Msc_p_%=:\n" \
    "buffer_load_dwordx2 v[240:241], %[stpo], %[rsH], s90 offen sc1\n" \
    "s_add_u32 s66, s66, 1\n" \
    "s_waitcnt vmcnt(0)\n" \
    "v_cmp_eq_u32 vcc, -1, v240\n" \
    "s_cbranch_vccz Msc_ok_%=\n" \
    "s_sleep 3\n" \
    "s_cmp_lt_u32 s66, s61\n" \
    "s_cbranch_scc1 Msc_p_%=\n" \
    "s_mov_b32 s61, 0\n" \
    "Msc_ok_%=:\n" \
    "v_readfirstlane_b32 s60, v240\n" \
    "s_nop 3\n" \
    "v_subrev_u32 v240, s60, v240\n" \
    "s_nop 1\n" \
    "v_max_i32_dpp v240, v240, v240 row_ror:8 row_mask:0xf bank_mask:0xf\n" \
    "s_nop 1\n" \
    "v_max_i32_dpp v240, v240, v240 row_ror:4 row_mask:0xf bank_mask:0xf\n" \
    "s_nop 1\n" \
    "v_max_i32_dpp v240, v240, v240 row_ror:2 row_mask:0xf bank_mask:0xf\n" \
    "s_nop 1\n" \
    "v_max_i32_dpp v240, v240, v240 row_ror:1 row_mask:0xf bank_mask:0xf\n" \
    "v_mov_b32 v243, v240\n" \
    "s_nop 1\n" \
    "v_permlane16_swap_b32 v240, v243\n" \
    "s_nop 1\n" \
    "v_max_i32 v240, v240, v243\n" \
    "v_mov_b32 v243, v240\n" \
    "s_nop 1\n" \
    "v_permlane32_swap_b32 v240, v243\n" \
    "s_nop 1\n" \
    "v_max_i32 v240, v240, v243\n" \
    "s_nop 1\n" \
    "v_readfirstlane_b32 s57, v240\n" \
    "s_nop 3\n" \
    "s_add_u32 s60, s60, s57\n" \
    "s_sub_u32 s57, s53, %[thr]\n" \
    "s_sub_i32 s57, s60, s57\n" \
    "s_cmp_gt_i32 s57, 0\n" \
    "s_cbranch_scc0 Msc_k_%=\n" \
    "s_sub_i32 s57, s60, s53\n" \
    "s_add_u32 s60, s57, s37\n" \
    "s_cmp_le_i32 s57, -3200\n" \
    "s_cselect_b32 s60, 1825, s60\n" \
    "s_cmp_le_i32 s57, -4100\n" \
    "s_cselect_b32 s60, 920, s60\n" \
    "s_add_u32 s53, s53, s60\n" \
    "s_mov_b32 s89, 190\n" \
    "Msc_k_%=:\n" \
    "Msc_n_%=:\n" \
    "v_readlane_b32 s57, v241, 28\n" \
    "s_nop 3\n" \
    "s_and_b32 s57, s57, 0xffff\n" \
    "s_add_u32 s53, s53, s57\n" \
    "s_add_u32 s53, s53, s89\n" \
    "s_add_u32 s37, s37, 400\n" \
    "s_min_u32 s37, s37, 2550\n" \
    "s_cmp_eq_u32 s89, 0\n" \
    "s_cselect_b32 s37, 950, s37\n" \
    "s_add_u32 s90, s90, 16384\n" \
    "s_add_u32 s40, s40, 32768\n" \
    "s_add_u32 s41, s41, 32768\n" \
    "s_add_u32 s94, s94, 229376\n" \
    "s_add_u32 s92, s93, 1\n" \
    "s_lshr_b32 s60, s67, s92\n" \
    "s_and_b32 s60, s60, 1\n" \
    "s_lshl_b32 s60, s60, 13\n" \
    "s_lshl_b32 s91, s92, 21\n" \
    "s_add_u32 s60, s60, s91\n" \
    "s_add_u32 s95, s95, 1\n" \
    "s_cmp_eq_u32 s95, 9\n" \
    "s_cselect_b32 s94, s60, s94\n" \
    "s_cselect_b32 s93, s92, s93\n" \
    "s_cselect_b32 s95, 0, s95\n" \
    "s_add_u32 s51, s94, %[vmoff]\n" \
    "s_add_u32 s48, s48, 1\n" \
    "s_cmp_ge_u32 s48, 64\n" \
    "s_cbranch_scc1 Mend_%=\n" \
    "s_branch Mstep_%=\n" \
    "Mfail_%=:\n" \
    "s_add_u32 s58, s58, 1\n" \
    "s_add_u32 s59, s59, 1\n" \
    "s_cmp_ge_u32 s58, s61\n" \
    "s_cbranch_scc1 Mfail_up_%=\n" \
    "s_cmp_le_u32 s58, 0\n" \
    "s_cbranch_scc1 Magain_%=\n" \
    "s_mov_b32 s66, 0\n" \
    "Mfail_p_%=:\n" \
    "buffer_load_dwordx2 v[240:241], %[stpo], %[rsH], s90 offen sc1\n" \
    "s_add_u32 s66, s66, 1\n" \
    "s_waitcnt vmcnt(0)\n" \
    "v_cmp_eq_u32 vcc, -1, v240\n" \
    "s_cbranch_vccz Mfail_ok_%=\n" \
    "s_sleep 3\n" \
    "s_cmp_lt_u32 s66, s61\n" \
    "s_cbranch_scc1 Mfail_p_%=\n" \
    "s_mov_b32 s61, 0\n" \
    "Mfail_ok_%=:\n" \
    "v_readfirstlane_b32 s60, v240\n" \
    "s_nop 3\n" \
    "v_subrev_u32 v240, s60, v240\n" \
    "s_nop 1\n" \
    "v_max_i32_dpp v240, v240, v240 row_ror:8 row_mask:0xf bank_mask:0xf\n" \
    "s_nop 1\n" \
    "v_max_i32_dpp v240, v240, v240 row_ror:4 row_mask:0xf bank_mask:0xf\n" \
    "s_nop 1\n" \
    "v_max_i32_dpp v240, v240, v240 row_ror:2 row_mask:0xf bank_mask:0xf\n" \
    "s_nop 1\n" \
    "v_max_i32_dpp v240, v240, v240 row_ror:1 row_mask:0xf bank_mask:0xf\n" \
    "v_mov_b32 v243, v240\n" \
    "s_nop 1\n" \
    "v_permlane16_swap_b32 v240, v243\n" \
    "s_nop 1\n" \
    "v_max_i32 v240, v240, v243\n" \
    "v_mov_b32 v243, v240\n" \
    "s_nop 1\n" \
    "v_permlane32_swap_b32 v240, v243\n" \
    "s_nop 1\n" \
    "v_max_i32 v240, v240, v243\n" \
    "s_nop 1\n" \
    "v_readfirstlane_b32 s57, v240\n" \
    "s_nop 3\n" \
    "s_add_u32 s60, s60, s57\n" \
    "s_sub_u32 s57, s53, %[thr]\n" \
    "s_sub_i32 s57, s60, s57\n" \
    "s_cmp_gt_i32 s57, 0\n" \
    "s_cbranch_scc0 Mfail_k_%=\n" \
    "s_sub_i32 s57, s60, s53\n" \
    "s_add_u32 s60, s57, s37\n" \
    "s_cmp_le_i32 s57, -3200\n" \
    "s_cselect_b32 s60, 1825, s60\n" \
    "s_cmp_le_i32 s57, -4100\n" \
    "s_cselect_b32 s60, 920, s60\n" \
    "s_add_u32 s53, s53, s60\n" \
    "s_mov_b32 s89, 190\n" \
    "Mfail_k_%=:\n" \
    "s_branch Mtry_%=\n" \
    "Mfail_up_%=:\n" \
    "s_mov_b32 s61, 0\n" \
    "s_branch Mfail_go_%=\n" \
    "Mend_%=:\n" \
    "s_waitcnt vmcnt(0) lgkmcnt(0)\n"

#define ASM_V \
    "s_nop 4\n" \
    "s_mov_b32 s48, 0\n" \
    "s_mov_b32 s49, 3\n" \
    "s_mov_b32 s59, 0\n" \
    "s_mov_b32 s61, 20000\n" \
    "s_mov_b32 s64, 0\n" \
    "s_mov_b32 s65, 0\n" \
    "v_mov_b32 v112, 0x3f803f80\n" \
    "v_mov_b32 v113, 0x3f803f80\n" \
    "v_mov_b32 v114, 0x3f803f80\n" \
    "v_mov_b32 v115, 0x3f803f80\n" \
    "v_mov_b32 v118, 0x7149f2ca\n" \
    "v_mov_b32 v121, 0x7fff0000\n" \
    "v_not_b32 v117, %[mlo]\n" \
    "s_mov_b32 s67, %[selm]\n" \
    "s_mov_b32 s37, 950\n" \
    "s_and_b32 s88, s67, 1\n" \
    "s_lshl_b32 s88, s88, 13\n" \
    "s_and_b32 s89, s67, 2\n" \
    "s_lshl_b32 s89, s89, 12\n" \
    "s_add_u32 s89, s89, 0x200000\n" \
    "s_add_u32 s90, s89, 0x100000\n" \
    "s_lshr_b32 s52, s48, 1\n" \
    "s_lshr_b32 s60, s67, 16\n" \
    "s_xor_b32 s60, s60, s52\n" \
    "s_and_b32 s60, s60, 1\n" \
    "s_lshl_b32 s60, s60, 13\n" \
    "s_lshl_b32 s52, s48, 20\n" \
    "s_add_u32 s52, s52, s60\n" \
    "s_add_u32 s52, s52, %[uboff]\n" \
    "s_add_u32 s45, s88, 32768\n" \
    "s_add_u32 s47, s88, 16384\n" \
    "s_mov_b32 s46, s89\n" \
    "s_mov_b32 s93, 2\n" \
    "s_mov_b32 s95, 0\n" \
    "s_bfe_u32 s94, s67, 0x10002\n" \
    "s_lshl_b32 s94, s94, 13\n" \
    "s_add_u32 s94, s94, 0x400000\n" \
    "s_add_u32 s51, s94, %[vmoff]\n" \
    "s_cmp_gt_u32 s64, s65\n" \
    "s_cbranch_scc1 Vf0_ok_%=\n" \
    "s_mov_b32 s66, 0\n" \
    "Vf0_poll_%=:\n" \
    "buffer_load_dword v240, %[flg], %[rsW], 0 offen sc1\n" \
    "s_waitcnt vmcnt(0)\n" \
    "s_nop 1\n" \
    "v_min_u32_dpp v240, v240, v240 row_ror:8 row_mask:0xf bank_mask:0xf\n" \
    "s_nop 1\n" \
    "v_min_u32_dpp v240, v240, v240 row_ror:4 row_mask:0xf bank_mask:0xf\n" \
    "s_nop 1\n" \
    "v_min_u32_dpp v240, v240, v240 row_ror:2 row_mask:0xf bank_mask:0xf\n" \
    "s_nop 1\n" \
    "v_min_u32_dpp v240, v240, v240 row_ror:1 row_mask:0xf bank_mask:0xf\n" \
    "s_nop 1\n" \
    "v_readfirstlane_b32 s64, v240\n" \
    "s_nop 3\n" \
    "s_cmp_gt_u32 s64, s65\n" \
    "s_cbranch_scc1 Vf0_ok_%=\n" \
    "s_sleep 4\n" \
    "s_add_u32 s66, s66, 1\n" \
    "s_cmp_lt_u32 s66, s61\n" \
    "s_cbranch_scc1 Vf0_poll_%=\n" \
    "s_mov_b32 s61, 0\n" \
    "Vf0_ok_%=:\n" \
    "s_waitcnt vmcnt(0)\n" \
    "s_barrier\n" \
    "s_memrealtime s[54:55]\n" \
    "s_waitcnt lgkmcnt(0)\n" \
    "s_lshl_b32 s54, s54, 3\n" \
    "s_or_b32 s54, s54, 1\n" \
    "v_mov_b32 v240, s54\n" \
    "buffer_store_dword v240, %[rdyw], %[rsW], 0 offen sc1\n" \
    "s_mov_b32 s66, 0\n" \
    "Vini_%=:\n" \
    "buffer_load_dword v240, %[rdy0], %[rsW], 0 offen sc1\n" \
    "buffer_load_dword v242, %[rdy0b], %[rsW], 0 offen sc1\n" \
    "s_add_u32 s66, s66, 1\n" \
    "s_waitcnt vmcnt(0)\n" \
    "v_min_u32 v241, v240, v242\n" \
    "v_cmp_eq_u32 vcc, 0, v241\n" \
    "s_cbranch_vccz Vini_ok_%=\n" \
    "s_sleep 3\n" \
    "s_cmp_lt_u32 s66, s61\n" \
    "s_cbranch_scc1 Vini_%=\n" \
    "s_mov_b32 s61, 0\n" \
    "Vini_ok_%=:\n" \
    "v_sub_u32 v241, v242, v240\n" \
    "v_max_i32 v241, 0, v241\n" \
    "v_add_u32 v240, v240, v241\n" \
    "s_nop 1\n" \
    "v_readfirstlane_b32 s60, v240\n" \
    "s_nop 3\n" \
    "v_subrev_u32 v240, s60, v240\n" \
    "s_nop 1\n" \
    "v_max_i32_dpp v240, v240, v240 row_ror:8 row_mask:0xf bank_mask:0xf\n" \
    "s_nop 1\n" \
    "v_max_i32_dpp v240, v240, v240 row_ror:4 row_mask:0xf bank_mask:0xf\n" \
    "s_nop 1\n" \
    "v_max_i32_dpp v240, v240, v240 row_ror:2 row_mask:0xf bank_mask:0xf\n" \
    "s_nop 1\n" \
    "v_max_i32_dpp v240, v240, v240 row_ror:1 row_mask:0xf bank_mask:0xf\n" \
    "v_mov_b32 v243, v240\n" \
    "s_nop 1\n" \
    "v_permlane16_swap_b32 v240, v243\n" \
    "s_nop 1\n" \
    "v_max_i32 v240, v240, v243\n" \
    "v_mov_b32 v243, v240\n" \
    "s_nop 1\n" \
    "v_permlane32_swap_b32 v240, v243\n" \
    "s_nop 1\n" \
    "v_max_i32 v240, v240, v243\n" \
    "s_nop 1\n" \
    "v_readfirstlane_b32 s57, v240\n" \
    "s_nop 3\n" \
    "s_add_u32 s60, s60, s57\n" \
    "s_add_u32 s53, s60, 700\n" \
    "buffer_load_dword v234, %[paro], %[rsH], s90 offen sc1\n" \
    "Vstep_%=:\n" \
    "s_cmp_eq_u32 s48, 0\n" \
    "s_cbranch_scc1 Vb0_%=\n" \
    "s_add_u32 s45, s45, 32768\n" \
    "s_add_u32 s47, s47, 32768\n" \
    "s_add_u32 s46, s46, 16384\n" \
    "s_add_u32 s94, s94, 229376\n" \
    "s_add_u32 s92, s93, 1\n" \
    "s_lshr_b32 s60, s67, s92\n" \
    "s_and_b32 s60, s60, 1\n" \
    "s_lshl_b32 s60, s60, 13\n" \
    "s_lshl_b32 s91, s92, 21\n" \
    "s_add_u32 s60, s60, s91\n" \
    "s_add_u32 s95, s95, 1\n" \
    "s_cmp_eq_u32 s95, 9\n" \
    "s_cselect_b32 s94, s60, s94\n" \
    "s_cselect_b32 s93, s92, s93\n" \
    "s_cselect_b32 s95, 0, s95\n" \
    "s_add_u32 s51, s94, %[vmoff]\n" \
    "s_add_u32 s49, s49, 4\n" \
    "s_lshr_b32 s52, s48, 1\n" \
    "s_lshr_b32 s60, s67, 16\n" \
    "s_xor_b32 s60, s60, s52\n" \
    "s_and_b32 s60, s60, 1\n" \
    "s_lshl_b32 s60, s60, 13\n" \
    "s_lshl_b32 s52, s48, 20\n" \
    "s_add_u32 s52, s52, s60\n" \
    "s_add_u32 s52, s52, %[uboff]\n" \
    "s_mov_b32 s65, s48\n" \
    "v_min_u32_dpp v239, v239, v239 row_ror:8 row_mask:0xf bank_mask:0xf\n" \
    "s_nop 1\n" \
    "v_min_u32_dpp v239, v239, v239 row_ror:4 row_mask:0xf bank_mask:0xf\n" \
    "s_nop 1\n" \
    "v_min_u32_dpp v239, v239, v239 row_ror:2 row_mask:0xf bank_mask:0xf\n" \
    "s_nop 1\n" \
    "v_min_u32_dpp v239, v239, v239 row_ror:1 row_mask:0xf bank_mask:0xf\n" \
    "s_nop 1\n" \
    "v_readfirstlane_b32 s60, v239\n" \
    "s_nop 3\n" \
    "s_max_u32 s64, s64, s60\n" \
    "s_cmp_gt_u32 s64, s65\n" \
    "s_cbranch_scc1 Vb0f_ok_%=\n" \
    "s_mov_b32 s66, 0\n" \
    "Vb0f_poll_%=:\n" \
    "buffer_load_dword v240, %[flg], %[rsW], 0 offen sc1\n" \
    "s_waitcnt vmcnt(0)\n" \
    "s_nop 1\n" \
    "v_min_u32_dpp v240, v240, v240 row_ror:8 row_mask:0xf bank_mask:0xf\n" \
    "s_nop 1\n" \
    "v_min_u32_dpp v240, v240, v240 row_ror:4 row_mask:0xf bank_mask:0xf\n" \
    "s_nop 1\n" \
    "v_min_u32_dpp v240, v240, v240 row_ror:2 row_mask:0xf bank_mask:0xf\n" \
    "s_nop 1\n" \
    "v_min_u32_dpp v240, v240, v240 row_ror:1 row_mask:0xf bank_mask:0xf\n" \
    "s_nop 1\n" \
    "v_readfirstlane_b32 s64, v240\n" \
    "s_nop 3\n" \
    "s_cmp_gt_u32 s64, s65\n" \
    "s_cbranch_scc1 Vb0f_ok_%=\n" \
    "s_sleep 4\n" \
    "s_add_u32 s66, s66, 1\n" \
    "s_cmp_lt_u32 s66, s61\n" \
    "s_cbranch_scc1 Vb0f_poll_%=\n" \
    "s_mov_b32 s61, 0\n" \
    "Vb0f_ok_%=:\n" \
    "Vb0_%=:\n" \
    "buffer_load_dwordx4 v[80:83], %[l16], %[rsW], s52 offen offset:0 sc1\n" \
    "buffer_load_dwordx4 v[84:87], %[l16], %[rsW], s52 offen offset:1024 sc1\n" \
    "buffer_load_dwordx4 v[88:91], %[l16], %[rsW], s52 offen offset:2048 sc1\n" \
    "buffer_load_dwordx4 v[92:95], %[l16], %[rsW], s52 offen offset:3072 sc1\n" \
    "s_mov_b32 s89, 0\n" \
    "s_sub_u32 s68, s53, %[thr]\n" \
    "s_add_u32 s69, s53, %[d1]\n" \
    "Vwc_%=:\n" \
    "s_memrealtime s[54:55]\n" \
    "s_waitcnt lgkmcnt(0)\n" \
    "s_lshl_b32 s54, s54, 3\n" \
    "s_sub_i32 s56, s69, s54\n" \
    "s_cmp_gt_i32 s56, 64\n" \
    "s_cbranch_scc1 Vwc_%=\n" \
    "s_sub_i32 s56, s56, 40\n" \
    "s_max_i32 s56, s56, 0\n" \
    "Vw_%=:\n" \
    "s_sub_u32 s56, s56, 9\n" \
    "s_cbranch_scc0 Vw_%=\n" \
    "buffer_load_dwordx2 v[240:241], %[stpo], %[rsH], s90 offen sc1\n" \
    "s_add_u32 s60, s48, 3\n" \
    "s_cmp_gt_u32 s64, s60\n" \
    "s_cselect_b64 vcc, -1, 0\n" \
    "v_cndmask_b32 v238, %[flg], v121, vcc\n" \
    "buffer_load_dword v239, v238, %[rsW], 0 offen sc1\n" \
    "s_waitcnt vmcnt(6)\n" \
    "s_mov_b32 s66, 0\n" \
    "Vpp_%=:\n" \
    "v_readfirstlane_b32 s44, v234\n" \
    "s_add_u32 s66, s66, 1\n" \
    "s_cmp_ge_u32 s66, s61\n" \
    "s_cbranch_scc1 Vpk_%=\n" \
    "s_cmp_eq_u32 s44, -1\n" \
    "s_cbranch_scc0 Vpk_%=\n" \
    "buffer_load_dword v234, %[paro], %[rsH], s90 offen sc1\n" \
    "s_waitcnt vmcnt(0)\n" \
    "s_branch Vpp_%=\n" \
    "Vpk_%=:\n" \
    "s_min_u32 s44, s44, 0x7ffffff\n" \
    "s_lshr_b32 s44, s44, 16\n" \
    "s_mov_b32 s58, 0\n" \
    "s_add_u32 s69, s53, s44\n" \
    "Vw2c_%=:\n" \
    "s_memrealtime s[54:55]\n" \
    "s_waitcnt lgkmcnt(0)\n" \
    "s_lshl_b32 s54, s54, 3\n" \
    "s_sub_i32 s56, s69, s54\n" \
    "s_cmp_gt_i32 s56, 170\n" \
    "s_cbranch_scc1 Vw2c_%=\n" \
    "s_cmp_lt_i32 s56, 125\n" \
    "s_cbranch_scc1 Vw2o_%=\n" \
    "s_memrealtime s[70:71]\n" \
    "s_nop 1\n" \
    "s_memrealtime s[72:73]\n" \
    "s_nop 1\n" \
    "s_memrealtime s[74:75]\n" \
    "s_nop 1\n" \
    "s_memrealtime s[76:77]\n" \
    "s_nop 1\n" \
    "s_memrealtime s[78:79]\n" \
    "s_nop 1\n" \
    "s_memrealtime s[80:81]\n" \
    "s_nop 1\n" \
    "s_memrealtime s[82:83]\n" \
    "s_nop 1\n" \
    "s_memrealtime s[84:85]\n" \
    "s_nop 1\n" \
    "s_memrealtime s[86:87]\n" \
    "s_mov_b32 s56, 0\n" \
    "s_waitcnt lgkmcnt(0)\n" \
    "s_cmp_eq_u32 s70, s72\n" \
    "s_addc_u32 s56, s56, 0\n" \
    "s_cmp_eq_u32 s70, s74\n" \
    "s_addc_u32 s56, s56, 0\n" \
    "s_cmp_eq_u32 s70, s76\n" \
    "s_addc_u32 s56, s56, 0\n" \
    "s_cmp_eq_u32 s70, s78\n" \
    "s_addc_u32 s56, s56, 0\n" \
    "s_cmp_eq_u32 s70, s80\n" \
    "s_addc_u32 s56, s56, 0\n" \
    "s_cmp_eq_u32 s70, s82\n" \
    "s_addc_u32 s56, s56, 0\n" \
    "s_cmp_eq_u32 s70, s84\n" \
    "s_addc_u32 s56, s56, 0\n" \
    "s_cmp_eq_u32 s70, s86\n" \
    "s_addc_u32 s56, s56, 0\n" \
    "s_lshl_b32 s70, s70, 3\n" \
    "s_sub_i32 s70, s69, s70\n" \
    "s_mul_i32 s70, s70, 94\n" \
    "s_mul_i32 s56, s56, 384\n" \
    "s_add_i32 s70, s70, s56\n" \
    "s_sub_i32 s70, s70, 10688\n" \
    "s_ashr_i32 s70, s70, 8\n" \
    "s_max_i32 s70, s70, 0\n" \
    "s_min_i32 s70, s70, 40\n" \
    "s_lshl_b32 s70, s70, 2\n" \
    "s_getpc_b64 s[54:55]\n" \
    "Vw2b_%=:\n" \
    "s_add_u32 s54, s54, Vw2d_%=-Vw2b_%=\n" \
    "s_addc_u32 s55, s55, 0\n" \
    "s_sub_u32 s54, s54, s70\n" \
    "s_subb_u32 s55, s55, 0\n" \
    "s_setpc_b64 s[54:55]\n" \
    "Vw2o_%=:\n" \
    "s_sub_i32 s56, s56, 40\n" \
    "s_max_i32 s56, s56, 0\n" \
    "Vw2_%=:\n" \
    "s_sub_u32 s56, s56, 9\n" \
    "s_cbranch_scc0 Vw2_%=\n" \
    "s_branch Vw2d_%=\n" \
    "s_nop 1\n" \
    "s_nop 1\n" \
    "s_nop 1\n" \
    "s_nop 1\n" \
    "s_nop 1\n" \
    "s_nop 1\n" \
    "s_nop 1\n" \
    "s_nop 1\n" \
    "s_nop 1\n" \
    "s_nop 1\n" \
    "s_nop 1\n" \
    "s_nop 1\n" \
    "s_nop 1\n" \
    "s_nop 1\n" \
    "s_nop 1\n" \
    "s_nop 1\n" \
    "s_nop 1\n" \
    "s_nop 1\n" \
    "s_nop 1\n" \
    "s_nop 1\n" \
    "s_nop 1\n" \
    "s_nop 1\n" \
    "s_nop 1\n" \
    "s_nop 1\n" \
    "s_nop 1\n" \
    "s_nop 1\n" \
    "s_nop 1\n" \
    "s_nop 1\n" \
    "s_nop 1\n" \
    "s_nop 1\n" \
    "s_nop 1\n" \
    "s_nop 1\n" \
    "s_nop 1\n" \
    "s_nop 1\n" \
    "s_nop 1\n" \
    "s_nop 1\n" \
    "s_nop 1\n" \
    "s_nop 1\n" \
    "s_nop 1\n" \
    "s_nop 1\n" \
    "Vw2d_%=:\n" \
    "buffer_load_dwordx4 v[96:99], %[eo], %[rsH], s46 offen sc1\n" \
    "buffer_load_dwordx4 v[100:103], %[eo], %[rsH], s46 offen offset:512 sc1\n" \
    "s_memtime s[96:97]\n" \
    "s_memrealtime s[62:63]\n" \
    "s_nop 15\n" \
    "s_nop 15\n" \
    "s_nop 15\n" \
    "s_nop 1\n" \
    "buffer_load_dwordx4 v[104:107], %[so], %[rsH], s47 offen sc1\n" \
    "buffer_load_dwordx4 v[108:111], %[so], %[rsH], s47 offen offset:256 sc1\n" \
    "s_nop 15\n" \
    "buffer_load_dwordx4 v[76:79], %[l16], %[rsH], s51 offen sc1\n" \
    "s_waitcnt vmcnt(3)\n" \
    "Vtry2_%=:\n" \
    "v_mfma_f32_16x16x32_bf16 v[144:147], v[112:115], v[96:99], 0\n" \
    "v_mfma_f32_16x16x32_bf16 v[148:151], v[112:115], v[100:103], 0\n" \
    "v_mfma_f32_16x16x32_bf16 v[128:131], v[80:83], v[96:99], 0\n" \
    "v_mfma_f32_16x16x32_bf16 v[132:135], v[84:87], v[100:103], 0\n" \
    "v_mfma_f32_16x16x32_bf16 v[136:139], v[88:91], v[96:99], 0\n" \
    "v_mfma_f32_16x16x32_bf16 v[140:143], v[92:95], v[100:103], 0\n" \
    "s_nop 3\n" \
    "v_mov_b32_dpp v144, v148 quad_perm:[0,1,2,3] row_mask:0xc bank_mask:0xf\n" \
    "v_mul_f32 v152, 0.5, v144\n" \
    "s_nop 1\n" \
    "v_mov_b32_dpp v128, v136 quad_perm:[0,1,2,3] row_mask:0xf bank_mask:0xc\n" \
    "v_mov_b32_dpp v129, v137 quad_perm:[0,1,2,3] row_mask:0xf bank_mask:0xc\n" \
    "v_mov_b32_dpp v130, v138 quad_perm:[0,1,2,3] row_mask:0xf bank_mask:0xc\n" \
    "v_mov_b32_dpp v131, v139 quad_perm:[0,1,2,3] row_mask:0xf bank_mask:0xc\n" \
    "v_mov_b32_dpp v132, v140 quad_perm:[0,1,2,3] row_mask:0xf bank_mask:0xc\n" \
    "v_mov_b32_dpp v133, v141 quad_perm:[0,1,2,3] row_mask:0xf bank_mask:0xc\n" \
    "v_mov_b32_dpp v134, v142 quad_perm:[0,1,2,3] row_mask:0xf bank_mask:0xc\n" \
    "v_mov_b32_dpp v135, v143 quad_perm:[0,1,2,3] row_mask:0xf bank_mask:0xc\n" \
    "s_waitcnt vmcnt(1)\n" \
    "v_pk_add_f32 v[156:157], v[104:105], v[108:109]\n" \
    "v_pk_add_f32 v[158:159], v[106:107], v[110:111]\n" \
    "Vexp_%=:\n" \
    "v_exp_f32 v160, v156\n" \
    "v_exp_f32 v161, v157\n" \
    "v_exp_f32 v162, v158\n" \
    "v_exp_f32 v163, v159\n" \
    "v_add_f32 v164, v160, v161\n" \
    "v_add_f32 v165, v162, v152\n" \
    "v_add_f32 v166, v165, v163\n" \
    "v_add_f32 v165, v164, v166\n" \
    "v_add_f32 v164, v164, v166\n" \
    "s_nop 1\n" \
    "v_permlane16_swap_b32 v164, v165\n" \
    "v_add_f32 v164, v164, v165\n" \
    "v_rcp_f32 v166, v164\n" \
    "v_cmp_nle_f32 vcc, v164, v118\n" \
    "v_pk_mul_f32 v[160:161], v[160:161], v[166:167] op_sel_hi:[1,0]\n" \
    "v_pk_mul_f32 v[162:163], v[162:163], v[166:167] op_sel_hi:[1,0]\n" \
    "v_cvt_pk_bf16_f32 v160, v160, v161\n" \
    "v_cvt_pk_bf16_f32 v161, v162, v163\n" \
    "v_and_b32 v168, v160, %[mlo]\n" \
    "v_and_b32 v169, v161, %[mlo]\n" \
    "v_and_b32 v170, v160, v117\n" \
    "v_and_b32 v171, v161, v117\n" \
    "v_mov_b32 v164, v166\n" \
    "s_waitcnt vmcnt(0)\n" \
    "v_mfma_f32_16x16x32_bf16 v[172:175], v[76:79], v[168:171], 0\n" \
    "v_cmp_eq_u32 s[38:39], -1, v79\n" \
    "v_permlane32_swap_b32 v166, v164\n" \
    "s_or_b64 vcc, vcc, s[38:39]\n" \
    "s_cbranch_vccnz Vslow_%=\n" \
    "Vslow_go_%=:\n" \
    "v_pk_mul_f32 v[176:177], v[132:133], v[164:165] op_sel_hi:[1,0]\n" \
    "v_pk_mul_f32 v[178:179], v[134:135], v[164:165] op_sel_hi:[1,0]\n" \
    "v_pk_fma_f32 v[176:177], v[128:129], v[166:167], v[176:177] op_sel_hi:[1,0,1]\n" \
    "v_pk_fma_f32 v[178:179], v[130:131], v[166:167], v[178:179] op_sel_hi:[1,0,1]\n" \
    "v_pk_add_f32 v[176:177], v[176:177], v[172:173]\n" \
    "v_pk_add_f32 v[178:179], v[178:179], v[174:175]\n" \
    "s_nop 0\n" \
    "v_add_f32_dpp v180, v176, v176 row_ror:8 row_mask:0xf bank_mask:0x3\n" \
    "v_add_f32_dpp v181, v177, v177 row_ror:8 row_mask:0xf bank_mask:0x3\n" \
    "v_add_f32_dpp v180, v178, v178 row_ror:8 row_mask:0xf bank_mask:0xc\n" \
    "v_add_f32_dpp v181, v179, v179 row_ror:8 row_mask:0xf bank_mask:0xc\n" \
    "ds_write_b64 %[aO], v[180:181]\n" \
    "ds_add_rtn_u32 v188, %[cnt], %[one16]\n" \
    "ds_read_b64 v[192:193], %[rdO] offset:0\n" \
    "ds_read_b64 v[194:195], %[rdO] offset:512\n" \
    "ds_read_b64 v[196:197], %[rdO] offset:1024\n" \
    "ds_read_b64 v[198:199], %[rdO] offset:1536\n" \
    "s_waitcnt lgkmcnt(0)\n" \
    "v_pk_add_f32 v[192:193], v[192:193], v[194:195]\n" \
    "v_pk_add_f32 v[196:197], v[196:197], v[198:199]\n" \
    "v_cmp_eq_u32 vcc, s49, v188\n" \
    "v_pk_add_f32 v[192:193], v[192:193], v[196:197]\n" \
    "v_cvt_pk_bf16_f32 v208, v192, v193\n" \
    "v_cvt_pk_bf16_f32 v209, v192, v193\n" \
    "s_cbranch_vccz Vnp_%=\n" \
    "v_cndmask_b32 v122, v121, %[pubM], vcc\n" \
    "v_permlane16_swap_b32 v208, v209\n" \
    "v_mov_b32 v210, v208\n" \
    "v_mov_b32 v211, v209\n" \
    "s_nop 0\n" \
    "v_permlane32_swap_b32 v208, v210\n" \
    "v_permlane32_swap_b32 v209, v211\n" \
    "buffer_store_dwordx4 v[208:211], v122, %[rsH], s45 offen sc1\n" \
    "s_memtime s[98:99]\n" \
    "s_add_u32 s60, s48, 2\n" \
    "v_mov_b32 v243, s60\n" \
    "v_cndmask_b32 v122, v121, %[prog], vcc\n" \
    "s_add_u32 s63, s53, s44\n" \
    "s_waitcnt lgkmcnt(0)\n" \
    "s_sub_u32 s98, s98, s96\n" \
    "s_mul_i32 s98, s98, 87\n" \
    "s_lshr_b32 s98, s98, 8\n" \
    "s_lshl_b32 s57, s62, 3\n" \
    "s_sub_i32 s57, s57, s63\n" \
    "s_max_i32 s57, s57, 0\n" \
    "s_add_u32 s57, s57, s63\n" \
    "s_add_u32 s57, s57, s98\n" \
    "s_and_b32 s57, s57, -2\n" \
    "v_mov_b32 v242, s57\n" \
    "v_cndmask_b32 v123, v121, %[rdyv], vcc\n" \
    "s_add_u32 s60, s90, 16384\n" \
    "buffer_store_dword v242, v123, %[rsH], s60 offen sc1\n" \
    "s_lshl_b32 s60, s48, 7\n" \
    "buffer_store_dword v243, v122, %[rsW], s60 offen sc1\n" \
    "s_branch Vnq_%=\n" \
    "Vnp_%=:\n" \
    "Vnq_%=:\n" \
    "s_add_u32 s48, s48, 1\n" \
    "s_cmp_ge_u32 s48, 63\n" \
    "s_cbranch_scc1 Vend_%=\n" \
    "v_subrev_u32 v237, s68, v240\n" \
    "v_cmp_eq_u32 s[38:39], -1, v240\n" \
    "v_cmp_lt_i32 vcc, 0, v237\n" \
    "s_or_b64 vcc, vcc, s[38:39]\n" \
    "s_cbranch_vccz Vsc_n_%=\n" \
    "s_mov_b32 s66, 0\n" \
    "Vsc_p_%=:\n" \
    "buffer_load_dwordx2 v[240:241], %[stpo], %[rsH], s90 offen sc1\n" \
    "s_add_u32 s66, s66, 1\n" \
    "s_waitcnt vmcnt(0)\n" \
    "v_cmp_eq_u32 vcc, -1, v240\n" \
    "s_cbranch_vccz Vsc_ok_%=\n" \
    "s_sleep 3\n" \
    "s_cmp_lt_u32 s66, s61\n" \
    "s_cbranch_scc1 Vsc_p_%=\n" \
    "s_mov_b32 s61, 0\n" \
    "Vsc_ok_%=:\n" \
    "v_readfirstlane_b32 s60, v240\n" \
    "s_nop 3\n" \
    "v_subrev_u32 v240, s60, v240\n" \
    "s_nop 1\n" \
    "v_max_i32_dpp v240, v240, v240 row_ror:8 row_mask:0xf bank_mask:0xf\n" \
    "s_nop 1\n" \
    "v_max_i32_dpp v240, v240, v240 row_ror:4 row_mask:0xf bank_mask:0xf\n" \
    "s_nop 1\n" \
    "v_max_i32_dpp v240, v240, v240 row_ror:2 row_mask:0xf bank_mask:0xf\n" \
    "s_nop 1\n" \
    "v_max_i32_dpp v240, v240, v240 row_ror:1 row_mask:0xf bank_mask:0xf\n" \
    "v_mov_b32 v243, v240\n" \
    "s_nop 1\n" \
    "v_permlane16_swap_b32 v240, v243\n" \
    "s_nop 1\n" \
    "v_max_i32 v240, v240, v243\n" \
    "v_mov_b32 v243, v240\n" \
    "s_nop 1\n" \
    "v_permlane32_swap_b32 v240, v243\n" \
    "s_nop 1\n" \
    "v_max_i32 v240, v240, v243\n" \
    "s_nop 1\n" \
    "v_readfirstlane_b32 s57, v240\n" \
    "s_nop 3\n" \
    "s_add_u32 s60, s60, s57\n" \
    "s_sub_u32 s57, s53, %[thr]\n" \
    "s_sub_i32 s57, s60, s57\n" \
    "s_cmp_gt_i32 s57, 0\n" \
    "s_cbranch_scc0 Vsc_k_%=\n" \
    "s_sub_i32 s57, s60, s53\n" \
    "s_add_u32 s60, s57, s37\n" \
    "s_cmp_le_i32 s57, -3200\n" \
    "s_cselect_b32 s60, 1825, s60\n" \
    "s_cmp_le_i32 s57, -4100\n" \
    "s_cselect_b32 s60, 920, s60\n" \
    "s_add_u32 s53, s53, s60\n" \
    "s_mov_b32 s89, 190\n" \
    "Vsc_k_%=:\n" \
    "Vsc_n_%=:\n" \
    "v_readlane_b32 s57, v241, 28\n" \
    "s_nop 3\n" \
    "s_and_b32 s57, s57, 0xffff\n" \
    "s_add_u32 s53, s53, s57\n" \
    "s_add_u32 s53, s53, s89\n" \
    "s_add_u32 s37, s37, 400\n" \
    "s_min_u32 s37, s37, 2550\n" \
    "s_cmp_eq_u32 s89, 0\n" \
    "s_cselect_b32 s37, 950, s37\n" \
    "s_add_u32 s90, s90, 16384\n" \
    "buffer_load_dword v234, %[paro], %[rsH], s90 offen sc1\n" \
    "s_branch Vstep_%=\n" \
    "Vslow_%=:\n" \
    "v_max3_u32 v244, v76, v77, v78\n" \
    "v_max3_u32 v244, v244, v79, v79\n" \
    "v_max3_u32 v244, v244, v96, v97\n" \
    "v_max3_u32 v244, v244, v98, v99\n" \
    "v_max3_u32 v244, v244, v100, v101\n" \
    "v_max3_u32 v244, v244, v102, v103\n" \
    "v_max3_u32 v244, v244, v104, v105\n" \
    "v_max3_u32 v244, v244, v106, v107\n" \
    "v_max3_u32 v244, v244, v108, v109\n" \
    "v_max3_u32 v244, v244, v110, v111\n" \
    "v_cmp_eq_u32 vcc, -1, v244\n" \
    "s_cbranch_vccz Vsafe_%=\n" \
    "s_add_u32 s58, s58, 1\n" \
    "s_cmp_ge_u32 s58, s61\n" \
    "s_cbranch_scc1 Vgiveup_%=\n" \
    "v_subrev_u32 v237, s68, v240\n" \
    "v_cmp_eq_u32 s[38:39], -1, v240\n" \
    "v_cmp_lt_i32 vcc, 0, v237\n" \
    "s_or_b64 vcc, vcc, s[38:39]\n" \
    "s_cbranch_vccz Vre_%=\n" \
    "s_mov_b32 s66, 0\n" \
    "Vrs_p_%=:\n" \
    "buffer_load_dwordx2 v[240:241], %[stpo], %[rsH], s90 offen sc1\n" \
    "s_add_u32 s66, s66, 1\n" \
    "s_waitcnt vmcnt(0)\n" \
    "v_cmp_eq_u32 vcc, -1, v240\n" \
    "s_cbranch_vccz Vrs_ok_%=\n" \
    "s_sleep 3\n" \
    "s_cmp_lt_u32 s66, s61\n" \
    "s_cbranch_scc1 Vrs_p_%=\n" \
    "s_mov_b32 s61, 0\n" \
    "Vrs_ok_%=:\n" \
    "v_readfirstlane_b32 s60, v240\n" \
    "s_nop 3\n" \
    "v_subrev_u32 v240, s60, v240\n" \
    "s_nop 1\n" \
    "v_max_i32_dpp v240, v240, v240 row_ror:8 row_mask:0xf bank_mask:0xf\n" \
    "s_nop 1\n" \
    "v_max_i32_dpp v240, v240, v240 row_ror:4 row_mask:0xf bank_mask:0xf\n" \
    "s_nop 1\n" \
    "v_max_i32_dpp v240, v240, v240 row_ror:2 row_mask:0xf bank_mask:0xf\n" \
    "s_nop 1\n" \
    "v_max_i32_dpp v240, v240, v240 row_ror:1 row_mask:0xf bank_mask:0xf\n" \
    "v_mov_b32 v243, v240\n" \
    "s_nop 1\n" \
    "v_permlane16_swap_b32 v240, v243\n" \
    "s_nop 1\n" \
    "v_max_i32 v240, v240, v243\n" \
    "v_mov_b32 v243, v240\n" \
    "s_nop 1\n" \
    "v_permlane32_swap_b32 v240, v243\n" \
    "s_nop 1\n" \
    "v_max_i32 v240, v240, v243\n" \
    "s_nop 1\n" \
    "v_readfirstlane_b32 s57, v240\n" \
    "s_nop 3\n" \
    "s_add_u32 s60, s60, s57\n" \
    "s_sub_u32 s57, s53, %[thr]\n" \
    "s_sub_i32 s57, s60, s57\n" \
    "s_cmp_gt_i32 s57, 0\n" \
    "s_cbranch_scc0 Vrs_k_%=\n" \
    "s_sub_i32 s57, s60, s53\n" \
    "s_add_u32 s60, s57, s37\n" \
    "s_cmp_le_i32 s57, -3200\n" \
    "s_cselect_b32 s60, 1825, s60\n" \
    "s_cmp_le_i32 s57, -4100\n" \
    "s_cselect_b32 s60, 920, s60\n" \
    "s_add_u32 s53, s53, s60\n" \
    "s_mov_b32 s89, 190\n" \
    "Vrs_k_%=:\n" \
    "v_mov_b32 v240, s53\n" \
    "v_subrev_u32 v240, %[thr], v240\n" \
    "s_sub_u32 s68, s53, %[thr]\n" \
    "s_add_u32 s57, s44, 145\n" \
    "s_add_u32 s69, s53, s57\n" \
    "Vw5c_%=:\n" \
    "s_memrealtime s[54:55]\n" \
    "s_waitcnt lgkmcnt(0)\n" \
    "s_lshl_b32 s54, s54, 3\n" \
    "s_sub_i32 s56, s69, s54\n" \
    "s_cmp_gt_i32 s56, 64\n" \
    "s_cbranch_scc1 Vw5c_%=\n" \
    "s_sub_i32 s56, s56, 40\n" \
    "s_max_i32 s56, s56, 0\n" \
    "Vw5_%=:\n" \
    "s_sub_u32 s56, s56, 9\n" \
    "s_cbranch_scc0 Vw5_%=\n" \
    "s_memtime s[96:97]\n" \
    "s_memrealtime s[62:63]\n" \
    "Vre_%=:\n" \
    "buffer_load_dwordx4 v[96:99], %[eo], %[rsH], s46 offen sc1\n" \
    "buffer_load_dwordx4 v[100:103], %[eo], %[rsH], s46 offen offset:512 sc1\n" \
    "buffer_load_dwordx4 v[104:107], %[so], %[rsH], s47 offen sc1\n" \
    "buffer_load_dwordx4 v[108:111], %[so], %[rsH], s47 offen offset:256 sc1\n" \
    "buffer_load_dwordx4 v[76:79], %[l16], %[rsH], s51 offen sc1\n" \
    "s_waitcnt vmcnt(3)\n" \
    "s_branch Vtry2_%=\n" \
    "Vgiveup_%=:\n" \
    "s_mov_b32 s61, 0\n" \
    "s_branch Vslow_go_%=\n" \
    "Vsafe_%=:\n" \
    "s_bitcmp1_b32 s58, 30\n" \
    "s_cbranch_scc1 Vslow_go_%=\n" \
    "s_bitset1_b32 s58, 30\n" \
    "v_max3_f32 v245, v156, v157, v158\n" \
    "v_max3_f32 v245, v245, v159, 0\n" \
    "v_mov_b32 v246, v245\n" \
    "s_nop 1\n" \
    "v_permlane16_swap_b32 v245, v246\n" \
    "s_nop 1\n" \
    "v_max_f32 v245, v245, v246\n" \
    "v_sub_f32 v156, v156, v245\n" \
    "v_sub_f32 v157, v157, v245\n" \
    "v_sub_f32 v158, v158, v245\n" \
    "v_sub_f32 v159, v159, v245\n" \
    "v_exp_f32 v245, -v245\n" \
    "s_nop 0\n" \
    "v_mul_f32 v152, v152, v245\n" \
    "v_mov_b32 v246, v245\n" \
    "s_nop 1\n" \
    "v_permlane32_swap_b32 v245, v246\n" \
    "s_nop 1\n" \
    "v_mul_f32 v128, v128, v245\n" \
    "v_mul_f32 v129, v129, v245\n" \
    "v_mul_f32 v130, v130, v245\n" \
    "v_mul_f32 v131, v131, v245\n" \
    "v_mul_f32 v132, v132, v246\n" \
    "v_mul_f32 v133, v133, v246\n" \
    "v_mul_f32 v134, v134, v246\n" \
    "v_mul_f32 v135, v135, v246\n" \
    "s_branch Vexp_%=\n" \
    "Vend_%=:\n" \
    "s_waitcnt vmcnt(0) lgkmcnt(0)\n"

#define ASM_CLOB \
    "v32", "v33", "v34", "v35", "v36", "v37", "v38", "v39", "v40", "v41", "v42", "v43", "v44", "v45", "v46", "v47", "v48", "v49", "v50", "v51", "v52", "v53", "v54", "v55", \
    "v56", "v57", "v58", "v59", "v60", "v61", "v62", "v63", "v64", "v65", "v66", "v67", "v68", "v69", "v70", "v71", "v72", "v73", "v74", "v75", "v76", "v77", "v78", "v79", \
    "v80", "v81", "v82", "v83", "v84", "v85", "v86", "v87", "v88", "v89", "v90", "v91", "v92", "v93", "v94", "v95", "v96", "v97", "v98", "v99", "v100", "v101", "v102", "v103", \
    "v104", "v105", "v106", "v107", "v108", "v109", "v110", "v111", "v112", "v113", "v114", "v115", "v116", "v117", "v118", "v119", "v120", "v121", "v122", "v123", "v124", "v125", "v126", "v127", \
    "v128", "v129", "v130", "v131", "v132", "v133", "v134", "v135", "v136", "v137", "v138", "v139", "v140", "v141", "v142", "v143", "v144", "v145", "v146", "v147", "v148", "v149", "v150", "v151", \
    "v152", "v153", "v154", "v155", "v156", "v157", "v158", "v159", "v160", "v161", "v162", "v163", "v164", "v165", "v166", "v167", "v168", "v169", "v170", "v171", "v172", "v173", "v174", "v175", \
    "v176", "v177", "v178", "v179", "v180", "v181", "v182", "v183", "v184", "v185", "v186", "v187", "v188", "v189", "v190", "v191", "v192", "v193", "v194", "v195", "v196", "v197", "v198", "v199", \
    "v200", "v201", "v202", "v203", "v204", "v205", "v206", "v207", "v208", "v209", "v210", "v211", "v212", "v213", "v214", "v215", "v216", "v217", "v218", "v219", "v220", "v221", "v222", "v223", \
    "v224", "v225", "v226", "v227", "v228", "v229", "v230", "v231", "v232", "v233", "v234", "v235", "v236", "v237", "v238", "v239", "v240", "v241", "v242", "v243", "v244", "v245", "v246", "v247", \
    "v248", "v249", "v250", "v251", "v252", "v253", "v254", "v255", "a0", "a1", "a2", "a3", "a4", "a5", "a6", "a7", "a8", "a9", "a10", "a11", "a12", "a13", "a14", "a15", \
    "a16", "a17", "a18", "a19", "a20", "a21", "a22", "a23", "a24", "a25", "a26", "a27", "a28", "a29", "a30", "a31", "a32", "a33", "a34", "a35", "a36", "a37", "a38", "a39", \
    "a40", "a41", "a42", "a43", "a44", "a45", "a46", "a47", "a48", "a49", "a50", "a51", "a52", "a53", "a54", "a55", "a56", "a57", "a58", "a59", "a60", "a61", "a62", "a63", \
    "a64", "a65", "a66", "a67", "a68", "a69", "a70", "a71", "a72", "a73", "a74", "a75", "a76", "a77", "a78", "a79", "a80", "a81", "a82", "a83", "a84", "a85", "a86", "a87", \
    "a88", "a89", "a90", "a91", "a92", "a93", "a94", "a95", "a96", "a97", "a98", "a99", "a100", "a101", "a102", "a103", "a104", "a105", "a106", "a107", "a108", "a109", "a110", "a111", \
    "a112", "a113", "a114", "a115", "a116", "a117", "a118", "a119", "a120", "a121", "a122", "a123", "a124", "a125", "a126", "a127", "a128", "a129", "a130", "a131", "a132", "a133", "a134", "a135", \
    "a136", "a137", "a138", "a139", "a140", "a141", "a142", "a143", "a144", "a145", "a146", "a147", "a148", "a149", "a150", "a151", "a152", "a153", "a154", "a155", "a156", "a157", "a158", "a159", \
    "a160", "a161", "a162", "a163", "a164", "a165", "a166", "a167", "a168", "a169", "a170", "a171", "a172", "a173", "a174", "a175", "a176", "a177", "a178", "a179", "a180", "a181", "a182", "a183", \
    "a184", "a185", "a186", "a187", "a188", "a189", "a190", "a191", "a192", "a193", "a194", "a195", "a196", "a197", "a198", "a199", "a200", "a201", "a202", "a203", "a204", "a205", "a206", "a207", \
    "a208", "a209", "a210", "a211", "a212", "a213", "a214", "a215", "a216", "a217", "a218", "a219", "a220", "a221", "a222", "a223", "s37", "s38", "s39", "s40", "s41", "s42", "s43", "s44", \
    "s45", "s46", "s47", "s48", "s49", "s50", "s51", "s52", "s53", "s54", "s55", "s56", "s57", "s58", "s59", "s60", "s61", "s62", "s63", "s64", "s65", "s66", "s67", "s68", \
    "s69", "s70", "s71", "s72", "s73", "s74", "s75", "s76", "s77", "s78", "s79", "s80", "s81", "s82", "s83", "s84", "s85", "s86", "s87", "s88", "s89", "s90", "s91", "s92", \
    "s93", "s94", "s95", "s96", "s97", "s98", "s99", "vcc", "scc", "memory"

#define DBGOP

#define ASM_GK \
    "s_nop 4\n" \
    "s_add_u32 s44, %[ld0], 0\n" \
    "s_add_u32 s43, %[so0], 0\n" \
    "s_mov_b32 m0, s44\n" \
    "s_nop 0\n" \
    "buffer_load_dwordx4 %[l16], %[rs], s43 offen offset:0 lds\n" \
    "buffer_load_dwordx4 %[l16], %[rs], s43 offen offset:1024 lds\n" \
    "buffer_load_dwordx4 %[l16], %[rs], s43 offen offset:2048 lds\n" \
    "buffer_load_dwordx4 %[l16], %[rs], s43 offen offset:3072 lds\n" \
    "s_add_u32 s44, %[ld0], 4096\n" \
    "s_add_u32 s43, %[so0], 4096\n" \
    "s_mov_b32 m0, s44\n" \
    "s_nop 0\n" \
    "buffer_load_dwordx4 %[l16], %[rs], s43 offen offset:0 lds\n" \
    "buffer_load_dwordx4 %[l16], %[rs], s43 offen offset:1024 lds\n" \
    "buffer_load_dwordx4 %[l16], %[rs], s43 offen offset:2048 lds\n" \
    "s_add_u32 s44, %[ld0], 28672\n" \
    "s_add_u32 s43, %[so0], 28672\n" \
    "s_mov_b32 m0, s44\n" \
    "s_nop 0\n" \
    "buffer_load_dwordx4 %[l16], %[rs], s43 offen offset:0 lds\n" \
    "buffer_load_dwordx4 %[l16], %[rs], s43 offen offset:1024 lds\n" \
    "buffer_load_dwordx4 %[l16], %[rs], s43 offen offset:2048 lds\n" \
    "buffer_load_dwordx4 %[l16], %[rs], s43 offen offset:3072 lds\n" \
    "s_add_u32 s44, %[ld0], 32768\n" \
    "s_add_u32 s43, %[so0], 32768\n" \
    "s_mov_b32 m0, s44\n" \
    "s_nop 0\n" \
    "buffer_load_dwordx4 %[l16], %[rs], s43 offen offset:0 lds\n" \
    "buffer_load_dwordx4 %[l16], %[rs], s43 offen offset:1024 lds\n" \
    "buffer_load_dwordx4 %[l16], %[rs], s43 offen offset:2048 lds\n" \
    "s_add_u32 s44, %[ld0], 57344\n" \
    "s_add_u32 s43, %[so0], 57344\n" \
    "s_mov_b32 m0, s44\n" \
    "s_nop 0\n" \
    "buffer_load_dwordx4 %[l16], %[rs], s43 offen offset:0 lds\n" \
    "buffer_load_dwordx4 %[l16], %[rs], s43 offen offset:1024 lds\n" \
    "buffer_load_dwordx4 %[l16], %[rs], s43 offen offset:2048 lds\n" \
    "buffer_load_dwordx4 %[l16], %[rs], s43 offen offset:3072 lds\n" \
    "s_add_u32 s44, %[ld0], 61440\n" \
    "s_add_u32 s43, %[so0], 61440\n" \
    "s_mov_b32 m0, s44\n" \
    "s_nop 0\n" \
    "buffer_load_dwordx4 %[l16], %[rs], s43 offen offset:0 lds\n" \
    "buffer_load_dwordx4 %[l16], %[rs], s43 offen offset:1024 lds\n" \
    "buffer_load_dwordx4 %[l16], %[rs], s43 offen offset:2048 lds\n" \
    "s_add_u32 s50, %[woff], 0\n" \
    "buffer_load_dwordx4 a[0:3], %[wv], %[rs], s50 offen offset:0\n" \
    "s_add_u32 s51, %[woff], 28672\n" \
    "buffer_load_dwordx4 a[112:115], %[wv], %[rs], s51 offen offset:0\n" \
    "buffer_load_dwordx4 a[4:7], %[wv], %[rs], s50 offen offset:512\n" \
    "buffer_load_dwordx4 a[116:119], %[wv], %[rs], s51 offen offset:512\n" \
    "buffer_load_dwordx4 a[8:11], %[wv], %[rs], s50 offen offset:1024\n" \
    "buffer_load_dwordx4 a[120:123], %[wv], %[rs], s51 offen offset:1024\n" \
    "buffer_load_dwordx4 a[12:15], %[wv], %[rs], s50 offen offset:1536\n" \
    "buffer_load_dwordx4 a[124:127], %[wv], %[rs], s51 offen offset:1536\n" \
    "buffer_load_dwordx4 a[16:19], %[wv], %[rs], s50 offen offset:2048\n" \
    "buffer_load_dwordx4 a[128:131], %[wv], %[rs], s51 offen offset:2048\n" \
    "buffer_load_dwordx4 a[20:23], %[wv], %[rs], s50 offen offset:2560\n" \
    "buffer_load_dwordx4 a[132:135], %[wv], %[rs], s51 offen offset:2560\n" \
    "buffer_load_dwordx4 a[24:27], %[wv], %[rs], s50 offen offset:3072\n" \
    "buffer_load_dwordx4 a[136:139], %[wv], %[rs], s51 offen offset:3072\n" \
    "buffer_load_dwordx4 a[28:31], %[wv], %[rs], s50 offen offset:3584\n" \
    "buffer_load_dwordx4 a[140:143], %[wv], %[rs], s51 offen offset:3584\n" \
    "s_add_u32 s50, %[woff], 4096\n" \
    "buffer_load_dwordx4 a[32:35], %[wv], %[rs], s50 offen offset:0\n" \
    "s_add_u32 s51, %[woff], 32768\n" \
    "buffer_load_dwordx4 a[144:147], %[wv], %[rs], s51 offen offset:0\n" \
    "buffer_load_dwordx4 a[36:39], %[wv], %[rs], s50 offen offset:512\n" \
    "buffer_load_dwordx4 a[148:151], %[wv], %[rs], s51 offen offset:512\n" \
    "buffer_load_dwordx4 a[40:43], %[wv], %[rs], s50 offen offset:1024\n" \
    "buffer_load_dwordx4 a[152:155], %[wv], %[rs], s51 offen offset:1024\n" \
    "buffer_load_dwordx4 a[44:47], %[wv], %[rs], s50 offen offset:1536\n" \
    "buffer_load_dwordx4 a[156:159], %[wv], %[rs], s51 offen offset:1536\n" \
    "buffer_load_dwordx4 a[48:51], %[wv], %[rs], s50 offen offset:2048\n" \
    "buffer_load_dwordx4 a[160:163], %[wv], %[rs], s51 offen offset:2048\n" \
    "buffer_load_dwordx4 a[52:55], %[wv], %[rs], s50 offen offset:2560\n" \
    "buffer_load_dwordx4 a[164:167], %[wv], %[rs], s51 offen offset:2560\n" \
    "buffer_load_dwordx4 a[56:59], %[wv], %[rs], s50 offen offset:3072\n" \
    "buffer_load_dwordx4 a[168:171], %[wv], %[rs], s51 offen offset:3072\n" \
    "buffer_load_dwordx4 a[60:63], %[wv], %[rs], s50 offen offset:3584\n" \
    "buffer_load_dwordx4 a[172:175], %[wv], %[rs], s51 offen offset:3584\n" \
    "s_add_u32 s50, %[woff], 8192\n" \
    "buffer_load_dwordx4 a[64:67], %[wv], %[rs], s50 offen offset:0\n" \
    "s_add_u32 s51, %[woff], 36864\n" \
    "buffer_load_dwordx4 a[176:179], %[wv], %[rs], s51 offen offset:0\n" \
    "buffer_load_dwordx4 a[68:71], %[wv], %[rs], s50 offen offset:512\n" \
    "buffer_load_dwordx4 a[180:183], %[wv], %[rs], s51 offen offset:512\n" \
    "buffer_load_dwordx4 a[72:75], %[wv], %[rs], s50 offen offset:1024\n" \
    "buffer_load_dwordx4 a[184:187], %[wv], %[rs], s51 offen offset:1024\n" \
    "buffer_load_dwordx4 a[76:79], %[wv], %[rs], s50 offen offset:1536\n" \
    "buffer_load_dwordx4 a[188:191], %[wv], %[rs], s51 offen offset:1536\n" \
    "buffer_load_dwordx4 a[80:83], %[wv], %[rs], s50 offen offset:2048\n" \
    "buffer_load_dwordx4 a[192:195], %[wv], %[rs], s51 offen offset:2048\n" \
    "buffer_load_dwordx4 a[84:87], %[wv], %[rs], s50 offen offset:2560\n" \
    "buffer_load_dwordx4 a[196:199], %[wv], %[rs], s51 offen offset:2560\n" \
    "buffer_load_dwordx4 a[88:91], %[wv], %[rs], s50 offen offset:3072\n" \
    "buffer_load_dwordx4 a[200:203], %[wv], %[rs], s51 offen offset:3072\n" \
    "buffer_load_dwordx4 a[92:95], %[wv], %[rs], s50 offen offset:3584\n" \
    "buffer_load_dwordx4 a[204:207], %[wv], %[rs], s51 offen offset:3584\n" \
    "s_add_u32 s50, %[woff], 12288\n" \
    "buffer_load_dwordx4 a[96:99], %[wv], %[rs], s50 offen offset:0\n" \
    "s_add_u32 s51, %[woff], 40960\n" \
    "buffer_load_dwordx4 a[208:211], %[wv], %[rs], s51 offen offset:0\n" \
    "buffer_load_dwordx4 a[100:103], %[wv], %[rs], s50 offen offset:512\n" \
    "buffer_load_dwordx4 a[212:215], %[wv], %[rs], s51 offen offset:512\n" \
    "buffer_load_dwordx4 a[104:107], %[wv], %[rs], s50 offen offset:1024\n" \
    "buffer_load_dwordx4 a[216:219], %[wv], %[rs], s51 offen offset:1024\n" \
    "buffer_load_dwordx4 a[108:111], %[wv], %[rs], s50 offen offset:1536\n" \
    "buffer_load_dwordx4 a[220:223], %[wv], %[rs], s51 offen offset:1536\n" \
    "s_mov_b32 s40, 0\n" \
    "s_mov_b32 s54, 0\n" \
    "s_lshl_b32 s41, %[pol], 13\n" \
    "s_add_u32 s41, s41, %[out0]\n" \
    "v_mov_b32 v61, %[xb]\n" \
    "v_add_u32 v62, 28672, %[xb]\n" \
    "s_mov_b32 s45, 28672\n" \
    "s_add_u32 s44, %[ld0], 86016\n" \
    "s_add_u32 s43, %[so0], 86016\n" \
    "s_waitcnt vmcnt(0)\n" \
    "s_barrier\n" \
    "ds_read_b128 v[32:35], v61 offset:0\n" \
    "ds_read_b128 v[36:39], v61 offset:512\n" \
    "ds_read_b128 v[40:43], v61 offset:1024\n" \
    "s_mov_b32 s53, 0\n" \
    "ds_read_b128 v[44:47], v61 offset:1536\n" \
    "s_waitcnt lgkmcnt(3)\n" \
    "v_mfma_f32_32x32x16_bf16 v[64:79], a[0:3], v[32:35], 0\n" \
    "s_waitcnt vmcnt(8)\n" \
    "s_barrier\n" \
    "v_mfma_f32_32x32x16_bf16 v[80:95], a[112:115], v[32:35], 0\n" \
    "ds_read_b128 v[32:35], v61 offset:2048\n" \
    "s_waitcnt lgkmcnt(3)\n" \
    "v_mfma_f32_32x32x16_bf16 v[64:79], a[4:7], v[36:39], v[64:79]\n" \
    "v_mfma_f32_32x32x16_bf16 v[80:95], a[116:119], v[36:39], v[80:95]\n" \
    "ds_read_b128 v[36:39], v61 offset:2560\n" \
    "s_waitcnt lgkmcnt(3)\n" \
    "v_mfma_f32_32x32x16_bf16 v[64:79], a[8:11], v[40:43], v[64:79]\n" \
    "v_mfma_f32_32x32x16_bf16 v[80:95], a[120:123], v[40:43], v[80:95]\n" \
    "ds_read_b128 v[40:43], v61 offset:3072\n" \
    "s_waitcnt lgkmcnt(3)\n" \
    "v_mfma_f32_32x32x16_bf16 v[64:79], a[12:15], v[44:47], v[64:79]\n" \
    "v_mfma_f32_32x32x16_bf16 v[80:95], a[124:127], v[44:47], v[80:95]\n" \
    "ds_read_b128 v[44:47], v61 offset:3584\n" \
    "s_waitcnt lgkmcnt(3)\n" \
    "v_mfma_f32_32x32x16_bf16 v[64:79], a[16:19], v[32:35], v[64:79]\n" \
    "v_mfma_f32_32x32x16_bf16 v[80:95], a[128:131], v[32:35], v[80:95]\n" \
    "ds_read_b128 v[32:35], v61 offset:4096\n" \
    "s_waitcnt lgkmcnt(3)\n" \
    "v_mfma_f32_32x32x16_bf16 v[64:79], a[20:23], v[36:39], v[64:79]\n" \
    "v_mfma_f32_32x32x16_bf16 v[80:95], a[132:135], v[36:39], v[80:95]\n" \
    "ds_read_b128 v[36:39], v61 offset:4608\n" \
    "s_waitcnt lgkmcnt(3)\n" \
    "v_mfma_f32_32x32x16_bf16 v[64:79], a[24:27], v[40:43], v[64:79]\n" \
    "v_mfma_f32_32x32x16_bf16 v[80:95], a[136:139], v[40:43], v[80:95]\n" \
    "ds_read_b128 v[40:43], v61 offset:5120\n" \
    "s_waitcnt lgkmcnt(3)\n" \
    "v_mfma_f32_32x32x16_bf16 v[64:79], a[28:31], v[44:47], v[64:79]\n" \
    "v_mfma_f32_32x32x16_bf16 v[80:95], a[140:143], v[44:47], v[80:95]\n" \
    "ds_read_b128 v[44:47], v61 offset:5632\n" \
    "s_waitcnt lgkmcnt(3)\n" \
    "v_mfma_f32_32x32x16_bf16 v[64:79], a[32:35], v[32:35], v[64:79]\n" \
    "v_mfma_f32_32x32x16_bf16 v[80:95], a[144:147], v[32:35], v[80:95]\n" \
    "ds_read_b128 v[32:35], v61 offset:6144\n" \
    "s_waitcnt lgkmcnt(3)\n" \
    "v_mfma_f32_32x32x16_bf16 v[64:79], a[36:39], v[36:39], v[64:79]\n" \
    "v_mfma_f32_32x32x16_bf16 v[80:95], a[148:151], v[36:39], v[80:95]\n" \
    "ds_read_b128 v[36:39], v61 offset:6656\n" \
    "s_waitcnt lgkmcnt(3)\n" \
    "v_mfma_f32_32x32x16_bf16 v[64:79], a[40:43], v[40:43], v[64:79]\n" \
    "v_mfma_f32_32x32x16_bf16 v[80:95], a[152:155], v[40:43], v[80:95]\n" \
    "ds_read_b128 v[40:43], v61 offset:7168\n" \
    "s_waitcnt lgkmcnt(3)\n" \
    "v_mfma_f32_32x32x16_bf16 v[64:79], a[44:47], v[44:47], v[64:79]\n" \
    "v_mfma_f32_32x32x16_bf16 v[80:95], a[156:159], v[44:47], v[80:95]\n" \
    "ds_read_b128 v[44:47], v61 offset:7680\n" \
    "s_mov_b32 m0, s44\n" \
    "s_waitcnt lgkmcnt(3)\n" \
    "v_mfma_f32_32x32x16_bf16 v[64:79], a[48:51], v[32:35], v[64:79]\n" \
    "buffer_load_dwordx4 %[l16], %[rs], s43 offen offset:0 lds\n" \
    "v_mfma_f32_32x32x16_bf16 v[80:95], a[160:163], v[32:35], v[80:95]\n" \
    "ds_read_b128 v[32:35], v61 offset:8192\n" \
    "s_waitcnt lgkmcnt(3)\n" \
    "v_mfma_f32_32x32x16_bf16 v[64:79], a[52:55], v[36:39], v[64:79]\n" \
    "v_mfma_f32_32x32x16_bf16 v[80:95], a[164:167], v[36:39], v[80:95]\n" \
    "ds_read_b128 v[36:39], v61 offset:8704\n" \
    "s_waitcnt lgkmcnt(3)\n" \
    "v_mfma_f32_32x32x16_bf16 v[64:79], a[56:59], v[40:43], v[64:79]\n" \
    "buffer_load_dwordx4 %[l16], %[rs], s43 offen offset:1024 lds\n" \
    "v_mfma_f32_32x32x16_bf16 v[80:95], a[168:171], v[40:43], v[80:95]\n" \
    "ds_read_b128 v[40:43], v61 offset:9216\n" \
    "s_waitcnt lgkmcnt(3)\n" \
    "v_mfma_f32_32x32x16_bf16 v[64:79], a[60:63], v[44:47], v[64:79]\n" \
    "v_mfma_f32_32x32x16_bf16 v[80:95], a[172:175], v[44:47], v[80:95]\n" \
    "ds_read_b128 v[44:47], v61 offset:9728\n" \
    "s_waitcnt lgkmcnt(3)\n" \
    "v_mfma_f32_32x32x16_bf16 v[64:79], a[64:67], v[32:35], v[64:79]\n" \
    "buffer_load_dwordx4 %[l16], %[rs], s43 offen offset:2048 lds\n" \
    "v_mfma_f32_32x32x16_bf16 v[80:95], a[176:179], v[32:35], v[80:95]\n" \
    "ds_read_b128 v[32:35], v61 offset:10240\n" \
    "s_waitcnt lgkmcnt(3)\n" \
    "v_mfma_f32_32x32x16_bf16 v[64:79], a[68:71], v[36:39], v[64:79]\n" \
    "v_mfma_f32_32x32x16_bf16 v[80:95], a[180:183], v[36:39], v[80:95]\n" \
    "ds_read_b128 v[36:39], v61 offset:10752\n" \
    "s_waitcnt lgkmcnt(3)\n" \
    "v_mfma_f32_32x32x16_bf16 v[64:79], a[72:75], v[40:43], v[64:79]\n" \
    "buffer_load_dwordx4 %[l16], %[rs], s43 offen offset:3072 lds\n" \
    "v_mfma_f32_32x32x16_bf16 v[80:95], a[184:187], v[40:43], v[80:95]\n" \
    "ds_read_b128 v[40:43], v61 offset:11264\n" \
    "s_waitcnt lgkmcnt(3)\n" \
    "v_mfma_f32_32x32x16_bf16 v[64:79], a[76:79], v[44:47], v[64:79]\n" \
    "s_add_u32 s44, s44, 4096\n" \
    "s_add_u32 s43, s43, 4096\n" \
    "v_mfma_f32_32x32x16_bf16 v[80:95], a[188:191], v[44:47], v[80:95]\n" \
    "ds_read_b128 v[44:47], v61 offset:11776\n" \
    "s_waitcnt lgkmcnt(3)\n" \
    "v_mfma_f32_32x32x16_bf16 v[64:79], a[80:83], v[32:35], v[64:79]\n" \
    "v_mfma_f32_32x32x16_bf16 v[80:95], a[192:195], v[32:35], v[80:95]\n" \
    "ds_read_b128 v[32:35], v61 offset:12288\n" \
    "s_mov_b32 m0, s44\n" \
    "s_waitcnt lgkmcnt(3)\n" \
    "v_mfma_f32_32x32x16_bf16 v[64:79], a[84:87], v[36:39], v[64:79]\n" \
    "buffer_load_dwordx4 %[l16], %[rs], s43 offen offset:0 lds\n" \
    "v_mfma_f32_32x32x16_bf16 v[80:95], a[196:199], v[36:39], v[80:95]\n" \
    "ds_read_b128 v[36:39], v61 offset:12800\n" \
    "s_waitcnt lgkmcnt(3)\n" \
    "v_mfma_f32_32x32x16_bf16 v[64:79], a[88:91], v[40:43], v[64:79]\n" \
    "v_mfma_f32_32x32x16_bf16 v[80:95], a[200:203], v[40:43], v[80:95]\n" \
    "ds_read_b128 v[40:43], v61 offset:13312\n" \
    "s_waitcnt lgkmcnt(3)\n" \
    "v_mfma_f32_32x32x16_bf16 v[64:79], a[92:95], v[44:47], v[64:79]\n" \
    "buffer_load_dwordx4 %[l16], %[rs], s43 offen offset:1024 lds\n" \
    "v_mfma_f32_32x32x16_bf16 v[80:95], a[204:207], v[44:47], v[80:95]\n" \
    "ds_read_b128 v[44:47], v61 offset:13824\n" \
    "s_waitcnt lgkmcnt(3)\n" \
    "v_mfma_f32_32x32x16_bf16 v[64:79], a[96:99], v[32:35], v[64:79]\n" \
    "v_mfma_f32_32x32x16_bf16 v[80:95], a[208:211], v[32:35], v[80:95]\n" \
    "ds_read_b128 v[32:35], v62 offset:0\n" \
    "s_waitcnt lgkmcnt(3)\n" \
    "v_mfma_f32_32x32x16_bf16 v[64:79], a[100:103], v[36:39], v[64:79]\n" \
    "buffer_load_dwordx4 %[l16], %[rs], s43 offen offset:2048 lds\n" \
    "v_mfma_f32_32x32x16_bf16 v[80:95], a[212:215], v[36:39], v[80:95]\n" \
    "ds_read_b128 v[36:39], v62 offset:512\n" \
    "s_waitcnt lgkmcnt(3)\n" \
    "v_mfma_f32_32x32x16_bf16 v[64:79], a[104:107], v[40:43], v[64:79]\n" \
    "s_add_u32 s40, s40, 1\n" \
    "s_cmp_lt_u32 s40, 61\n" \
    "s_cselect_b32 s50, 28672, 0\n" \
    "s_add_u32 s43, s43, s50\n" \
    "v_mfma_f32_32x32x16_bf16 v[80:95], a[216:219], v[40:43], v[80:95]\n" \
    "ds_read_b128 v[40:43], v62 offset:1024\n" \
    "s_waitcnt lgkmcnt(3)\n" \
    "v_mfma_f32_32x32x16_bf16 v[64:79], a[108:111], v[44:47], v[64:79]\n" \
    "s_sub_u32 s43, s43, 4096\n" \
    "s_add_u32 s45, s45, 28672\n" \
    "s_cmp_eq_u32 s45, 114688\n" \
    "s_cselect_b32 s45, 0, s45\n" \
    "v_mfma_f32_32x32x16_bf16 v[80:95], a[220:223], v[44:47], v[80:95]\n" \
    "v_mov_b32 v61, v62\n" \
    "v_add_u32 v62, s45, %[xb]\n" \
    "s_add_u32 s44, s45, 57344\n" \
    "s_cmp_ge_u32 s44, 114688\n" \
    "s_cselect_b32 s50, 114688, 0\n" \
    "s_sub_u32 s44, s44, s50\n" \
    "s_add_u32 s44, s44, %[ld0]\n" \
    "s_add_u32 s46, s41, 2048\n" \
    "s_nop 7\n" \
    "v_cvt_pk_bf16_f32 v48, v64, v65\n" \
    "v_cvt_pk_bf16_f32 v49, v66, v67\n" \
    "v_cvt_pk_bf16_f32 v50, v68, v69\n" \
    "v_cvt_pk_bf16_f32 v51, v70, v71\n" \
    "buffer_store_dwordx4 v[48:51], %[ov], %[rs], s41 offen offset:0 sc1\n" \
    "v_cvt_pk_bf16_f32 v52, v72, v73\n" \
    "v_cvt_pk_bf16_f32 v53, v74, v75\n" \
    "v_cvt_pk_bf16_f32 v54, v76, v77\n" \
    "v_cvt_pk_bf16_f32 v55, v78, v79\n" \
    "buffer_store_dwordx4 v[52:55], %[ov], %[rs], s41 offen offset:512 sc1\n" \
    "s_nop 1\n" \
    "v_cvt_pk_bf16_f32 v48, v80, v81\n" \
    "v_cvt_pk_bf16_f32 v49, v82, v83\n" \
    "v_cvt_pk_bf16_f32 v50, v84, v85\n" \
    "v_cvt_pk_bf16_f32 v51, v86, v87\n" \
    "buffer_store_dwordx4 v[48:51], %[ov], %[rs], s46 offen offset:0 sc1\n" \
    "v_cvt_pk_bf16_f32 v52, v88, v89\n" \
    "v_cvt_pk_bf16_f32 v53, v90, v91\n" \
    "v_cvt_pk_bf16_f32 v54, v92, v93\n" \
    "v_cvt_pk_bf16_f32 v55, v94, v95\n" \
    "buffer_store_dwordx4 v[52:55], %[ov], %[rs], s46 offen offset:512 sc1\n" \
    "v_mov_b32 v60, 1\n" \
    "s_waitcnt vmcnt(0)\n" \
    "buffer_store_dword v60, %[fo], %[rs], 0 offen sc1\n" \
    "s_add_u32 s54, s54, 1\n" \
    "s_lshr_b32 s55, s54, 1\n" \
    "s_xor_b32 s55, s55, %[pol]\n" \
    "s_and_b32 s55, s55, 1\n" \
    "s_lshl_b32 s55, s55, 13\n" \
    "s_lshl_b32 s41, s54, 20\n" \
    "s_add_u32 s41, s41, s55\n" \
    "s_add_u32 s41, s41, %[out0]\n" \
    "s_mov_b32 s53, 1\n" \
    "ds_read_b128 v[44:47], v61 offset:1536\n" \
    "s_waitcnt lgkmcnt(3)\n" \
    "v_mfma_f32_32x32x16_bf16 v[96:111], a[0:3], v[32:35], 0\n" \
    "s_waitcnt vmcnt(8)\n" \
    "s_barrier\n" \
    "v_mfma_f32_32x32x16_bf16 v[112:127], a[112:115], v[32:35], 0\n" \
    "ds_read_b128 v[32:35], v61 offset:2048\n" \
    "s_waitcnt lgkmcnt(3)\n" \
    "v_mfma_f32_32x32x16_bf16 v[96:111], a[4:7], v[36:39], v[96:111]\n" \
    "v_mfma_f32_32x32x16_bf16 v[112:127], a[116:119], v[36:39], v[112:127]\n" \
    "ds_read_b128 v[36:39], v61 offset:2560\n" \
    "s_waitcnt lgkmcnt(3)\n" \
    "v_mfma_f32_32x32x16_bf16 v[96:111], a[8:11], v[40:43], v[96:111]\n" \
    "v_mfma_f32_32x32x16_bf16 v[112:127], a[120:123], v[40:43], v[112:127]\n" \
    "ds_read_b128 v[40:43], v61 offset:3072\n" \
    "s_waitcnt lgkmcnt(3)\n" \
    "v_mfma_f32_32x32x16_bf16 v[96:111], a[12:15], v[44:47], v[96:111]\n" \
    "v_mfma_f32_32x32x16_bf16 v[112:127], a[124:127], v[44:47], v[112:127]\n" \
    "ds_read_b128 v[44:47], v61 offset:3584\n" \
    "s_waitcnt lgkmcnt(3)\n" \
    "v_mfma_f32_32x32x16_bf16 v[96:111], a[16:19], v[32:35], v[96:111]\n" \
    "v_mfma_f32_32x32x16_bf16 v[112:127], a[128:131], v[32:35], v[112:127]\n" \
    "ds_read_b128 v[32:35], v61 offset:4096\n" \
    "s_waitcnt lgkmcnt(3)\n" \
    "v_mfma_f32_32x32x16_bf16 v[96:111], a[20:23], v[36:39], v[96:111]\n" \
    "v_mfma_f32_32x32x16_bf16 v[112:127], a[132:135], v[36:39], v[112:127]\n" \
    "ds_read_b128 v[36:39], v61 offset:4608\n" \
    "s_waitcnt lgkmcnt(3)\n" \
    "v_mfma_f32_32x32x16_bf16 v[96:111], a[24:27], v[40:43], v[96:111]\n" \
    "v_mfma_f32_32x32x16_bf16 v[112:127], a[136:139], v[40:43], v[112:127]\n" \
    "ds_read_b128 v[40:43], v61 offset:5120\n" \
    "s_waitcnt lgkmcnt(3)\n" \
    "v_mfma_f32_32x32x16_bf16 v[96:111], a[28:31], v[44:47], v[96:111]\n" \
    "v_mfma_f32_32x32x16_bf16 v[112:127], a[140:143], v[44:47], v[112:127]\n" \
    "ds_read_b128 v[44:47], v61 offset:5632\n" \
    "s_waitcnt lgkmcnt(3)\n" \
    "v_mfma_f32_32x32x16_bf16 v[96:111], a[32:35], v[32:35], v[96:111]\n" \
    "v_mfma_f32_32x32x16_bf16 v[112:127], a[144:147], v[32:35], v[112:127]\n" \
    "ds_read_b128 v[32:35], v61 offset:6144\n" \
    "s_waitcnt lgkmcnt(3)\n" \
    "v_mfma_f32_32x32x16_bf16 v[96:111], a[36:39], v[36:39], v[96:111]\n" \
    "v_mfma_f32_32x32x16_bf16 v[112:127], a[148:151], v[36:39], v[112:127]\n" \
    "ds_read_b128 v[36:39], v61 offset:6656\n" \
    "s_waitcnt lgkmcnt(3)\n" \
    "v_mfma_f32_32x32x16_bf16 v[96:111], a[40:43], v[40:43], v[96:111]\n" \
    "v_mfma_f32_32x32x16_bf16 v[112:127], a[152:155], v[40:43], v[112:127]\n" \
    "ds_read_b128 v[40:43], v61 offset:7168\n" \
    "s_waitcnt lgkmcnt(3)\n" \
    "v_mfma_f32_32x32x16_bf16 v[96:111], a[44:47], v[44:47], v[96:111]\n" \
    "v_mfma_f32_32x32x16_bf16 v[112:127], a[156:159], v[44:47], v[112:127]\n" \
    "ds_read_b128 v[44:47], v61 offset:7680\n" \
    "s_mov_b32 m0, s44\n" \
    "s_waitcnt lgkmcnt(3)\n" \
    "v_mfma_f32_32x32x16_bf16 v[96:111], a[48:51], v[32:35], v[96:111]\n" \
    "buffer_load_dwordx4 %[l16], %[rs], s43 offen offset:0 lds\n" \
    "v_mfma_f32_32x32x16_bf16 v[112:127], a[160:163], v[32:35], v[112:127]\n" \
    "ds_read_b128 v[32:35], v61 offset:8192\n" \
    "s_waitcnt lgkmcnt(3)\n" \
    "v_mfma_f32_32x32x16_bf16 v[96:111], a[52:55], v[36:39], v[96:111]\n" \
    "v_mfma_f32_32x32x16_bf16 v[112:127], a[164:167], v[36:39], v[112:127]\n" \
    "ds_read_b128 v[36:39], v61 offset:8704\n" \
    "s_waitcnt lgkmcnt(3)\n" \
    "v_mfma_f32_32x32x16_bf16 v[96:111], a[56:59], v[40:43], v[96:111]\n" \
    "buffer_load_dwordx4 %[l16], %[rs], s43 offen offset:1024 lds\n" \
    "v_mfma_f32_32x32x16_bf16 v[112:127], a[168:171], v[40:43], v[112:127]\n" \
    "ds_read_b128 v[40:43], v61 offset:9216\n" \
    "s_waitcnt lgkmcnt(3)\n" \
    "v_mfma_f32_32x32x16_bf16 v[96:111], a[60:63], v[44:47], v[96:111]\n" \
    "v_mfma_f32_32x32x16_bf16 v[112:127], a[172:175], v[44:47], v[112:127]\n" \
    "ds_read_b128 v[44:47], v61 offset:9728\n" \
    "s_waitcnt lgkmcnt(3)\n" \
    "v_mfma_f32_32x32x16_bf16 v[96:111], a[64:67], v[32:35], v[96:111]\n" \
    "buffer_load_dwordx4 %[l16], %[rs], s43 offen offset:2048 lds\n" \
    "v_mfma_f32_32x32x16_bf16 v[112:127], a[176:179], v[32:35], v[112:127]\n" \
    "ds_read_b128 v[32:35], v61 offset:10240\n" \
    "s_waitcnt lgkmcnt(3)\n" \
    "v_mfma_f32_32x32x16_bf16 v[96:111], a[68:71], v[36:39], v[96:111]\n" \
    "v_mfma_f32_32x32x16_bf16 v[112:127], a[180:183], v[36:39], v[112:127]\n" \
    "ds_read_b128 v[36:39], v61 offset:10752\n" \
    "s_waitcnt lgkmcnt(3)\n" \
    "v_mfma_f32_32x32x16_bf16 v[96:111], a[72:75], v[40:43], v[96:111]\n" \
    "buffer_load_dwordx4 %[l16], %[rs], s43 offen offset:3072 lds\n" \
    "v_mfma_f32_32x32x16_bf16 v[112:127], a[184:187], v[40:43], v[112:127]\n" \
    "ds_read_b128 v[40:43], v61 offset:11264\n" \
    "s_waitcnt lgkmcnt(3)\n" \
    "v_mfma_f32_32x32x16_bf16 v[96:111], a[76:79], v[44:47], v[96:111]\n" \
    "s_add_u32 s44, s44, 4096\n" \
    "s_add_u32 s43, s43, 4096\n" \
    "v_mfma_f32_32x32x16_bf16 v[112:127], a[188:191], v[44:47], v[112:127]\n" \
    "ds_read_b128 v[44:47], v61 offset:11776\n" \
    "s_waitcnt lgkmcnt(3)\n" \
    "v_mfma_f32_32x32x16_bf16 v[96:111], a[80:83], v[32:35], v[96:111]\n" \
    "v_mfma_f32_32x32x16_bf16 v[112:127], a[192:195], v[32:35], v[112:127]\n" \
    "ds_read_b128 v[32:35], v61 offset:12288\n" \
    "s_mov_b32 m0, s44\n" \
    "s_waitcnt lgkmcnt(3)\n" \
    "v_mfma_f32_32x32x16_bf16 v[96:111], a[84:87], v[36:39], v[96:111]\n" \
    "buffer_load_dwordx4 %[l16], %[rs], s43 offen offset:0 lds\n" \
    "v_mfma_f32_32x32x16_bf16 v[112:127], a[196:199], v[36:39], v[112:127]\n" \
    "ds_read_b128 v[36:39], v61 offset:12800\n" \
    "s_waitcnt lgkmcnt(3)\n" \
    "v_mfma_f32_32x32x16_bf16 v[96:111], a[88:91], v[40:43], v[96:111]\n" \
    "v_mfma_f32_32x32x16_bf16 v[112:127], a[200:203], v[40:43], v[112:127]\n" \
    "ds_read_b128 v[40:43], v61 offset:13312\n" \
    "s_waitcnt lgkmcnt(3)\n" \
    "v_mfma_f32_32x32x16_bf16 v[96:111], a[92:95], v[44:47], v[96:111]\n" \
    "buffer_load_dwordx4 %[l16], %[rs], s43 offen offset:1024 lds\n" \
    "v_mfma_f32_32x32x16_bf16 v[112:127], a[204:207], v[44:47], v[112:127]\n" \
    "ds_read_b128 v[44:47], v61 offset:13824\n" \
    "s_waitcnt lgkmcnt(3)\n" \
    "v_mfma_f32_32x32x16_bf16 v[96:111], a[96:99], v[32:35], v[96:111]\n" \
    "v_mfma_f32_32x32x16_bf16 v[112:127], a[208:211], v[32:35], v[112:127]\n" \
    "ds_read_b128 v[32:35], v62 offset:0\n" \
    "s_waitcnt lgkmcnt(3)\n" \
    "v_mfma_f32_32x32x16_bf16 v[96:111], a[100:103], v[36:39], v[96:111]\n" \
    "buffer_load_dwordx4 %[l16], %[rs], s43 offen offset:2048 lds\n" \
    "v_mfma_f32_32x32x16_bf16 v[112:127], a[212:215], v[36:39], v[112:127]\n" \
    "ds_read_b128 v[36:39], v62 offset:512\n" \
    "s_waitcnt lgkmcnt(3)\n" \
    "v_mfma_f32_32x32x16_bf16 v[96:111], a[104:107], v[40:43], v[96:111]\n" \
    "s_add_u32 s40, s40, 1\n" \
    "s_cmp_lt_u32 s40, 61\n" \
    "s_cselect_b32 s50, 28672, 0\n" \
    "s_add_u32 s43, s43, s50\n" \
    "v_mfma_f32_32x32x16_bf16 v[112:127], a[216:219], v[40:43], v[112:127]\n" \
    "ds_read_b128 v[40:43], v62 offset:1024\n" \
    "s_waitcnt lgkmcnt(3)\n" \
    "v_mfma_f32_32x32x16_bf16 v[96:111], a[108:111], v[44:47], v[96:111]\n" \
    "s_sub_u32 s43, s43, 4096\n" \
    "s_add_u32 s45, s45, 28672\n" \
    "s_cmp_eq_u32 s45, 114688\n" \
    "s_cselect_b32 s45, 0, s45\n" \
    "v_mfma_f32_32x32x16_bf16 v[112:127], a[220:223], v[44:47], v[112:127]\n" \
    "v_mov_b32 v61, v62\n" \
    "v_add_u32 v62, s45, %[xb]\n" \
    "s_add_u32 s44, s45, 57344\n" \
    "s_cmp_ge_u32 s44, 114688\n" \
    "s_cselect_b32 s50, 114688, 0\n" \
    "s_sub_u32 s44, s44, s50\n" \
    "s_add_u32 s44, s44, %[ld0]\n" \
    "Gloop_%=:\n" \
    "ds_read_b128 v[44:47], v61 offset:1536\n" \
    "s_waitcnt lgkmcnt(3)\n" \
    "v_mfma_f32_32x32x16_bf16 v[64:79], a[0:3], v[32:35], 0\n" \
    "s_waitcnt vmcnt(8)\n" \
    "s_barrier\n" \
    "v_mfma_f32_32x32x16_bf16 v[80:95], a[112:115], v[32:35], 0\n" \
    "ds_read_b128 v[32:35], v61 offset:2048\n" \
    "s_add_u32 s46, s41, 2048\n" \
    "s_waitcnt lgkmcnt(3)\n" \
    "v_mfma_f32_32x32x16_bf16 v[64:79], a[4:7], v[36:39], v[64:79]\n" \
    "v_cvt_pk_bf16_f32 v48, v96, v97\n" \
    "v_cvt_pk_bf16_f32 v49, v98, v99\n" \
    "v_cvt_pk_bf16_f32 v50, v100, v101\n" \
    "v_cvt_pk_bf16_f32 v51, v102, v103\n" \
    "v_mfma_f32_32x32x16_bf16 v[80:95], a[116:119], v[36:39], v[80:95]\n" \
    "ds_read_b128 v[36:39], v61 offset:2560\n" \
    "s_waitcnt lgkmcnt(3)\n" \
    "v_mfma_f32_32x32x16_bf16 v[64:79], a[8:11], v[40:43], v[64:79]\n" \
    "buffer_store_dwordx4 v[48:51], %[ov], %[rs], s41 offen offset:0 sc1\n" \
    "v_mfma_f32_32x32x16_bf16 v[80:95], a[120:123], v[40:43], v[80:95]\n" \
    "ds_read_b128 v[40:43], v61 offset:3072\n" \
    "s_waitcnt lgkmcnt(3)\n" \
    "v_mfma_f32_32x32x16_bf16 v[64:79], a[12:15], v[44:47], v[64:79]\n" \
    "v_cvt_pk_bf16_f32 v52, v104, v105\n" \
    "v_cvt_pk_bf16_f32 v53, v106, v107\n" \
    "v_cvt_pk_bf16_f32 v54, v108, v109\n" \
    "v_cvt_pk_bf16_f32 v55, v110, v111\n" \
    "v_mfma_f32_32x32x16_bf16 v[80:95], a[124:127], v[44:47], v[80:95]\n" \
    "ds_read_b128 v[44:47], v61 offset:3584\n" \
    "s_waitcnt lgkmcnt(3)\n" \
    "v_mfma_f32_32x32x16_bf16 v[64:79], a[16:19], v[32:35], v[64:79]\n" \
    "buffer_store_dwordx4 v[52:55], %[ov], %[rs], s41 offen offset:512 sc1\n" \
    "v_mfma_f32_32x32x16_bf16 v[80:95], a[128:131], v[32:35], v[80:95]\n" \
    "ds_read_b128 v[32:35], v61 offset:4096\n" \
    "s_waitcnt lgkmcnt(3)\n" \
    "v_mfma_f32_32x32x16_bf16 v[64:79], a[20:23], v[36:39], v[64:79]\n" \
    "v_cvt_pk_bf16_f32 v48, v112, v113\n" \
    "v_cvt_pk_bf16_f32 v49, v114, v115\n" \
    "v_cvt_pk_bf16_f32 v50, v116, v117\n" \
    "v_cvt_pk_bf16_f32 v51, v118, v119\n" \
    "v_mfma_f32_32x32x16_bf16 v[80:95], a[132:135], v[36:39], v[80:95]\n" \
    "ds_read_b128 v[36:39], v61 offset:4608\n" \
    "s_waitcnt lgkmcnt(3)\n" \
    "v_mfma_f32_32x32x16_bf16 v[64:79], a[24:27], v[40:43], v[64:79]\n" \
    "buffer_store_dwordx4 v[48:51], %[ov], %[rs], s46 offen offset:0 sc1\n" \
    "v_mfma_f32_32x32x16_bf16 v[80:95], a[136:139], v[40:43], v[80:95]\n" \
    "ds_read_b128 v[40:43], v61 offset:5120\n" \
    "s_waitcnt lgkmcnt(3)\n" \
    "v_mfma_f32_32x32x16_bf16 v[64:79], a[28:31], v[44:47], v[64:79]\n" \
    "v_cvt_pk_bf16_f32 v52, v120, v121\n" \
    "v_cvt_pk_bf16_f32 v53, v122, v123\n" \
    "v_cvt_pk_bf16_f32 v54, v124, v125\n" \
    "v_cvt_pk_bf16_f32 v55, v126, v127\n" \
    "v_mfma_f32_32x32x16_bf16 v[80:95], a[140:143], v[44:47], v[80:95]\n" \
    "ds_read_b128 v[44:47], v61 offset:5632\n" \
    "s_waitcnt lgkmcnt(3)\n" \
    "v_mfma_f32_32x32x16_bf16 v[64:79], a[32:35], v[32:35], v[64:79]\n" \
    "buffer_store_dwordx4 v[52:55], %[ov], %[rs], s46 offen offset:512 sc1\n" \
    "v_mfma_f32_32x32x16_bf16 v[80:95], a[144:147], v[32:35], v[80:95]\n" \
    "ds_read_b128 v[32:35], v61 offset:6144\n" \
    "s_waitcnt lgkmcnt(3)\n" \
    "v_mfma_f32_32x32x16_bf16 v[64:79], a[36:39], v[36:39], v[64:79]\n" \
    "s_add_u32 s54, s54, 1\n" \
    "s_lshr_b32 s55, s54, 1\n" \
    "s_xor_b32 s55, s55, %[pol]\n" \
    "s_and_b32 s55, s55, 1\n" \
    "v_mfma_f32_32x32x16_bf16 v[80:95], a[148:151], v[36:39], v[80:95]\n" \
    "ds_read_b128 v[36:39], v61 offset:6656\n" \
    "s_waitcnt lgkmcnt(3)\n" \
    "v_mfma_f32_32x32x16_bf16 v[64:79], a[40:43], v[40:43], v[64:79]\n" \
    "v_mfma_f32_32x32x16_bf16 v[80:95], a[152:155], v[40:43], v[80:95]\n" \
    "ds_read_b128 v[40:43], v61 offset:7168\n" \
    "s_waitcnt lgkmcnt(3)\n" \
    "v_mfma_f32_32x32x16_bf16 v[64:79], a[44:47], v[44:47], v[64:79]\n" \
    "s_lshl_b32 s55, s55, 13\n" \
    "s_lshl_b32 s41, s54, 20\n" \
    "s_add_u32 s41, s41, s55\n" \
    "s_add_u32 s41, s41, %[out0]\n" \
    "v_mfma_f32_32x32x16_bf16 v[80:95], a[156:159], v[44:47], v[80:95]\n" \
    "ds_read_b128 v[44:47], v61 offset:7680\n" \
    "s_mov_b32 m0, s44\n" \
    "s_waitcnt lgkmcnt(3)\n" \
    "v_mfma_f32_32x32x16_bf16 v[64:79], a[48:51], v[32:35], v[64:79]\n" \
    "buffer_load_dwordx4 %[l16], %[rs], s43 offen offset:0 lds\n" \
    "v_mfma_f32_32x32x16_bf16 v[80:95], a[160:163], v[32:35], v[80:95]\n" \
    "ds_read_b128 v[32:35], v61 offset:8192\n" \
    "s_waitcnt lgkmcnt(3)\n" \
    "v_mfma_f32_32x32x16_bf16 v[64:79], a[52:55], v[36:39], v[64:79]\n" \
    "v_mfma_f32_32x32x16_bf16 v[80:95], a[164:167], v[36:39], v[80:95]\n" \
    "ds_read_b128 v[36:39], v61 offset:8704\n" \
    "s_waitcnt lgkmcnt(3)\n" \
    "v_mfma_f32_32x32x16_bf16 v[64:79], a[56:59], v[40:43], v[64:79]\n" \
    "buffer_load_dwordx4 %[l16], %[rs], s43 offen offset:1024 lds\n" \
    "v_mfma_f32_32x32x16_bf16 v[80:95], a[168:171], v[40:43], v[80:95]\n" \
    "ds_read_b128 v[40:43], v61 offset:9216\n" \
    "s_waitcnt lgkmcnt(3)\n" \
    "v_mfma_f32_32x32x16_bf16 v[64:79], a[60:63], v[44:47], v[64:79]\n" \
    "v_mfma_f32_32x32x16_bf16 v[80:95], a[172:175], v[44:47], v[80:95]\n" \
    "ds_read_b128 v[44:47], v61 offset:9728\n" \
    "s_waitcnt lgkmcnt(3)\n" \
    "v_mfma_f32_32x32x16_bf16 v[64:79], a[64:67], v[32:35], v[64:79]\n" \
    "buffer_load_dwordx4 %[l16], %[rs], s43 offen offset:2048 lds\n" \
    "v_mfma_f32_32x32x16_bf16 v[80:95], a[176:179], v[32:35], v[80:95]\n" \
    "ds_read_b128 v[32:35], v61 offset:10240\n" \
    "s_waitcnt lgkmcnt(3)\n" \
    "v_mfma_f32_32x32x16_bf16 v[64:79], a[68:71], v[36:39], v[64:79]\n" \
    "v_mfma_f32_32x32x16_bf16 v[80:95], a[180:183], v[36:39], v[80:95]\n" \
    "ds_read_b128 v[36:39], v61 offset:10752\n" \
    "s_waitcnt lgkmcnt(3)\n" \
    "v_mfma_f32_32x32x16_bf16 v[64:79], a[72:75], v[40:43], v[64:79]\n" \
    "buffer_load_dwordx4 %[l16], %[rs], s43 offen offset:3072 lds\n" \
    "v_mfma_f32_32x32x16_bf16 v[80:95], a[184:187], v[40:43], v[80:95]\n" \
    "ds_read_b128 v[40:43], v61 offset:11264\n" \
    "s_waitcnt lgkmcnt(3)\n" \
    "v_mfma_f32_32x32x16_bf16 v[64:79], a[76:79], v[44:47], v[64:79]\n" \
    "s_add_u32 s44, s44, 4096\n" \
    "s_add_u32 s43, s43, 4096\n" \
    "v_mfma_f32_32x32x16_bf16 v[80:95], a[188:191], v[44:47], v[80:95]\n" \
    "ds_read_b128 v[44:47], v61 offset:11776\n" \
    "s_waitcnt lgkmcnt(3)\n" \
    "v_mfma_f32_32x32x16_bf16 v[64:79], a[80:83], v[32:35], v[64:79]\n" \
    "v_mfma_f32_32x32x16_bf16 v[80:95], a[192:195], v[32:35], v[80:95]\n" \
    "ds_read_b128 v[32:35], v61 offset:12288\n" \
    "s_mov_b32 m0, s44\n" \
    "s_waitcnt lgkmcnt(3)\n" \
    "v_mfma_f32_32x32x16_bf16 v[64:79], a[84:87], v[36:39], v[64:79]\n" \
    "buffer_load_dwordx4 %[l16], %[rs], s43 offen offset:0 lds\n" \
    "v_mfma_f32_32x32x16_bf16 v[80:95], a[196:199], v[36:39], v[80:95]\n" \
    "ds_read_b128 v[36:39], v61 offset:12800\n" \
    "s_waitcnt lgkmcnt(3)\n" \
    "v_mfma_f32_32x32x16_bf16 v[64:79], a[88:91], v[40:43], v[64:79]\n" \
    "v_mfma_f32_32x32x16_bf16 v[80:95], a[200:203], v[40:43], v[80:95]\n" \
    "ds_read_b128 v[40:43], v61 offset:13312\n" \
    "s_waitcnt lgkmcnt(3)\n" \
    "v_mfma_f32_32x32x16_bf16 v[64:79], a[92:95], v[44:47], v[64:79]\n" \
    "buffer_load_dwordx4 %[l16], %[rs], s43 offen offset:1024 lds\n" \
    "v_mfma_f32_32x32x16_bf16 v[80:95], a[204:207], v[44:47], v[80:95]\n" \
    "ds_read_b128 v[44:47], v61 offset:13824\n" \
    "s_waitcnt lgkmcnt(3)\n" \
    "v_mfma_f32_32x32x16_bf16 v[64:79], a[96:99], v[32:35], v[64:79]\n" \
    "v_mov_b32 v60, s40\n" \
    "s_waitcnt vmcnt(6)\n" \
    "buffer_store_dword v60, %[fo], %[rs], 0 offen sc1\n" \
    "v_mfma_f32_32x32x16_bf16 v[80:95], a[208:211], v[32:35], v[80:95]\n" \
    "ds_read_b128 v[32:35], v62 offset:0\n" \
    "s_waitcnt lgkmcnt(3)\n" \
    "v_mfma_f32_32x32x16_bf16 v[64:79], a[100:103], v[36:39], v[64:79]\n" \
    "buffer_load_dwordx4 %[l16], %[rs], s43 offen offset:2048 lds\n" \
    "v_mfma_f32_32x32x16_bf16 v[80:95], a[212:215], v[36:39], v[80:95]\n" \
    "ds_read_b128 v[36:39], v62 offset:512\n" \
    "s_waitcnt lgkmcnt(3)\n" \
    "v_mfma_f32_32x32x16_bf16 v[64:79], a[104:107], v[40:43], v[64:79]\n" \
    "s_add_u32 s40, s40, 1\n" \
    "s_cmp_lt_u32 s40, 61\n" \
    "s_cselect_b32 s50, 28672, 0\n" \
    "s_add_u32 s43, s43, s50\n" \
    "v_mfma_f32_32x32x16_bf16 v[80:95], a[216:219], v[40:43], v[80:95]\n" \
    "ds_read_b128 v[40:43], v62 offset:1024\n" \
    "s_waitcnt lgkmcnt(3)\n" \
    "v_mfma_f32_32x32x16_bf16 v[64:79], a[108:111], v[44:47], v[64:79]\n" \
    "s_sub_u32 s43, s43, 4096\n" \
    "s_add_u32 s45, s45, 28672\n" \
    "s_cmp_eq_u32 s45, 114688\n" \
    "s_cselect_b32 s45, 0, s45\n" \
    "v_mfma_f32_32x32x16_bf16 v[80:95], a[220:223], v[44:47], v[80:95]\n" \
    "v_mov_b32 v61, v62\n" \
    "v_add_u32 v62, s45, %[xb]\n" \
    "s_add_u32 s44, s45, 57344\n" \
    "s_cmp_ge_u32 s44, 114688\n" \
    "s_cselect_b32 s50, 114688, 0\n" \
    "s_sub_u32 s44, s44, s50\n" \
    "s_add_u32 s44, s44, %[ld0]\n" \
    "ds_read_b128 v[44:47], v61 offset:1536\n" \
    "s_waitcnt lgkmcnt(3)\n" \
    "v_mfma_f32_32x32x16_bf16 v[96:111], a[0:3], v[32:35], 0\n" \
    "s_waitcnt vmcnt(8)\n" \
    "s_barrier\n" \
    "v_mfma_f32_32x32x16_bf16 v[112:127], a[112:115], v[32:35], 0\n" \
    "ds_read_b128 v[32:35], v61 offset:2048\n" \
    "s_add_u32 s46, s41, 2048\n" \
    "s_waitcnt lgkmcnt(3)\n" \
    "v_mfma_f32_32x32x16_bf16 v[96:111], a[4:7], v[36:39], v[96:111]\n" \
    "v_cvt_pk_bf16_f32 v48, v64, v65\n" \
    "v_cvt_pk_bf16_f32 v49, v66, v67\n" \
    "v_cvt_pk_bf16_f32 v50, v68, v69\n" \
    "v_cvt_pk_bf16_f32 v51, v70, v71\n" \
    "v_mfma_f32_32x32x16_bf16 v[112:127], a[116:119], v[36:39], v[112:127]\n" \
    "ds_read_b128 v[36:39], v61 offset:2560\n" \
    "s_waitcnt lgkmcnt(3)\n" \
    "v_mfma_f32_32x32x16_bf16 v[96:111], a[8:11], v[40:43], v[96:111]\n" \
    "buffer_store_dwordx4 v[48:51], %[ov], %[rs], s41 offen offset:0 sc1\n" \
    "v_mfma_f32_32x32x16_bf16 v[112:127], a[120:123], v[40:43], v[112:127]\n" \
    "ds_read_b128 v[40:43], v61 offset:3072\n" \
    "s_waitcnt lgkmcnt(3)\n" \
    "v_mfma_f32_32x32x16_bf16 v[96:111], a[12:15], v[44:47], v[96:111]\n" \
    "v_cvt_pk_bf16_f32 v52, v72, v73\n" \
    "v_cvt_pk_bf16_f32 v53, v74, v75\n" \
    "v_cvt_pk_bf16_f32 v54, v76, v77\n" \
    "v_cvt_pk_bf16_f32 v55, v78, v79\n" \
    "v_mfma_f32_32x32x16_bf16 v[112:127], a[124:127], v[44:47], v[112:127]\n" \
    "ds_read_b128 v[44:47], v61 offset:3584\n" \
    "s_waitcnt lgkmcnt(3)\n" \
    "v_mfma_f32_32x32x16_bf16 v[96:111], a[16:19], v[32:35], v[96:111]\n" \
    "buffer_store_dwordx4 v[52:55], %[ov], %[rs], s41 offen offset:512 sc1\n" \
    "v_mfma_f32_32x32x16_bf16 v[112:127], a[128:131], v[32:35], v[112:127]\n" \
    "ds_read_b128 v[32:35], v61 offset:4096\n" \
    "s_waitcnt lgkmcnt(3)\n" \
    "v_mfma_f32_32x32x16_bf16 v[96:111], a[20:23], v[36:39], v[96:111]\n" \
    "v_cvt_pk_bf16_f32 v48, v80, v81\n" \
    "v_cvt_pk_bf16_f32 v49, v82, v83\n" \
    "v_cvt_pk_bf16_f32 v50, v84, v85\n" \
    "v_cvt_pk_bf16_f32 v51, v86, v87\n" \
    "v_mfma_f32_32x32x16_bf16 v[112:127], a[132:135], v[36:39], v[112:127]\n" \
    "ds_read_b128 v[36:39], v61 offset:4608\n" \
    "s_waitcnt lgkmcnt(3)\n" \
    "v_mfma_f32_32x32x16_bf16 v[96:111], a[24:27], v[40:43], v[96:111]\n" \
    "buffer_store_dwordx4 v[48:51], %[ov], %[rs], s46 offen offset:0 sc1\n" \
    "v_mfma_f32_32x32x16_bf16 v[112:127], a[136:139], v[40:43], v[112:127]\n" \
    "ds_read_b128 v[40:43], v61 offset:5120\n" \
    "s_waitcnt lgkmcnt(3)\n" \
    "v_mfma_f32_32x32x16_bf16 v[96:111], a[28:31], v[44:47], v[96:111]\n" \
    "v_cvt_pk_bf16_f32 v52, v88, v89\n" \
    "v_cvt_pk_bf16_f32 v53, v90, v91\n" \
    "v_cvt_pk_bf16_f32 v54, v92, v93\n" \
    "v_cvt_pk_bf16_f32 v55, v94, v95\n" \
    "v_mfma_f32_32x32x16_bf16 v[112:127], a[140:143], v[44:47], v[112:127]\n" \
    "ds_read_b128 v[44:47], v61 offset:5632\n" \
    "s_waitcnt lgkmcnt(3)\n" \
    "v_mfma_f32_32x32x16_bf16 v[96:111], a[32:35], v[32:35], v[96:111]\n" \
    "buffer_store_dwordx4 v[52:55], %[ov], %[rs], s46 offen offset:512 sc1\n" \
    "v_mfma_f32_32x32x16_bf16 v[112:127], a[144:147], v[32:35], v[112:127]\n" \
    "ds_read_b128 v[32:35], v61 offset:6144\n" \
    "s_waitcnt lgkmcnt(3)\n" \
    "v_mfma_f32_32x32x16_bf16 v[96:111], a[36:39], v[36:39], v[96:111]\n" \
    "s_add_u32 s54, s54, 1\n" \
    "s_lshr_b32 s55, s54, 1\n" \
    "s_xor_b32 s55, s55, %[pol]\n" \
    "s_and_b32 s55, s55, 1\n" \
    "v_mfma_f32_32x32x16_bf16 v[112:127], a[148:151], v[36:39], v[112:127]\n" \
    "ds_read_b128 v[36:39], v61 offset:6656\n" \
    "s_waitcnt lgkmcnt(3)\n" \
    "v_mfma_f32_32x32x16_bf16 v[96:111], a[40:43], v[40:43], v[96:111]\n" \
    "v_mfma_f32_32x32x16_bf16 v[112:127], a[152:155], v[40:43], v[112:127]\n" \
    "ds_read_b128 v[40:43], v61 offset:7168\n" \
    "s_waitcnt lgkmcnt(3)\n" \
    "v_mfma_f32_32x32x16_bf16 v[96:111], a[44:47], v[44:47], v[96:111]\n" \
    "s_lshl_b32 s55, s55, 13\n" \
    "s_lshl_b32 s41, s54, 20\n" \
    "s_add_u32 s41, s41, s55\n" \
    "s_add_u32 s41, s41, %[out0]\n" \
    "v_mfma_f32_32x32x16_bf16 v[112:127], a[156:159], v[44:47], v[112:127]\n" \
    "ds_read_b128 v[44:47], v61 offset:7680\n" \
    "s_mov_b32 m0, s44\n" \
    "s_waitcnt lgkmcnt(3)\n" \
    "v_mfma_f32_32x32x16_bf16 v[96:111], a[48:51], v[32:35], v[96:111]\n" \
    "buffer_load_dwordx4 %[l16], %[rs], s43 offen offset:0 lds\n" \
    "v_mfma_f32_32x32x16_bf16 v[112:127], a[160:163], v[32:35], v[112:127]\n" \
    "ds_read_b128 v[32:35], v61 offset:8192\n" \
    "s_waitcnt lgkmcnt(3)\n" \
    "v_mfma_f32_32x32x16_bf16 v[96:111], a[52:55], v[36:39], v[96:111]\n" \
    "v_mfma_f32_32x32x16_bf16 v[112:127], a[164:167], v[36:39], v[112:127]\n" \
    "ds_read_b128 v[36:39], v61 offset:8704\n" \
    "s_waitcnt lgkmcnt(3)\n" \
    "v_mfma_f32_32x32x16_bf16 v[96:111], a[56:59], v[40:43], v[96:111]\n" \
    "buffer_load_dwordx4 %[l16], %[rs], s43 offen offset:1024 lds\n" \
    "v_mfma_f32_32x32x16_bf16 v[112:127], a[168:171], v[40:43], v[112:127]\n" \
    "ds_read_b128 v[40:43], v61 offset:9216\n" \
    "s_waitcnt lgkmcnt(3)\n" \
    "v_mfma_f32_32x32x16_bf16 v[96:111], a[60:63], v[44:47], v[96:111]\n" \
    "v_mfma_f32_32x32x16_bf16 v[112:127], a[172:175], v[44:47], v[112:127]\n" \
    "ds_read_b128 v[44:47], v61 offset:9728\n" \
    "s_waitcnt lgkmcnt(3)\n" \
    "v_mfma_f32_32x32x16_bf16 v[96:111], a[64:67], v[32:35], v[96:111]\n" \
    "buffer_load_dwordx4 %[l16], %[rs], s43 offen offset:2048 lds\n" \
    "v_mfma_f32_32x32x16_bf16 v[112:127], a[176:179], v[32:35], v[112:127]\n" \
    "ds_read_b128 v[32:35], v61 offset:10240\n" \
    "s_waitcnt lgkmcnt(3)\n" \
    "v_mfma_f32_32x32x16_bf16 v[96:111], a[68:71], v[36:39], v[96:111]\n" \
    "v_mfma_f32_32x32x16_bf16 v[112:127], a[180:183], v[36:39], v[112:127]\n" \
    "ds_read_b128 v[36:39], v61 offset:10752\n" \
    "s_waitcnt lgkmcnt(3)\n" \
    "v_mfma_f32_32x32x16_bf16 v[96:111], a[72:75], v[40:43], v[96:111]\n" \
    "buffer_load_dwordx4 %[l16], %[rs], s43 offen offset:3072 lds\n" \
    "v_mfma_f32_32x32x16_bf16 v[112:127], a[184:187], v[40:43], v[112:127]\n" \
    "ds_read_b128 v[40:43], v61 offset:11264\n" \
    "s_waitcnt lgkmcnt(3)\n" \
    "v_mfma_f32_32x32x16_bf16 v[96:111], a[76:79], v[44:47], v[96:111]\n" \
    "s_add_u32 s44, s44, 4096\n" \
    "s_add_u32 s43, s43, 4096\n" \
    "v_mfma_f32_32x32x16_bf16 v[112:127], a[188:191], v[44:47], v[112:127]\n" \
    "ds_read_b128 v[44:47], v61 offset:11776\n" \
    "s_waitcnt lgkmcnt(3)\n" \
    "v_mfma_f32_32x32x16_bf16 v[96:111], a[80:83], v[32:35], v[96:111]\n" \
    "v_mfma_f32_32x32x16_bf16 v[112:127], a[192:195], v[32:35], v[112:127]\n" \
    "ds_read_b128 v[32:35], v61 offset:12288\n" \
    "s_mov_b32 m0, s44\n" \
    "s_waitcnt lgkmcnt(3)\n" \
    "v_mfma_f32_32x32x16_bf16 v[96:111], a[84:87], v[36:39], v[96:111]\n" \
    "buffer_load_dwordx4 %[l16], %[rs], s43 offen offset:0 lds\n" \
    "v_mfma_f32_32x32x16_bf16 v[112:127], a[196:199], v[36:39], v[112:127]\n" \
    "ds_read_b128 v[36:39], v61 offset:12800\n" \
    "s_waitcnt lgkmcnt(3)\n" \
    "v_mfma_f32_32x32x16_bf16 v[96:111], a[88:91], v[40:43], v[96:111]\n" \
    "v_mfma_f32_32x32x16_bf16 v[112:127], a[200:203], v[40:43], v[112:127]\n" \
    "ds_read_b128 v[40:43], v61 offset:13312\n" \
    "s_waitcnt lgkmcnt(3)\n" \
    "v_mfma_f32_32x32x16_bf16 v[96:111], a[92:95], v[44:47], v[96:111]\n" \
    "buffer_load_dwordx4 %[l16], %[rs], s43 offen offset:1024 lds\n" \
    "v_mfma_f32_32x32x16_bf16 v[112:127], a[204:207], v[44:47], v[112:127]\n" \
    "ds_read_b128 v[44:47], v61 offset:13824\n" \
    "s_waitcnt lgkmcnt(3)\n" \
    "v_mfma_f32_32x32x16_bf16 v[96:111], a[96:99], v[32:35], v[96:111]\n" \
    "v_mov_b32 v60, s40\n" \
    "s_waitcnt vmcnt(6)\n" \
    "buffer_store_dword v60, %[fo], %[rs], 0 offen sc1\n" \
    "v_mfma_f32_32x32x16_bf16 v[112:127], a[208:211], v[32:35], v[112:127]\n" \
    "ds_read_b128 v[32:35], v62 offset:0\n" \
    "s_waitcnt lgkmcnt(3)\n" \
    "v_mfma_f32_32x32x16_bf16 v[96:111], a[100:103], v[36:39], v[96:111]\n" \
    "buffer_load_dwordx4 %[l16], %[rs], s43 offen offset:2048 lds\n" \
    "v_mfma_f32_32x32x16_bf16 v[112:127], a[212:215], v[36:39], v[112:127]\n" \
    "ds_read_b128 v[36:39], v62 offset:512\n" \
    "s_waitcnt lgkmcnt(3)\n" \
    "v_mfma_f32_32x32x16_bf16 v[96:111], a[104:107], v[40:43], v[96:111]\n" \
    "s_add_u32 s40, s40, 1\n" \
    "s_cmp_lt_u32 s40, 61\n" \
    "s_cselect_b32 s50, 28672, 0\n" \
    "s_add_u32 s43, s43, s50\n" \
    "v_mfma_f32_32x32x16_bf16 v[112:127], a[216:219], v[40:43], v[112:127]\n" \
    "ds_read_b128 v[40:43], v62 offset:1024\n" \
    "s_waitcnt lgkmcnt(3)\n" \
    "v_mfma_f32_32x32x16_bf16 v[96:111], a[108:111], v[44:47], v[96:111]\n" \
    "s_sub_u32 s43, s43, 4096\n" \
    "s_add_u32 s45, s45, 28672\n" \
    "s_cmp_eq_u32 s45, 114688\n" \
    "s_cselect_b32 s45, 0, s45\n" \
    "v_mfma_f32_32x32x16_bf16 v[112:127], a[220:223], v[44:47], v[112:127]\n" \
    "v_mov_b32 v61, v62\n" \
    "v_add_u32 v62, s45, %[xb]\n" \
    "s_add_u32 s44, s45, 57344\n" \
    "s_cmp_ge_u32 s44, 114688\n" \
    "s_cselect_b32 s50, 114688, 0\n" \
    "s_sub_u32 s44, s44, s50\n" \
    "s_add_u32 s44, s44, %[ld0]\n" \
    "s_cmp_lt_u32 s40, 64\n" \
    "s_cbranch_scc1 Gloop_%=\n" \
    "s_add_u32 s46, s41, 2048\n" \
    "s_nop 7\n" \
    "v_cvt_pk_bf16_f32 v48, v96, v97\n" \
    "v_cvt_pk_bf16_f32 v49, v98, v99\n" \
    "v_cvt_pk_bf16_f32 v50, v100, v101\n" \
    "v_cvt_pk_bf16_f32 v51, v102, v103\n" \
    "buffer_store_dwordx4 v[48:51], %[ov], %[rs], s41 offen offset:0 sc1\n" \
    "v_cvt_pk_bf16_f32 v52, v104, v105\n" \
    "v_cvt_pk_bf16_f32 v53, v106, v107\n" \
    "v_cvt_pk_bf16_f32 v54, v108, v109\n" \
    "v_cvt_pk_bf16_f32 v55, v110, v111\n" \
    "buffer_store_dwordx4 v[52:55], %[ov], %[rs], s41 offen offset:512 sc1\n" \
    "s_nop 1\n" \
    "v_cvt_pk_bf16_f32 v48, v112, v113\n" \
    "v_cvt_pk_bf16_f32 v49, v114, v115\n" \
    "v_cvt_pk_bf16_f32 v50, v116, v117\n" \
    "v_cvt_pk_bf16_f32 v51, v118, v119\n" \
    "buffer_store_dwordx4 v[48:51], %[ov], %[rs], s46 offen offset:0 sc1\n" \
    "v_cvt_pk_bf16_f32 v52, v120, v121\n" \
    "v_cvt_pk_bf16_f32 v53, v122, v123\n" \
    "v_cvt_pk_bf16_f32 v54, v124, v125\n" \
    "v_cvt_pk_bf16_f32 v55, v126, v127\n" \
    "buffer_store_dwordx4 v[52:55], %[ov], %[rs], s46 offen offset:512 sc1\n" \
    "v_mov_b32 v60, 64\n" \
    "s_waitcnt vmcnt(0)\n" \
    "buffer_store_dword v60, %[fo], %[rs], 0 offen sc1\n" \
    "s_add_u32 s54, s54, 1\n" \
    "s_lshr_b32 s55, s54, 1\n" \
    "s_xor_b32 s55, s55, %[pol]\n" \
    "s_and_b32 s55, s55, 1\n" \
    "s_lshl_b32 s55, s55, 13\n" \
    "s_lshl_b32 s41, s54, 20\n" \
    "s_add_u32 s41, s41, s55\n" \
    "s_add_u32 s41, s41, %[out0]\n" \
    "s_waitcnt vmcnt(0) lgkmcnt(0)\n"

#define ASM_GU \
    "s_nop 4\n" \
    "s_add_u32 s44, %[ld0], 0\n" \
    "s_add_u32 s43, %[so0], 0\n" \
    "s_mov_b32 m0, s44\n" \
    "s_nop 0\n" \
    "buffer_load_dwordx4 %[l16], %[rs], s43 offen offset:0 lds\n" \
    "buffer_load_dwordx4 %[l16], %[rs], s43 offen offset:1024 lds\n" \
    "buffer_load_dwordx4 %[l16], %[rs], s43 offen offset:2048 lds\n" \
    "buffer_load_dwordx4 %[l16], %[rs], s43 offen offset:3072 lds\n" \
    "s_add_u32 s44, %[ld0], 4096\n" \
    "s_add_u32 s43, %[so0], 4096\n" \
    "s_mov_b32 m0, s44\n" \
    "s_nop 0\n" \
    "buffer_load_dwordx4 %[l16], %[rs], s43 offen offset:0 lds\n" \
    "buffer_load_dwordx4 %[l16], %[rs], s43 offen offset:1024 lds\n" \
    "buffer_load_dwordx4 %[l16], %[rs], s43 offen offset:2048 lds\n" \
    "s_add_u32 s44, %[ld0], 28672\n" \
    "s_add_u32 s43, %[so0], 28672\n" \
    "s_mov_b32 m0, s44\n" \
    "s_nop 0\n" \
    "buffer_load_dwordx4 %[l16], %[rs], s43 offen offset:0 lds\n" \
    "buffer_load_dwordx4 %[l16], %[rs], s43 offen offset:1024 lds\n" \
    "buffer_load_dwordx4 %[l16], %[rs], s43 offen offset:2048 lds\n" \
    "buffer_load_dwordx4 %[l16], %[rs], s43 offen offset:3072 lds\n" \
    "s_add_u32 s44, %[ld0], 32768\n" \
    "s_add_u32 s43, %[so0], 32768\n" \
    "s_mov_b32 m0, s44\n" \
    "s_nop 0\n" \
    "buffer_load_dwordx4 %[l16], %[rs], s43 offen offset:0 lds\n" \
    "buffer_load_dwordx4 %[l16], %[rs], s43 offen offset:1024 lds\n" \
    "buffer_load_dwordx4 %[l16], %[rs], s43 offen offset:2048 lds\n" \
    "s_add_u32 s44, %[ld0], 57344\n" \
    "s_add_u32 s43, %[so0], 57344\n" \
    "s_mov_b32 m0, s44\n" \
    "s_nop 0\n" \
    "buffer_load_dwordx4 %[l16], %[rs], s43 offen offset:0 lds\n" \
    "buffer_load_dwordx4 %[l16], %[rs], s43 offen offset:1024 lds\n" \
    "buffer_load_dwordx4 %[l16], %[rs], s43 offen offset:2048 lds\n" \
    "buffer_load_dwordx4 %[l16], %[rs], s43 offen offset:3072 lds\n" \
    "s_add_u32 s44, %[ld0], 61440\n" \
    "s_add_u32 s43, %[so0], 61440\n" \
    "s_mov_b32 m0, s44\n" \
    "s_nop 0\n" \
    "buffer_load_dwordx4 %[l16], %[rs], s43 offen offset:0 lds\n" \
    "buffer_load_dwordx4 %[l16], %[rs], s43 offen offset:1024 lds\n" \
    "buffer_load_dwordx4 %[l16], %[rs], s43 offen offset:2048 lds\n" \
    "s_add_u32 s50, %[woff], 0\n" \
    "buffer_load_dwordx4 a[0:3], %[wv], %[rs], s50 offen offset:0\n" \
    "s_add_u32 s51, %[woff], 28672\n" \
    "buffer_load_dwordx4 a[112:115], %[wv], %[rs], s51 offen offset:0\n" \
    "buffer_load_dwordx4 a[4:7], %[wv], %[rs], s50 offen offset:512\n" \
    "buffer_load_dwordx4 a[116:119], %[wv], %[rs], s51 offen offset:512\n" \
    "buffer_load_dwordx4 a[8:11], %[wv], %[rs], s50 offen offset:1024\n" \
    "buffer_load_dwordx4 a[120:123], %[wv], %[rs], s51 offen offset:1024\n" \
    "buffer_load_dwordx4 a[12:15], %[wv], %[rs], s50 offen offset:1536\n" \
    "buffer_load_dwordx4 a[124:127], %[wv], %[rs], s51 offen offset:1536\n" \
    "buffer_load_dwordx4 a[16:19], %[wv], %[rs], s50 offen offset:2048\n" \
    "buffer_load_dwordx4 a[128:131], %[wv], %[rs], s51 offen offset:2048\n" \
    "buffer_load_dwordx4 a[20:23], %[wv], %[rs], s50 offen offset:2560\n" \
    "buffer_load_dwordx4 a[132:135], %[wv], %[rs], s51 offen offset:2560\n" \
    "buffer_load_dwordx4 a[24:27], %[wv], %[rs], s50 offen offset:3072\n" \
    "buffer_load_dwordx4 a[136:139], %[wv], %[rs], s51 offen offset:3072\n" \
    "buffer_load_dwordx4 a[28:31], %[wv], %[rs], s50 offen offset:3584\n" \
    "buffer_load_dwordx4 a[140:143], %[wv], %[rs], s51 offen offset:3584\n" \
    "s_add_u32 s50, %[woff], 4096\n" \
    "buffer_load_dwordx4 a[32:35], %[wv], %[rs], s50 offen offset:0\n" \
    "s_add_u32 s51, %[woff], 32768\n" \
    "buffer_load_dwordx4 a[144:147], %[wv], %[rs], s51 offen offset:0\n" \
    "buffer_load_dwordx4 a[36:39], %[wv], %[rs], s50 offen offset:512\n" \
    "buffer_load_dwordx4 a[148:151], %[wv], %[rs], s51 offen offset:512\n" \
    "buffer_load_dwordx4 a[40:43], %[wv], %[rs], s50 offen offset:1024\n" \
    "buffer_load_dwordx4 a[152:155], %[wv], %[rs], s51 offen offset:1024\n" \
    "buffer_load_dwordx4 a[44:47], %[wv], %[rs], s50 offen offset:1536\n" \
    "buffer_load_dwordx4 a[156:159], %[wv], %[rs], s51 offen offset:1536\n" \
    "buffer_load_dwordx4 a[48:51], %[wv], %[rs], s50 offen offset:2048\n" \
    "buffer_load_dwordx4 a[160:163], %[wv], %[rs], s51 offen offset:2048\n" \
    "buffer_load_dwordx4 a[52:55], %[wv], %[rs], s50 offen offset:2560\n" \
    "buffer_load_dwordx4 a[164:167], %[wv], %[rs], s51 offen offset:2560\n" \
    "buffer_load_dwordx4 a[56:59], %[wv], %[rs], s50 offen offset:3072\n" \
    "buffer_load_dwordx4 a[168:171], %[wv], %[rs], s51 offen offset:3072\n" \
    "buffer_load_dwordx4 a[60:63], %[wv], %[rs], s50 offen offset:3584\n" \
    "buffer_load_dwordx4 a[172:175], %[wv], %[rs], s51 offen offset:3584\n" \
    "s_add_u32 s50, %[woff], 8192\n" \
    "buffer_load_dwordx4 a[64:67], %[wv], %[rs], s50 offen offset:0\n" \
    "s_add_u32 s51, %[woff], 36864\n" \
    "buffer_load_dwordx4 a[176:179], %[wv], %[rs], s51 offen offset:0\n" \
    "buffer_load_dwordx4 a[68:71], %[wv], %[rs], s50 offen offset:512\n" \
    "buffer_load_dwordx4 a[180:183], %[wv], %[rs], s51 offen offset:512\n" \
    "buffer_load_dwordx4 a[72:75], %[wv], %[rs], s50 offen offset:1024\n" \
    "buffer_load_dwordx4 a[184:187], %[wv], %[rs], s51 offen offset:1024\n" \
    "buffer_load_dwordx4 a[76:79], %[wv], %[rs], s50 offen offset:1536\n" \
    "buffer_load_dwordx4 a[188:191], %[wv], %[rs], s51 offen offset:1536\n" \
    "buffer_load_dwordx4 a[80:83], %[wv], %[rs], s50 offen offset:2048\n" \
    "buffer_load_dwordx4 a[192:195], %[wv], %[rs], s51 offen offset:2048\n" \
    "buffer_load_dwordx4 a[84:87], %[wv], %[rs], s50 offen offset:2560\n" \
    "buffer_load_dwordx4 a[196:199], %[wv], %[rs], s51 offen offset:2560\n" \
    "buffer_load_dwordx4 a[88:91], %[wv], %[rs], s50 offen offset:3072\n" \
    "buffer_load_dwordx4 a[200:203], %[wv], %[rs], s51 offen offset:3072\n" \
    "buffer_load_dwordx4 a[92:95], %[wv], %[rs], s50 offen offset:3584\n" \
    "buffer_load_dwordx4 a[204:207], %[wv], %[rs], s51 offen offset:3584\n" \
    "s_add_u32 s50, %[woff], 12288\n" \
    "buffer_load_dwordx4 a[96:99], %[wv], %[rs], s50 offen offset:0\n" \
    "s_add_u32 s51, %[woff], 40960\n" \
    "buffer_load_dwordx4 a[208:211], %[wv], %[rs], s51 offen offset:0\n" \
    "buffer_load_dwordx4 a[100:103], %[wv], %[rs], s50 offen offset:512\n" \
    "buffer_load_dwordx4 a[212:215], %[wv], %[rs], s51 offen offset:512\n" \
    "buffer_load_dwordx4 a[104:107], %[wv], %[rs], s50 offen offset:1024\n" \
    "buffer_load_dwordx4 a[216:219], %[wv], %[rs], s51 offen offset:1024\n" \
    "buffer_load_dwordx4 a[108:111], %[wv], %[rs], s50 offen offset:1536\n" \
    "buffer_load_dwordx4 a[220:223], %[wv], %[rs], s51 offen offset:1536\n" \
    "s_mov_b32 s40, 0\n" \
    "s_mov_b32 s54, 0\n" \
    "s_lshl_b32 s41, %[pol], 13\n" \
    "s_add_u32 s41, s41, %[out0]\n" \
    "v_mov_b32 v61, %[xb]\n" \
    "v_add_u32 v62, 28672, %[xb]\n" \
    "s_mov_b32 s45, 28672\n" \
    "s_add_u32 s44, %[ld0], 86016\n" \
    "s_add_u32 s43, %[so0], 86016\n" \
    "s_waitcnt vmcnt(0)\n" \
    "s_barrier\n" \
    "ds_read_b128 v[32:35], v61 offset:0\n" \
    "ds_read_b128 v[36:39], v61 offset:512\n" \
    "ds_read_b128 v[40:43], v61 offset:1024\n" \
    "s_mov_b32 s53, 0\n" \
    "ds_read_b128 v[44:47], v61 offset:1536\n" \
    "s_waitcnt lgkmcnt(3)\n" \
    "v_mfma_f32_32x32x16_bf16 v[64:79], v[32:35], a[0:3], 0\n" \
    "s_waitcnt vmcnt(8)\n" \
    "s_barrier\n" \
    "v_mfma_f32_32x32x16_bf16 v[80:95], v[32:35], a[112:115], 0\n" \
    "ds_read_b128 v[32:35], v61 offset:2048\n" \
    "s_waitcnt lgkmcnt(3)\n" \
    "v_mfma_f32_32x32x16_bf16 v[64:79], v[36:39], a[4:7], v[64:79]\n" \
    "v_mfma_f32_32x32x16_bf16 v[80:95], v[36:39], a[116:119], v[80:95]\n" \
    "ds_read_b128 v[36:39], v61 offset:2560\n" \
    "s_waitcnt lgkmcnt(3)\n" \
    "v_mfma_f32_32x32x16_bf16 v[64:79], v[40:43], a[8:11], v[64:79]\n" \
    "v_mfma_f32_32x32x16_bf16 v[80:95], v[40:43], a[120:123], v[80:95]\n" \
    "ds_read_b128 v[40:43], v61 offset:3072\n" \
    "s_waitcnt lgkmcnt(3)\n" \
    "v_mfma_f32_32x32x16_bf16 v[64:79], v[44:47], a[12:15], v[64:79]\n" \
    "v_mfma_f32_32x32x16_bf16 v[80:95], v[44:47], a[124:127], v[80:95]\n" \
    "ds_read_b128 v[44:47], v61 offset:3584\n" \
    "s_waitcnt lgkmcnt(3)\n" \
    "v_mfma_f32_32x32x16_bf16 v[64:79], v[32:35], a[16:19], v[64:79]\n" \
    "v_mfma_f32_32x32x16_bf16 v[80:95], v[32:35], a[128:131], v[80:95]\n" \
    "ds_read_b128 v[32:35], v61 offset:4096\n" \
    "s_waitcnt lgkmcnt(3)\n" \
    "v_mfma_f32_32x32x16_bf16 v[64:79], v[36:39], a[20:23], v[64:79]\n" \
    "v_mfma_f32_32x32x16_bf16 v[80:95], v[36:39], a[132:135], v[80:95]\n" \
    "ds_read_b128 v[36:39], v61 offset:4608\n" \
    "s_waitcnt lgkmcnt(3)\n" \
    "v_mfma_f32_32x32x16_bf16 v[64:79], v[40:43], a[24:27], v[64:79]\n" \
    "v_mfma_f32_32x32x16_bf16 v[80:95], v[40:43], a[136:139], v[80:95]\n" \
    "ds_read_b128 v[40:43], v61 offset:5120\n" \
    "s_waitcnt lgkmcnt(3)\n" \
    "v_mfma_f32_32x32x16_bf16 v[64:79], v[44:47], a[28:31], v[64:79]\n" \
    "v_mfma_f32_32x32x16_bf16 v[80:95], v[44:47], a[140:143], v[80:95]\n" \
    "ds_read_b128 v[44:47], v61 offset:5632\n" \
    "s_waitcnt lgkmcnt(3)\n" \
    "v_mfma_f32_32x32x16_bf16 v[64:79], v[32:35], a[32:35], v[64:79]\n" \
    "v_mfma_f32_32x32x16_bf16 v[80:95], v[32:35], a[144:147], v[80:95]\n" \
    "ds_read_b128 v[32:35], v61 offset:6144\n" \
    "s_waitcnt lgkmcnt(3)\n" \
    "v_mfma_f32_32x32x16_bf16 v[64:79], v[36:39], a[36:39], v[64:79]\n" \
    "v_mfma_f32_32x32x16_bf16 v[80:95], v[36:39], a[148:151], v[80:95]\n" \
    "ds_read_b128 v[36:39], v61 offset:6656\n" \
    "s_waitcnt lgkmcnt(3)\n" \
    "v_mfma_f32_32x32x16_bf16 v[64:79], v[40:43], a[40:43], v[64:79]\n" \
    "v_mfma_f32_32x32x16_bf16 v[80:95], v[40:43], a[152:155], v[80:95]\n" \
    "ds_read_b128 v[40:43], v61 offset:7168\n" \
    "s_waitcnt lgkmcnt(3)\n" \
    "v_mfma_f32_32x32x16_bf16 v[64:79], v[44:47], a[44:47], v[64:79]\n" \
    "v_mfma_f32_32x32x16_bf16 v[80:95], v[44:47], a[156:159], v[80:95]\n" \
    "ds_read_b128 v[44:47], v61 offset:7680\n" \
    "s_mov_b32 m0, s44\n" \
    "s_waitcnt lgkmcnt(3)\n" \
    "v_mfma_f32_32x32x16_bf16 v[64:79], v[32:35], a[48:51], v[64:79]\n" \
    "buffer_load_dwordx4 %[l16], %[rs], s43 offen offset:0 lds\n" \
    "v_mfma_f32_32x32x16_bf16 v[80:95], v[32:35], a[160:163], v[80:95]\n" \
    "ds_read_b128 v[32:35], v61 offset:8192\n" \
    "s_waitcnt lgkmcnt(3)\n" \
    "v_mfma_f32_32x32x16_bf16 v[64:79], v[36:39], a[52:55], v[64:79]\n" \
    "v_mfma_f32_32x32x16_bf16 v[80:95], v[36:39], a[164:167], v[80:95]\n" \
    "ds_read_b128 v[36:39], v61 offset:8704\n" \
    "s_waitcnt lgkmcnt(3)\n" \
    "v_mfma_f32_32x32x16_bf16 v[64:79], v[40:43], a[56:59], v[64:79]\n" \
    "buffer_load_dwordx4 %[l16], %[rs], s43 offen offset:1024 lds\n" \
    "v_mfma_f32_32x32x16_bf16 v[80:95], v[40:43], a[168:171], v[80:95]\n" \
    "ds_read_b128 v[40:43], v61 offset:9216\n" \
    "s_waitcnt lgkmcnt(3)\n" \
    "v_mfma_f32_32x32x16_bf16 v[64:79], v[44:47], a[60:63], v[64:79]\n" \
    "v_mfma_f32_32x32x16_bf16 v[80:95], v[44:47], a[172:175], v[80:95]\n" \
    "ds_read_b128 v[44:47], v61 offset:9728\n" \
    "s_waitcnt lgkmcnt(3)\n" \
    "v_mfma_f32_32x32x16_bf16 v[64:79], v[32:35], a[64:67], v[64:79]\n" \
    "buffer_load_dwordx4 %[l16], %[rs], s43 offen offset:2048 lds\n" \
    "v_mfma_f32_32x32x16_bf16 v[80:95], v[32:35], a[176:179], v[80:95]\n" \
    "ds_read_b128 v[32:35], v61 offset:10240\n" \
    "s_waitcnt lgkmcnt(3)\n" \
    "v_mfma_f32_32x32x16_bf16 v[64:79], v[36:39], a[68:71], v[64:79]\n" \
    "v_mfma_f32_32x32x16_bf16 v[80:95], v[36:39], a[180:183], v[80:95]\n" \
    "ds_read_b128 v[36:39], v61 offset:10752\n" \
    "s_waitcnt lgkmcnt(3)\n" \
    "v_mfma_f32_32x32x16_bf16 v[64:79], v[40:43], a[72:75], v[64:79]\n" \
    "buffer_load_dwordx4 %[l16], %[rs], s43 offen offset:3072 lds\n" \
    "v_mfma_f32_32x32x16_bf16 v[80:95], v[40:43], a[184:187], v[80:95]\n" \
    "ds_read_b128 v[40:43], v61 offset:11264\n" \
    "s_waitcnt lgkmcnt(3)\n" \
    "v_mfma_f32_32x32x16_bf16 v[64:79], v[44:47], a[76:79], v[64:79]\n" \
    "s_add_u32 s44, s44, 4096\n" \
    "s_add_u32 s43, s43, 4096\n" \
    "v_mfma_f32_32x32x16_bf16 v[80:95], v[44:47], a[188:191], v[80:95]\n" \
    "ds_read_b128 v[44:47], v61 offset:11776\n" \
    "s_waitcnt lgkmcnt(3)\n" \
    "v_mfma_f32_32x32x16_bf16 v[64:79], v[32:35], a[80:83], v[64:79]\n" \
    "v_mfma_f32_32x32x16_bf16 v[80:95], v[32:35], a[192:195], v[80:95]\n" \
    "ds_read_b128 v[32:35], v61 offset:12288\n" \
    "s_mov_b32 m0, s44\n" \
    "s_waitcnt lgkmcnt(3)\n" \
    "v_mfma_f32_32x32x16_bf16 v[64:79], v[36:39], a[84:87], v[64:79]\n" \
    "buffer_load_dwordx4 %[l16], %[rs], s43 offen offset:0 lds\n" \
    "v_mfma_f32_32x32x16_bf16 v[80:95], v[36:39], a[196:199], v[80:95]\n" \
    "ds_read_b128 v[36:39], v61 offset:12800\n" \
    "s_waitcnt lgkmcnt(3)\n" \
    "v_mfma_f32_32x32x16_bf16 v[64:79], v[40:43], a[88:91], v[64:79]\n" \
    "v_mfma_f32_32x32x16_bf16 v[80:95], v[40:43], a[200:203], v[80:95]\n" \
    "ds_read_b128 v[40:43], v61 offset:13312\n" \
    "s_waitcnt lgkmcnt(3)\n" \
    "v_mfma_f32_32x32x16_bf16 v[64:79], v[44:47], a[92:95], v[64:79]\n" \
    "buffer_load_dwordx4 %[l16], %[rs], s43 offen offset:1024 lds\n" \
    "v_mfma_f32_32x32x16_bf16 v[80:95], v[44:47], a[204:207], v[80:95]\n" \
    "ds_read_b128 v[44:47], v61 offset:13824\n" \
    "s_waitcnt lgkmcnt(3)\n" \
    "v_mfma_f32_32x32x16_bf16 v[64:79], v[32:35], a[96:99], v[64:79]\n" \
    "v_mfma_f32_32x32x16_bf16 v[80:95], v[32:35], a[208:211], v[80:95]\n" \
    "ds_read_b128 v[32:35], v62 offset:0\n" \
    "s_waitcnt lgkmcnt(3)\n" \
    "v_mfma_f32_32x32x16_bf16 v[64:79], v[36:39], a[100:103], v[64:79]\n" \
    "buffer_load_dwordx4 %[l16], %[rs], s43 offen offset:2048 lds\n" \
    "v_mfma_f32_32x32x16_bf16 v[80:95], v[36:39], a[212:215], v[80:95]\n" \
    "ds_read_b128 v[36:39], v62 offset:512\n" \
    "s_waitcnt lgkmcnt(3)\n" \
    "v_mfma_f32_32x32x16_bf16 v[64:79], v[40:43], a[104:107], v[64:79]\n" \
    "s_add_u32 s40, s40, 1\n" \
    "s_cmp_lt_u32 s40, 61\n" \
    "s_cselect_b32 s50, 28672, 0\n" \
    "s_add_u32 s43, s43, s50\n" \
    "v_mfma_f32_32x32x16_bf16 v[80:95], v[40:43], a[216:219], v[80:95]\n" \
    "ds_read_b128 v[40:43], v62 offset:1024\n" \
    "s_waitcnt lgkmcnt(3)\n" \
    "v_mfma_f32_32x32x16_bf16 v[64:79], v[44:47], a[108:111], v[64:79]\n" \
    "s_sub_u32 s43, s43, 4096\n" \
    "s_add_u32 s45, s45, 28672\n" \
    "s_cmp_eq_u32 s45, 114688\n" \
    "s_cselect_b32 s45, 0, s45\n" \
    "v_mfma_f32_32x32x16_bf16 v[80:95], v[44:47], a[220:223], v[80:95]\n" \
    "v_mov_b32 v61, v62\n" \
    "v_add_u32 v62, s45, %[xb]\n" \
    "s_add_u32 s44, s45, 57344\n" \
    "s_cmp_ge_u32 s44, 114688\n" \
    "s_cselect_b32 s50, 114688, 0\n" \
    "s_sub_u32 s44, s44, s50\n" \
    "s_add_u32 s44, s44, %[ld0]\n" \
    "s_add_u32 s46, s41, 65536\n" \
    "s_nop 7\n" \
    "v_cvt_pk_bf16_f32 v48, v64, v65\n" \
    "v_cvt_pk_bf16_f32 v49, v66, v67\n" \
    "v_cvt_pk_bf16_f32 v50, v68, v69\n" \
    "v_cvt_pk_bf16_f32 v51, v70, v71\n" \
    "buffer_store_dwordx4 v[48:51], %[ov], %[rs], s41 offen offset:0 sc1\n" \
    "v_cvt_pk_bf16_f32 v52, v72, v73\n" \
    "v_cvt_pk_bf16_f32 v53, v74, v75\n" \
    "v_cvt_pk_bf16_f32 v54, v76, v77\n" \
    "v_cvt_pk_bf16_f32 v55, v78, v79\n" \
    "buffer_store_dwordx4 v[52:55], %[ov], %[rs], s41 offen offset:512 sc1\n" \
    "s_nop 1\n" \
    "v_cvt_pk_bf16_f32 v48, v80, v81\n" \
    "v_cvt_pk_bf16_f32 v49, v82, v83\n" \
    "v_cvt_pk_bf16_f32 v50, v84, v85\n" \
    "v_cvt_pk_bf16_f32 v51, v86, v87\n" \
    "buffer_store_dwordx4 v[48:51], %[ov], %[rs], s46 offen offset:0 sc1\n" \
    "v_cvt_pk_bf16_f32 v52, v88, v89\n" \
    "v_cvt_pk_bf16_f32 v53, v90, v91\n" \
    "v_cvt_pk_bf16_f32 v54, v92, v93\n" \
    "v_cvt_pk_bf16_f32 v55, v94, v95\n" \
    "buffer_store_dwordx4 v[52:55], %[ov], %[rs], s46 offen offset:512 sc1\n" \
    "v_mov_b32 v60, 1\n" \
    "s_waitcnt vmcnt(0)\n" \
    "buffer_store_dword v60, %[fo], %[rs], 0 offen sc1\n" \
    "s_add_u32 s54, s54, 1\n" \
    "s_lshr_b32 s55, s54, 1\n" \
    "s_xor_b32 s55, s55, %[pol]\n" \
    "s_and_b32 s55, s55, 1\n" \
    "s_lshl_b32 s55, s55, 13\n" \
    "s_lshl_b32 s41, s54, 20\n" \
    "s_add_u32 s41, s41, s55\n" \
    "s_add_u32 s41, s41, %[out0]\n" \
    "s_mov_b32 s53, 1\n" \
    "ds_read_b128 v[44:47], v61 offset:1536\n" \
    "s_waitcnt lgkmcnt(3)\n" \
    "v_mfma_f32_32x32x16_bf16 v[96:111], v[32:35], a[0:3], 0\n" \
    "s_waitcnt vmcnt(8)\n" \
    "s_barrier\n" \
    "v_mfma_f32_32x32x16_bf16 v[112:127], v[32:35], a[112:115], 0\n" \
    "ds_read_b128 v[32:35], v61 offset:2048\n" \
    "s_waitcnt lgkmcnt(3)\n" \
    "v_mfma_f32_32x32x16_bf16 v[96:111], v[36:39], a[4:7], v[96:111]\n" \
    "v_mfma_f32_32x32x16_bf16 v[112:127], v[36:39], a[116:119], v[112:127]\n" \
    "ds_read_b128 v[36:39], v61 offset:2560\n" \
    "s_waitcnt lgkmcnt(3)\n" \
    "v_mfma_f32_32x32x16_bf16 v[96:111], v[40:43], a[8:11], v[96:111]\n" \
    "v_mfma_f32_32x32x16_bf16 v[112:127], v[40:43], a[120:123], v[112:127]\n" \
    "ds_read_b128 v[40:43], v61 offset:3072\n" \
    "s_waitcnt lgkmcnt(3)\n" \
    "v_mfma_f32_32x32x16_bf16 v[96:111], v[44:47], a[12:15], v[96:111]\n" \
    "v_mfma_f32_32x32x16_bf16 v[112:127], v[44:47], a[124:127], v[112:127]\n" \
    "ds_read_b128 v[44:47], v61 offset:3584\n" \
    "s_waitcnt lgkmcnt(3)\n" \
    "v_mfma_f32_32x32x16_bf16 v[96:111], v[32:35], a[16:19], v[96:111]\n" \
    "v_mfma_f32_32x32x16_bf16 v[112:127], v[32:35], a[128:131], v[112:127]\n" \
    "ds_read_b128 v[32:35], v61 offset:4096\n" \
    "s_waitcnt lgkmcnt(3)\n" \
    "v_mfma_f32_32x32x16_bf16 v[96:111], v[36:39], a[20:23], v[96:111]\n" \
    "v_mfma_f32_32x32x16_bf16 v[112:127], v[36:39], a[132:135], v[112:127]\n" \
    "ds_read_b128 v[36:39], v61 offset:4608\n" \
    "s_waitcnt lgkmcnt(3)\n" \
    "v_mfma_f32_32x32x16_bf16 v[96:111], v[40:43], a[24:27], v[96:111]\n" \
    "v_mfma_f32_32x32x16_bf16 v[112:127], v[40:43], a[136:139], v[112:127]\n" \
    "ds_read_b128 v[40:43], v61 offset:5120\n" \
    "s_waitcnt lgkmcnt(3)\n" \
    "v_mfma_f32_32x32x16_bf16 v[96:111], v[44:47], a[28:31], v[96:111]\n" \
    "v_mfma_f32_32x32x16_bf16 v[112:127], v[44:47], a[140:143], v[112:127]\n" \
    "ds_read_b128 v[44:47], v61 offset:5632\n" \
    "s_waitcnt lgkmcnt(3)\n" \
    "v_mfma_f32_32x32x16_bf16 v[96:111], v[32:35], a[32:35], v[96:111]\n" \
    "v_mfma_f32_32x32x16_bf16 v[112:127], v[32:35], a[144:147], v[112:127]\n" \
    "ds_read_b128 v[32:35], v61 offset:6144\n" \
    "s_waitcnt lgkmcnt(3)\n" \
    "v_mfma_f32_32x32x16_bf16 v[96:111], v[36:39], a[36:39], v[96:111]\n" \
    "v_mfma_f32_32x32x16_bf16 v[112:127], v[36:39], a[148:151], v[112:127]\n" \
    "ds_read_b128 v[36:39], v61 offset:6656\n" \
    "s_waitcnt lgkmcnt(3)\n" \
    "v_mfma_f32_32x32x16_bf16 v[96:111], v[40:43], a[40:43], v[96:111]\n" \
    "v_mfma_f32_32x32x16_bf16 v[112:127], v[40:43], a[152:155], v[112:127]\n" \
    "ds_read_b128 v[40:43], v61 offset:7168\n" \
    "s_waitcnt lgkmcnt(3)\n" \
    "v_mfma_f32_32x32x16_bf16 v[96:111], v[44:47], a[44:47], v[96:111]\n" \
    "v_mfma_f32_32x32x16_bf16 v[112:127], v[44:47], a[156:159], v[112:127]\n" \
    "ds_read_b128 v[44:47], v61 offset:7680\n" \
    "s_mov_b32 m0, s44\n" \
    "s_waitcnt lgkmcnt(3)\n" \
    "v_mfma_f32_32x32x16_bf16 v[96:111], v[32:35], a[48:51], v[96:111]\n" \
    "buffer_load_dwordx4 %[l16], %[rs], s43 offen offset:0 lds\n" \
    "v_mfma_f32_32x32x16_bf16 v[112:127], v[32:35], a[160:163], v[112:127]\n" \
    "ds_read_b128 v[32:35], v61 offset:8192\n" \
    "s_waitcnt lgkmcnt(3)\n" \
    "v_mfma_f32_32x32x16_bf16 v[96:111], v[36:39], a[52:55], v[96:111]\n" \
    "v_mfma_f32_32x32x16_bf16 v[112:127], v[36:39], a[164:167], v[112:127]\n" \
    "ds_read_b128 v[36:39], v61 offset:8704\n" \
    "s_waitcnt lgkmcnt(3)\n" \
    "v_mfma_f32_32x32x16_bf16 v[96:111], v[40:43], a[56:59], v[96:111]\n" \
    "buffer_load_dwordx4 %[l16], %[rs], s43 offen offset:1024 lds\n" \
    "v_mfma_f32_32x32x16_bf16 v[112:127], v[40:43], a[168:171], v[112:127]\n" \
    "ds_read_b128 v[40:43], v61 offset:9216\n" \
    "s_waitcnt lgkmcnt(3)\n" \
    "v_mfma_f32_32x32x16_bf16 v[96:111], v[44:47], a[60:63], v[96:111]\n" \
    "v_mfma_f32_32x32x16_bf16 v[112:127], v[44:47], a[172:175], v[112:127]\n" \
    "ds_read_b128 v[44:47], v61 offset:9728\n" \
    "s_waitcnt lgkmcnt(3)\n" \
    "v_mfma_f32_32x32x16_bf16 v[96:111], v[32:35], a[64:67], v[96:111]\n" \
    "buffer_load_dwordx4 %[l16], %[rs], s43 offen offset:2048 lds\n" \
    "v_mfma_f32_32x32x16_bf16 v[112:127], v[32:35], a[176:179], v[112:127]\n" \
    "ds_read_b128 v[32:35], v61 offset:10240\n" \
    "s_waitcnt lgkmcnt(3)\n" \
    "v_mfma_f32_32x32x16_bf16 v[96:111], v[36:39], a[68:71], v[96:111]\n" \
    "v_mfma_f32_32x32x16_bf16 v[112:127], v[36:39], a[180:183], v[112:127]\n" \
    "ds_read_b128 v[36:39], v61 offset:10752\n" \
    "s_waitcnt lgkmcnt(3)\n" \
    "v_mfma_f32_32x32x16_bf16 v[96:111], v[40:43], a[72:75], v[96:111]\n" \
    "buffer_load_dwordx4 %[l16], %[rs], s43 offen offset:3072 lds\n" \
    "v_mfma_f32_32x32x16_bf16 v[112:127], v[40:43], a[184:187], v[112:127]\n" \
    "ds_read_b128 v[40:43], v61 offset:11264\n" \
    "s_waitcnt lgkmcnt(3)\n" \
    "v_mfma_f32_32x32x16_bf16 v[96:111], v[44:47], a[76:79], v[96:111]\n" \
    "s_add_u32 s44, s44, 4096\n" \
    "s_add_u32 s43, s43, 4096\n" \
    "v_mfma_f32_32x32x16_bf16 v[112:127], v[44:47], a[188:191], v[112:127]\n" \
    "ds_read_b128 v[44:47], v61 offset:11776\n" \
    "s_waitcnt lgkmcnt(3)\n" \
    "v_mfma_f32_32x32x16_bf16 v[96:111], v[32:35], a[80:83], v[96:111]\n" \
    "v_mfma_f32_32x32x16_bf16 v[112:127], v[32:35], a[192:195], v[112:127]\n" \
    "ds_read_b128 v[32:35], v61 offset:12288\n" \
    "s_mov_b32 m0, s44\n" \
    "s_waitcnt lgkmcnt(3)\n" \
    "v_mfma_f32_32x32x16_bf16 v[96:111], v[36:39], a[84:87], v[96:111]\n" \
    "buffer_load_dwordx4 %[l16], %[rs], s43 offen offset:0 lds\n" \
    "v_mfma_f32_32x32x16_bf16 v[112:127], v[36:39], a[196:199], v[112:127]\n" \
    "ds_read_b128 v[36:39], v61 offset:12800\n" \
    "s_waitcnt lgkmcnt(3)\n" \
    "v_mfma_f32_32x32x16_bf16 v[96:111], v[40:43], a[88:91], v[96:111]\n" \
    "v_mfma_f32_32x32x16_bf16 v[112:127], v[40:43], a[200:203], v[112:127]\n" \
    "ds_read_b128 v[40:43], v61 offset:13312\n" \
    "s_waitcnt lgkmcnt(3)\n" \
    "v_mfma_f32_32x32x16_bf16 v[96:111], v[44:47], a[92:95], v[96:111]\n" \
    "buffer_load_dwordx4 %[l16], %[rs], s43 offen offset:1024 lds\n" \
    "v_mfma_f32_32x32x16_bf16 v[112:127], v[44:47], a[204:207], v[112:127]\n" \
    "ds_read_b128 v[44:47], v61 offset:13824\n" \
    "s_waitcnt lgkmcnt(3)\n" \
    "v_mfma_f32_32x32x16_bf16 v[96:111], v[32:35], a[96:99], v[96:111]\n" \
    "v_mfma_f32_32x32x16_bf16 v[112:127], v[32:35], a[208:211], v[112:127]\n" \
    "ds_read_b128 v[32:35], v62 offset:0\n" \
    "s_waitcnt lgkmcnt(3)\n" \
    "v_mfma_f32_32x32x16_bf16 v[96:111], v[36:39], a[100:103], v[96:111]\n" \
    "buffer_load_dwordx4 %[l16], %[rs], s43 offen offset:2048 lds\n" \
    "v_mfma_f32_32x32x16_bf16 v[112:127], v[36:39], a[212:215], v[112:127]\n" \
    "ds_read_b128 v[36:39], v62 offset:512\n" \
    "s_waitcnt lgkmcnt(3)\n" \
    "v_mfma_f32_32x32x16_bf16 v[96:111], v[40:43], a[104:107], v[96:111]\n" \
    "s_add_u32 s40, s40, 1\n" \
    "s_cmp_lt_u32 s40, 61\n" \
    "s_cselect_b32 s50, 28672, 0\n" \
    "s_add_u32 s43, s43, s50\n" \
    "v_mfma_f32_32x32x16_bf16 v[112:127], v[40:43], a[216:219], v[112:127]\n" \
    "ds_read_b128 v[40:43], v62 offset:1024\n" \
    "s_waitcnt lgkmcnt(3)\n" \
    "v_mfma_f32_32x32x16_bf16 v[96:111], v[44:47], a[108:111], v[96:111]\n" \
    "s_sub_u32 s43, s43, 4096\n" \
    "s_add_u32 s45, s45, 28672\n" \
    "s_cmp_eq_u32 s45, 114688\n" \
    "s_cselect_b32 s45, 0, s45\n" \
    "v_mfma_f32_32x32x16_bf16 v[112:127], v[44:47], a[220:223], v[112:127]\n" \
    "v_mov_b32 v61, v62\n" \
    "v_add_u32 v62, s45, %[xb]\n" \
    "s_add_u32 s44, s45, 57344\n" \
    "s_cmp_ge_u32 s44, 114688\n" \
    "s_cselect_b32 s50, 114688, 0\n" \
    "s_sub_u32 s44, s44, s50\n" \
    "s_add_u32 s44, s44, %[ld0]\n" \
    "Gloop_%=:\n" \
    "ds_read_b128 v[44:47], v61 offset:1536\n" \
    "s_waitcnt lgkmcnt(3)\n" \
    "v_mfma_f32_32x32x16_bf16 v[64:79], v[32:35], a[0:3], 0\n" \
    "s_waitcnt vmcnt(8)\n" \
    "s_barrier\n" \
    "v_mfma_f32_32x32x16_bf16 v[80:95], v[32:35], a[112:115], 0\n" \
    "ds_read_b128 v[32:35], v61 offset:2048\n" \
    "s_add_u32 s46, s41, 65536\n" \
    "s_waitcnt lgkmcnt(3)\n" \
    "v_mfma_f32_32x32x16_bf16 v[64:79], v[36:39], a[4:7], v[64:79]\n" \
    "v_cvt_pk_bf16_f32 v48, v96, v97\n" \
    "v_cvt_pk_bf16_f32 v49, v98, v99\n" \
    "v_cvt_pk_bf16_f32 v50, v100, v101\n" \
    "v_cvt_pk_bf16_f32 v51, v102, v103\n" \
    "v_mfma_f32_32x32x16_bf16 v[80:95], v[36:39], a[116:119], v[80:95]\n" \
    "ds_read_b128 v[36:39], v61 offset:2560\n" \
    "s_waitcnt lgkmcnt(3)\n" \
    "v_mfma_f32_32x32x16_bf16 v[64:79], v[40:43], a[8:11], v[64:79]\n" \
    "buffer_store_dwordx4 v[48:51], %[ov], %[rs], s41 offen offset:0 sc1\n" \
    "v_mfma_f32_32x32x16_bf16 v[80:95], v[40:43], a[120:123], v[80:95]\n" \
    "ds_read_b128 v[40:43], v61 offset:3072\n" \
    "s_waitcnt lgkmcnt(3)\n" \
    "v_mfma_f32_32x32x16_bf16 v[64:79], v[44:47], a[12:15], v[64:79]\n" \
    "v_cvt_pk_bf16_f32 v52, v104, v105\n" \
    "v_cvt_pk_bf16_f32 v53, v106, v107\n" \
    "v_cvt_pk_bf16_f32 v54, v108, v109\n" \
    "v_cvt_pk_bf16_f32 v55, v110, v111\n" \
    "v_mfma_f32_32x32x16_bf16 v[80:95], v[44:47], a[124:127], v[80:95]\n" \
    "ds_read_b128 v[44:47], v61 offset:3584\n" \
    "s_waitcnt lgkmcnt(3)\n" \
    "v_mfma_f32_32x32x16_bf16 v[64:79], v[32:35], a[16:19], v[64:79]\n" \
    "buffer_store_dwordx4 v[52:55], %[ov], %[rs], s41 offen offset:512 sc1\n" \
    "v_mfma_f32_32x32x16_bf16 v[80:95], v[32:35], a[128:131], v[80:95]\n" \
    "ds_read_b128 v[32:35], v61 offset:4096\n" \
    "s_waitcnt lgkmcnt(3)\n" \
    "v_mfma_f32_32x32x16_bf16 v[64:79], v[36:39], a[20:23], v[64:79]\n" \
    "v_cvt_pk_bf16_f32 v48, v112, v113\n" \
    "v_cvt_pk_bf16_f32 v49, v114, v115\n" \
    "v_cvt_pk_bf16_f32 v50, v116, v117\n" \
    "v_cvt_pk_bf16_f32 v51, v118, v119\n" \
    "v_mfma_f32_32x32x16_bf16 v[80:95], v[36:39], a[132:135], v[80:95]\n" \
    "ds_read_b128 v[36:39], v61 offset:4608\n" \
    "s_waitcnt lgkmcnt(3)\n" \
    "v_mfma_f32_32x32x16_bf16 v[64:79], v[40:43], a[24:27], v[64:79]\n" \
    "buffer_store_dwordx4 v[48:51], %[ov], %[rs], s46 offen offset:0 sc1\n" \
    "v_mfma_f32_32x32x16_bf16 v[80:95], v[40:43], a[136:139], v[80:95]\n" \
    "ds_read_b128 v[40:43], v61 offset:5120\n" \
    "s_waitcnt lgkmcnt(3)\n" \
    "v_mfma_f32_32x32x16_bf16 v[64:79], v[44:47], a[28:31], v[64:79]\n" \
    "v_cvt_pk_bf16_f32 v52, v120, v121\n" \
    "v_cvt_pk_bf16_f32 v53, v122, v123\n" \
    "v_cvt_pk_bf16_f32 v54, v124, v125\n" \
    "v_cvt_pk_bf16_f32 v55, v126, v127\n" \
    "v_mfma_f32_32x32x16_bf16 v[80:95], v[44:47], a[140:143], v[80:95]\n" \
    "ds_read_b128 v[44:47], v61 offset:5632\n" \
    "s_waitcnt lgkmcnt(3)\n" \
    "v_mfma_f32_32x32x16_bf16 v[64:79], v[32:35], a[32:35], v[64:79]\n" \
    "buffer_store_dwordx4 v[52:55], %[ov], %[rs], s46 offen offset:512 sc1\n" \
    "v_mfma_f32_32x32x16_bf16 v[80:95], v[32:35], a[144:147], v[80:95]\n" \
    "ds_read_b128 v[32:35], v61 offset:6144\n" \
    "s_waitcnt lgkmcnt(3)\n" \
    "v_mfma_f32_32x32x16_bf16 v[64:79], v[36:39], a[36:39], v[64:79]\n" \
    "s_add_u32 s54, s54, 1\n" \
    "s_lshr_b32 s55, s54, 1\n" \
    "s_xor_b32 s55, s55, %[pol]\n" \
    "s_and_b32 s55, s55, 1\n" \
    "v_mfma_f32_32x32x16_bf16 v[80:95], v[36:39], a[148:151], v[80:95]\n" \
    "ds_read_b128 v[36:39], v61 offset:6656\n" \
    "s_waitcnt lgkmcnt(3)\n" \
    "v_mfma_f32_32x32x16_bf16 v[64:79], v[40:43], a[40:43], v[64:79]\n" \
    "v_mfma_f32_32x32x16_bf16 v[80:95], v[40:43], a[152:155], v[80:95]\n" \
    "ds_read_b128 v[40:43], v61 offset:7168\n" \
    "s_waitcnt lgkmcnt(3)\n" \
    "v_mfma_f32_32x32x16_bf16 v[64:79], v[44:47], a[44:47], v[64:79]\n" \
    "s_lshl_b32 s55, s55, 13\n" \
    "s_lshl_b32 s41, s54, 20\n" \
    "s_add_u32 s41, s41, s55\n" \
    "s_add_u32 s41, s41, %[out0]\n" \
    "v_mfma_f32_32x32x16_bf16 v[80:95], v[44:47], a[156:159], v[80:95]\n" \
    "ds_read_b128 v[44:47], v61 offset:7680\n" \
    "s_mov_b32 m0, s44\n" \
    "s_waitcnt lgkmcnt(3)\n" \
    "v_mfma_f32_32x32x16_bf16 v[64:79], v[32:35], a[48:51], v[64:79]\n" \
    "buffer_load_dwordx4 %[l16], %[rs], s43 offen offset:0 lds\n" \
    "v_mfma_f32_32x32x16_bf16 v[80:95], v[32:35], a[160:163], v[80:95]\n" \
    "ds_read_b128 v[32:35], v61 offset:8192\n" \
    "s_waitcnt lgkmcnt(3)\n" \
    "v_mfma_f32_32x32x16_bf16 v[64:79], v[36:39], a[52:55], v[64:79]\n" \
    "v_mfma_f32_32x32x16_bf16 v[80:95], v[36:39], a[164:167], v[80:95]\n" \
    "ds_read_b128 v[36:39], v61 offset:8704\n" \
    "s_waitcnt lgkmcnt(3)\n" \
    "v_mfma_f32_32x32x16_bf16 v[64:79], v[40:43], a[56:59], v[64:79]\n" \
    "buffer_load_dwordx4 %[l16], %[rs], s43 offen offset:1024 lds\n" \
    "v_mfma_f32_32x32x16_bf16 v[80:95], v[40:43], a[168:171], v[80:95]\n" \
    "ds_read_b128 v[40:43], v61 offset:9216\n" \
    "s_waitcnt lgkmcnt(3)\n" \
    "v_mfma_f32_32x32x16_bf16 v[64:79], v[44:47], a[60:63], v[64:79]\n" \
    "v_mfma_f32_32x32x16_bf16 v[80:95], v[44:47], a[172:175], v[80:95]\n" \
    "ds_read_b128 v[44:47], v61 offset:9728\n" \
    "s_waitcnt lgkmcnt(3)\n" \
    "v_mfma_f32_32x32x16_bf16 v[64:79], v[32:35], a[64:67], v[64:79]\n" \
    "buffer_load_dwordx4 %[l16], %[rs], s43 offen offset:2048 lds\n" \
    "v_mfma_f32_32x32x16_bf16 v[80:95], v[32:35], a[176:179], v[80:95]\n" \
    "ds_read_b128 v[32:35], v61 offset:10240\n" \
    "s_waitcnt lgkmcnt(3)\n" \
    "v_mfma_f32_32x32x16_bf16 v[64:79], v[36:39], a[68:71], v[64:79]\n" \
    "v_mfma_f32_32x32x16_bf16 v[80:95], v[36:39], a[180:183], v[80:95]\n" \
    "ds_read_b128 v[36:39], v61 offset:10752\n" \
    "s_waitcnt lgkmcnt(3)\n" \
    "v_mfma_f32_32x32x16_bf16 v[64:79], v[40:43], a[72:75], v[64:79]\n" \
    "buffer_load_dwordx4 %[l16], %[rs], s43 offen offset:3072 lds\n" \
    "v_mfma_f32_32x32x16_bf16 v[80:95], v[40:43], a[184:187], v[80:95]\n" \
    "ds_read_b128 v[40:43], v61 offset:11264\n" \
    "s_waitcnt lgkmcnt(3)\n" \
    "v_mfma_f32_32x32x16_bf16 v[64:79], v[44:47], a[76:79], v[64:79]\n" \
    "s_add_u32 s44, s44, 4096\n" \
    "s_add_u32 s43, s43, 4096\n" \
    "v_mfma_f32_32x32x16_bf16 v[80:95], v[44:47], a[188:191], v[80:95]\n" \
    "ds_read_b128 v[44:47], v61 offset:11776\n" \
    "s_waitcnt lgkmcnt(3)\n" \
    "v_mfma_f32_32x32x16_bf16 v[64:79], v[32:35], a[80:83], v[64:79]\n" \
    "v_mfma_f32_32x32x16_bf16 v[80:95], v[32:35], a[192:195], v[80:95]\n" \
    "ds_read_b128 v[32:35], v61 offset:12288\n" \
    "s_mov_b32 m0, s44\n" \
    "s_waitcnt lgkmcnt(3)\n" \
    "v_mfma_f32_32x32x16_bf16 v[64:79], v[36:39], a[84:87], v[64:79]\n" \
    "buffer_load_dwordx4 %[l16], %[rs], s43 offen offset:0 lds\n" \
    "v_mfma_f32_32x32x16_bf16 v[80:95], v[36:39], a[196:199], v[80:95]\n" \
    "ds_read_b128 v[36:39], v61 offset:12800\n" \
    "s_waitcnt lgkmcnt(3)\n" \
    "v_mfma_f32_32x32x16_bf16 v[64:79], v[40:43], a[88:91], v[64:79]\n" \
    "v_mfma_f32_32x32x16_bf16 v[80:95], v[40:43], a[200:203], v[80:95]\n" \
    "ds_read_b128 v[40:43], v61 offset:13312\n" \
    "s_waitcnt lgkmcnt(3)\n" \
    "v_mfma_f32_32x32x16_bf16 v[64:79], v[44:47], a[92:95], v[64:79]\n" \
    "buffer_load_dwordx4 %[l16], %[rs], s43 offen offset:1024 lds\n" \
    "v_mfma_f32_32x32x16_bf16 v[80:95], v[44:47], a[204:207], v[80:95]\n" \
    "ds_read_b128 v[44:47], v61 offset:13824\n" \
    "s_waitcnt lgkmcnt(3)\n" \
    "v_mfma_f32_32x32x16_bf16 v[64:79], v[32:35], a[96:99], v[64:79]\n" \
    "v_mov_b32 v60, s40\n" \
    "s_waitcnt vmcnt(6)\n" \
    "buffer_store_dword v60, %[fo], %[rs], 0 offen sc1\n" \
    "v_mfma_f32_32x32x16_bf16 v[80:95], v[32:35], a[208:211], v[80:95]\n" \
    "ds_read_b128 v[32:35], v62 offset:0\n" \
    "s_waitcnt lgkmcnt(3)\n" \
    "v_mfma_f32_32x32x16_bf16 v[64:79], v[36:39], a[100:103], v[64:79]\n" \
    "buffer_load_dwordx4 %[l16], %[rs], s43 offen offset:2048 lds\n" \
    "v_mfma_f32_32x32x16_bf16 v[80:95], v[36:39], a[212:215], v[80:95]\n" \
    "ds_read_b128 v[36:39], v62 offset:512\n" \
    "s_waitcnt lgkmcnt(3)\n" \
    "v_mfma_f32_32x32x16_bf16 v[64:79], v[40:43], a[104:107], v[64:79]\n" \
    "s_add_u32 s40, s40, 1\n" \
    "s_cmp_lt_u32 s40, 61\n" \
    "s_cselect_b32 s50, 28672, 0\n" \
    "s_add_u32 s43, s43, s50\n" \
    "v_mfma_f32_32x32x16_bf16 v[80:95], v[40:43], a[216:219], v[80:95]\n" \
    "ds_read_b128 v[40:43], v62 offset:1024\n" \
    "s_waitcnt lgkmcnt(3)\n" \
    "v_mfma_f32_32x32x16_bf16 v[64:79], v[44:47], a[108:111], v[64:79]\n" \
    "s_sub_u32 s43, s43, 4096\n" \
    "s_add_u32 s45, s45, 28672\n" \
    "s_cmp_eq_u32 s45, 114688\n" \
    "s_cselect_b32 s45, 0, s45\n" \
    "v_mfma_f32_32x32x16_bf16 v[80:95], v[44:47], a[220:223], v[80:95]\n" \
    "v_mov_b32 v61, v62\n" \
    "v_add_u32 v62, s45, %[xb]\n" \
    "s_add_u32 s44, s45, 57344\n" \
    "s_cmp_ge_u32 s44, 114688\n" \
    "s_cselect_b32 s50, 114688, 0\n" \
    "s_sub_u32 s44, s44, s50\n" \
    "s_add_u32 s44, s44, %[ld0]\n" \
    "ds_read_b128 v[44:47], v61 offset:1536\n" \
    "s_waitcnt lgkmcnt(3)\n" \
    "v_mfma_f32_32x32x16_bf16 v[96:111], v[32:35], a[0:3], 0\n" \
    "s_waitcnt vmcnt(8)\n" \
    "s_barrier\n" \
    "v_mfma_f32_32x32x16_bf16 v[112:127], v[32:35], a[112:115], 0\n" \
    "ds_read_b128 v[32:35], v61 offset:2048\n" \
    "s_add_u32 s46, s41, 65536\n" \
    "s_waitcnt lgkmcnt(3)\n" \
    "v_mfma_f32_32x32x16_bf16 v[96:111], v[36:39], a[4:7], v[96:111]\n" \
    "v_cvt_pk_bf16_f32 v48, v64, v65\n" \
    "v_cvt_pk_bf16_f32 v49, v66, v67\n" \
    "v_cvt_pk_bf16_f32 v50, v68, v69\n" \
    "v_cvt_pk_bf16_f32 v51, v70, v71\n" \
    "v_mfma_f32_32x32x16_bf16 v[112:127], v[36:39], a[116:119], v[112:127]\n" \
    "ds_read_b128 v[36:39], v61 offset:2560\n" \
    "s_waitcnt lgkmcnt(3)\n" \
    "v_mfma_f32_32x32x16_bf16 v[96:111], v[40:43], a[8:11], v[96:111]\n" \
    "buffer_store_dwordx4 v[48:51], %[ov], %[rs], s41 offen offset:0 sc1\n" \
    "v_mfma_f32_32x32x16_bf16 v[112:127], v[40:43], a[120:123], v[112:127]\n" \
    "ds_read_b128 v[40:43], v61 offset:3072\n" \
    "s_waitcnt lgkmcnt(3)\n" \
    "v_mfma_f32_32x32x16_bf16 v[96:111], v[44:47], a[12:15], v[96:111]\n" \
    "v_cvt_pk_bf16_f32 v52, v72, v73\n" \
    "v_cvt_pk_bf16_f32 v53, v74, v75\n" \
    "v_cvt_pk_bf16_f32 v54, v76, v77\n" \
    "v_cvt_pk_bf16_f32 v55, v78, v79\n" \
    "v_mfma_f32_32x32x16_bf16 v[112:127], v[44:47], a[124:127], v[112:127]\n" \
    "ds_read_b128 v[44:47], v61 offset:3584\n" \
    "s_waitcnt lgkmcnt(3)\n" \
    "v_mfma_f32_32x32x16_bf16 v[96:111], v[32:35], a[16:19], v[96:111]\n" \
    "buffer_store_dwordx4 v[52:55], %[ov], %[rs], s41 offen offset:512 sc1\n" \
    "v_mfma_f32_32x32x16_bf16 v[112:127], v[32:35], a[128:131], v[112:127]\n" \
    "ds_read_b128 v[32:35], v61 offset:4096\n" \
    "s_waitcnt lgkmcnt(3)\n" \
    "v_mfma_f32_32x32x16_bf16 v[96:111], v[36:39], a[20:23], v[96:111]\n" \
    "v_cvt_pk_bf16_f32 v48, v80, v81\n" \
    "v_cvt_pk_bf16_f32 v49, v82, v83\n" \
    "v_cvt_pk_bf16_f32 v50, v84, v85\n" \
    "v_cvt_pk_bf16_f32 v51, v86, v87\n" \
    "v_mfma_f32_32x32x16_bf16 v[112:127], v[36:39], a[132:135], v[112:127]\n" \
    "ds_read_b128 v[36:39], v61 offset:4608\n" \
    "s_waitcnt lgkmcnt(3)\n" \
    "v_mfma_f32_32x32x16_bf16 v[96:111], v[40:43], a[24:27], v[96:111]\n" \
    "buffer_store_dwordx4 v[48:51], %[ov], %[rs], s46 offen offset:0 sc1\n" \
    "v_mfma_f32_32x32x16_bf16 v[112:127], v[40:43], a[136:139], v[112:127]\n" \
    "ds_read_b128 v[40:43], v61 offset:5120\n" \
    "s_waitcnt lgkmcnt(3)\n" \
    "v_mfma_f32_32x32x16_bf16 v[96:111], v[44:47], a[28:31], v[96:111]\n" \
    "v_cvt_pk_bf16_f32 v52, v88, v89\n" \
    "v_cvt_pk_bf16_f32 v53, v90, v91\n" \
    "v_cvt_pk_bf16_f32 v54, v92, v93\n" \
    "v_cvt_pk_bf16_f32 v55, v94, v95\n" \
    "v_mfma_f32_32x32x16_bf16 v[112:127], v[44:47], a[140:143], v[112:127]\n" \
    "ds_read_b128 v[44:47], v61 offset:5632\n" \
    "s_waitcnt lgkmcnt(3)\n" \
    "v_mfma_f32_32x32x16_bf16 v[96:111], v[32:35], a[32:35], v[96:111]\n" \
    "buffer_store_dwordx4 v[52:55], %[ov], %[rs], s46 offen offset:512 sc1\n" \
    "v_mfma_f32_32x32x16_bf16 v[112:127], v[32:35], a[144:147], v[112:127]\n" \
    "ds_read_b128 v[32:35], v61 offset:6144\n" \
    "s_waitcnt lgkmcnt(3)\n" \
    "v_mfma_f32_32x32x16_bf16 v[96:111], v[36:39], a[36:39], v[96:111]\n" \
    "s_add_u32 s54, s54, 1\n" \
    "s_lshr_b32 s55, s54, 1\n" \
    "s_xor_b32 s55, s55, %[pol]\n" \
    "s_and_b32 s55, s55, 1\n" \
    "v_mfma_f32_32x32x16_bf16 v[112:127], v[36:39], a[148:151], v[112:127]\n" \
    "ds_read_b128 v[36:39], v61 offset:6656\n" \
    "s_waitcnt lgkmcnt(3)\n" \
    "v_mfma_f32_32x32x16_bf16 v[96:111], v[40:43], a[40:43], v[96:111]\n" \
    "v_mfma_f32_32x32x16_bf16 v[112:127], v[40:43], a[152:155], v[112:127]\n" \
    "ds_read_b128 v[40:43], v61 offset:7168\n" \
    "s_waitcnt lgkmcnt(3)\n" \
    "v_mfma_f32_32x32x16_bf16 v[96:111], v[44:47], a[44:47], v[96:111]\n" \
    "s_lshl_b32 s55, s55, 13\n" \
    "s_lshl_b32 s41, s54, 20\n" \
    "s_add_u32 s41, s41, s55\n" \
    "s_add_u32 s41, s41, %[out0]\n" \
    "v_mfma_f32_32x32x16_bf16 v[112:127], v[44:47], a[156:159], v[112:127]\n" \
    "ds_read_b128 v[44:47], v61 offset:7680\n" \
    "s_mov_b32 m0, s44\n" \
    "s_waitcnt lgkmcnt(3)\n" \
    "v_mfma_f32_32x32x16_bf16 v[96:111], v[32:35], a[48:51], v[96:111]\n" \
    "buffer_load_dwordx4 %[l16], %[rs], s43 offen offset:0 lds\n" \
    "v_mfma_f32_32x32x16_bf16 v[112:127], v[32:35], a[160:163], v[112:127]\n" \
    "ds_read_b128 v[32:35], v61 offset:8192\n" \
    "s_waitcnt lgkmcnt(3)\n" \
    "v_mfma_f32_32x32x16_bf16 v[96:111], v[36:39], a[52:55], v[96:111]\n" \
    "v_mfma_f32_32x32x16_bf16 v[112:127], v[36:39], a[164:167], v[112:127]\n" \
    "ds_read_b128 v[36:39], v61 offset:8704\n" \
    "s_waitcnt lgkmcnt(3)\n" \
    "v_mfma_f32_32x32x16_bf16 v[96:111], v[40:43], a[56:59], v[96:111]\n" \
    "buffer_load_dwordx4 %[l16], %[rs], s43 offen offset:1024 lds\n" \
    "v_mfma_f32_32x32x16_bf16 v[112:127], v[40:43], a[168:171], v[112:127]\n" \
    "ds_read_b128 v[40:43], v61 offset:9216\n" \
    "s_waitcnt lgkmcnt(3)\n" \
    "v_mfma_f32_32x32x16_bf16 v[96:111], v[44:47], a[60:63], v[96:111]\n" \
    "v_mfma_f32_32x32x16_bf16 v[112:127], v[44:47], a[172:175], v[112:127]\n" \
    "ds_read_b128 v[44:47], v61 offset:9728\n" \
    "s_waitcnt lgkmcnt(3)\n" \
    "v_mfma_f32_32x32x16_bf16 v[96:111], v[32:35], a[64:67], v[96:111]\n" \
    "buffer_load_dwordx4 %[l16], %[rs], s43 offen offset:2048 lds\n" \
    "v_mfma_f32_32x32x16_bf16 v[112:127], v[32:35], a[176:179], v[112:127]\n" \
    "ds_read_b128 v[32:35], v61 offset:10240\n" \
    "s_waitcnt lgkmcnt(3)\n" \
    "v_mfma_f32_32x32x16_bf16 v[96:111], v[36:39], a[68:71], v[96:111]\n" \
    "v_mfma_f32_32x32x16_bf16 v[112:127], v[36:39], a[180:183], v[112:127]\n" \
    "ds_read_b128 v[36:39], v61 offset:10752\n" \
    "s_waitcnt lgkmcnt(3)\n" \
    "v_mfma_f32_32x32x16_bf16 v[96:111], v[40:43], a[72:75], v[96:111]\n" \
    "buffer_load_dwordx4 %[l16], %[rs], s43 offen offset:3072 lds\n" \
    "v_mfma_f32_32x32x16_bf16 v[112:127], v[40:43], a[184:187], v[112:127]\n" \
    "ds_read_b128 v[40:43], v61 offset:11264\n" \
    "s_waitcnt lgkmcnt(3)\n" \
    "v_mfma_f32_32x32x16_bf16 v[96:111], v[44:47], a[76:79], v[96:111]\n" \
    "s_add_u32 s44, s44, 4096\n" \
    "s_add_u32 s43, s43, 4096\n" \
    "v_mfma_f32_32x32x16_bf16 v[112:127], v[44:47], a[188:191], v[112:127]\n" \
    "ds_read_b128 v[44:47], v61 offset:11776\n" \
    "s_waitcnt lgkmcnt(3)\n" \
    "v_mfma_f32_32x32x16_bf16 v[96:111], v[32:35], a[80:83], v[96:111]\n" \
    "v_mfma_f32_32x32x16_bf16 v[112:127], v[32:35], a[192:195], v[112:127]\n" \
    "ds_read_b128 v[32:35], v61 offset:12288\n" \
    "s_mov_b32 m0, s44\n" \
    "s_waitcnt lgkmcnt(3)\n" \
    "v_mfma_f32_32x32x16_bf16 v[96:111], v[36:39], a[84:87], v[96:111]\n" \
    "buffer_load_dwordx4 %[l16], %[rs], s43 offen offset:0 lds\n" \
    "v_mfma_f32_32x32x16_bf16 v[112:127], v[36:39], a[196:199], v[112:127]\n" \
    "ds_read_b128 v[36:39], v61 offset:12800\n" \
    "s_waitcnt lgkmcnt(3)\n" \
    "v_mfma_f32_32x32x16_bf16 v[96:111], v[40:43], a[88:91], v[96:111]\n" \
    "v_mfma_f32_32x32x16_bf16 v[112:127], v[40:43], a[200:203], v[112:127]\n" \
    "ds_read_b128 v[40:43], v61 offset:13312\n" \
    "s_waitcnt lgkmcnt(3)\n" \
    "v_mfma_f32_32x32x16_bf16 v[96:111], v[44:47], a[92:95], v[96:111]\n" \
    "buffer_load_dwordx4 %[l16], %[rs], s43 offen offset:1024 lds\n" \
    "v_mfma_f32_32x32x16_bf16 v[112:127], v[44:47], a[204:207], v[112:127]\n" \
    "ds_read_b128 v[44:47], v61 offset:13824\n" \
    "s_waitcnt lgkmcnt(3)\n" \
    "v_mfma_f32_32x32x16_bf16 v[96:111], v[32:35], a[96:99], v[96:111]\n" \
    "v_mov_b32 v60, s40\n" \
    "s_waitcnt vmcnt(6)\n" \
    "buffer_store_dword v60, %[fo], %[rs], 0 offen sc1\n" \
    "v_mfma_f32_32x32x16_bf16 v[112:127], v[32:35], a[208:211], v[112:127]\n" \
    "ds_read_b128 v[32:35], v62 offset:0\n" \
    "s_waitcnt lgkmcnt(3)\n" \
    "v_mfma_f32_32x32x16_bf16 v[96:111], v[36:39], a[100:103], v[96:111]\n" \
    "buffer_load_dwordx4 %[l16], %[rs], s43 offen offset:2048 lds\n" \
    "v_mfma_f32_32x32x16_bf16 v[112:127], v[36:39], a[212:215], v[112:127]\n" \
    "ds_read_b128 v[36:39], v62 offset:512\n" \
    "s_waitcnt lgkmcnt(3)\n" \
    "v_mfma_f32_32x32x16_bf16 v[96:111], v[40:43], a[104:107], v[96:111]\n" \
    "s_add_u32 s40, s40, 1\n" \
    "s_cmp_lt_u32 s40, 61\n" \
    "s_cselect_b32 s50, 28672, 0\n" \
    "s_add_u32 s43, s43, s50\n" \
    "v_mfma_f32_32x32x16_bf16 v[112:127], v[40:43], a[216:219], v[112:127]\n" \
    "ds_read_b128 v[40:43], v62 offset:1024\n" \
    "s_waitcnt lgkmcnt(3)\n" \
    "v_mfma_f32_32x32x16_bf16 v[96:111], v[44:47], a[108:111], v[96:111]\n" \
    "s_sub_u32 s43, s43, 4096\n" \
    "s_add_u32 s45, s45, 28672\n" \
    "s_cmp_eq_u32 s45, 114688\n" \
    "s_cselect_b32 s45, 0, s45\n" \
    "v_mfma_f32_32x32x16_bf16 v[112:127], v[44:47], a[220:223], v[112:127]\n" \
    "v_mov_b32 v61, v62\n" \
    "v_add_u32 v62, s45, %[xb]\n" \
    "s_add_u32 s44, s45, 57344\n" \
    "s_cmp_ge_u32 s44, 114688\n" \
    "s_cselect_b32 s50, 114688, 0\n" \
    "s_sub_u32 s44, s44, s50\n" \
    "s_add_u32 s44, s44, %[ld0]\n" \
    "s_cmp_lt_u32 s40, 64\n" \
    "s_cbranch_scc1 Gloop_%=\n" \
    "s_add_u32 s46, s41, 65536\n" \
    "s_nop 7\n" \
    "v_cvt_pk_bf16_f32 v48, v96, v97\n" \
    "v_cvt_pk_bf16_f32 v49, v98, v99\n" \
    "v_cvt_pk_bf16_f32 v50, v100, v101\n" \
    "v_cvt_pk_bf16_f32 v51, v102, v103\n" \
    "buffer_store_dwordx4 v[48:51], %[ov], %[rs], s41 offen offset:0 sc1\n" \
    "v_cvt_pk_bf16_f32 v52, v104, v105\n" \
    "v_cvt_pk_bf16_f32 v53, v106, v107\n" \
    "v_cvt_pk_bf16_f32 v54, v108, v109\n" \
    "v_cvt_pk_bf16_f32 v55, v110, v111\n" \
    "buffer_store_dwordx4 v[52:55], %[ov], %[rs], s41 offen offset:512 sc1\n" \
    "s_nop 1\n" \
    "v_cvt_pk_bf16_f32 v48, v112, v113\n" \
    "v_cvt_pk_bf16_f32 v49, v114, v115\n" \
    "v_cvt_pk_bf16_f32 v50, v116, v117\n" \
    "v_cvt_pk_bf16_f32 v51, v118, v119\n" \
    "buffer_store_dwordx4 v[48:51], %[ov], %[rs], s46 offen offset:0 sc1\n" \
    "v_cvt_pk_bf16_f32 v52, v120, v121\n" \
    "v_cvt_pk_bf16_f32 v53, v122, v123\n" \
    "v_cvt_pk_bf16_f32 v54, v124, v125\n" \
    "v_cvt_pk_bf16_f32 v55, v126, v127\n" \
    "buffer_store_dwordx4 v[52:55], %[ov], %[rs], s46 offen offset:512 sc1\n" \
    "v_mov_b32 v60, 64\n" \
    "s_waitcnt vmcnt(0)\n" \
    "buffer_store_dword v60, %[fo], %[rs], 0 offen sc1\n" \
    "s_add_u32 s54, s54, 1\n" \
    "s_lshr_b32 s55, s54, 1\n" \
    "s_xor_b32 s55, s55, %[pol]\n" \
    "s_and_b32 s55, s55, 1\n" \
    "s_lshl_b32 s55, s55, 13\n" \
    "s_lshl_b32 s41, s54, 20\n" \
    "s_add_u32 s41, s41, s55\n" \
    "s_add_u32 s41, s41, %[out0]\n" \
    "s_waitcnt vmcnt(0) lgkmcnt(0)\n"

#define ASM_FFN \
    "s_nop 4\n" \
    "s_add_u32 s44, %[ldw], 0\n" \
    "s_add_u32 s45, %[w1o], 0\n" \
    "s_mov_b32 m0, s44\n" \
    "s_nop 0\n" \
    "buffer_load_dwordx4 %[v1], %[rs1], s45 offen lds\n" \
    "s_add_u32 s44, %[ldw], 1024\n" \
    "s_add_u32 s45, %[w1o], 57344\n" \
    "s_mov_b32 m0, s44\n" \
    "s_nop 0\n" \
    "buffer_load_dwordx4 %[v1], %[rs1], s45 offen lds\n" \
    "s_add_u32 s44, %[ldw], 2048\n" \
    "s_add_u32 s45, %[w1o], 114688\n" \
    "s_mov_b32 m0, s44\n" \
    "s_nop 0\n" \
    "buffer_load_dwordx4 %[v1], %[rs1], s45 offen lds\n" \
    "s_add_u32 s44, %[ldw], 3072\n" \
    "s_add_u32 s45, %[w1o], 172032\n" \
    "s_mov_b32 m0, s44\n" \
    "s_nop 0\n" \
    "buffer_load_dwordx4 %[v1], %[rs1], s45 offen lds\n" \
    "s_add_u32 s44, %[ldw], 4096\n" \
    "s_add_u32 s45, %[w1o], 229376\n" \
    "s_mov_b32 m0, s44\n" \
    "s_nop 0\n" \
    "buffer_load_dwordx4 %[v1], %[rs1], s45 offen lds\n" \
    "s_add_u32 s44, %[ldw], 5120\n" \
    "s_add_u32 s45, %[w1o], 286720\n" \
    "s_mov_b32 m0, s44\n" \
    "s_nop 0\n" \
    "buffer_load_dwordx4 %[v1], %[rs1], s45 offen lds\n" \
    "s_add_u32 s44, %[ldw], 6144\n" \
    "s_add_u32 s45, %[w1o], 344064\n" \
    "s_mov_b32 m0, s44\n" \
    "s_nop 0\n" \
    "buffer_load_dwordx4 %[v1], %[rs1], s45 offen lds\n" \
    "s_add_u32 s44, %[xo], 0\n" \
    "buffer_load_dwordx4 v[32:35], %[xv], %[rs], s44 offen offset:0\n" \
    "buffer_load_dwordx4 v[36:39], %[xv], %[rs], s44 offen offset:512\n" \
    "buffer_load_dwordx4 v[40:43], %[xv], %[rs], s44 offen offset:1024\n" \
    "buffer_load_dwordx4 v[44:47], %[xv], %[rs], s44 offen offset:1536\n" \
    "buffer_load_dwordx4 v[48:51], %[xv], %[rs], s44 offen offset:2048\n" \
    "buffer_load_dwordx4 v[52:55], %[xv], %[rs], s44 offen offset:2560\n" \
    "buffer_load_dwordx4 v[56:59], %[xv], %[rs], s44 offen offset:3072\n" \
    "buffer_load_dwordx4 v[60:63], %[xv], %[rs], s44 offen offset:3584\n" \
    "s_add_u32 s44, %[xo], 4096\n" \
    "buffer_load_dwordx4 v[64:67], %[xv], %[rs], s44 offen offset:0\n" \
    "buffer_load_dwordx4 v[68:71], %[xv], %[rs], s44 offen offset:512\n" \
    "buffer_load_dwordx4 v[72:75], %[xv], %[rs], s44 offen offset:1024\n" \
    "buffer_load_dwordx4 v[76:79], %[xv], %[rs], s44 offen offset:1536\n" \
    "buffer_load_dwordx4 v[80:83], %[xv], %[rs], s44 offen offset:2048\n" \
    "buffer_load_dwordx4 v[84:87], %[xv], %[rs], s44 offen offset:2560\n" \
    "buffer_load_dwordx4 v[88:91], %[xv], %[rs], s44 offen offset:3072\n" \
    "buffer_load_dwordx4 v[92:95], %[xv], %[rs], s44 offen offset:3584\n" \
    "s_add_u32 s44, %[xo], 8192\n" \
    "buffer_load_dwordx4 v[96:99], %[xv], %[rs], s44 offen offset:0\n" \
    "buffer_load_dwordx4 v[100:103], %[xv], %[rs], s44 offen offset:512\n" \
    "buffer_load_dwordx4 v[104:107], %[xv], %[rs], s44 offen offset:1024\n" \
    "buffer_load_dwordx4 v[108:111], %[xv], %[rs], s44 offen offset:1536\n" \
    "buffer_load_dwordx4 v[112:115], %[xv], %[rs], s44 offen offset:2048\n" \
    "buffer_load_dwordx4 v[116:119], %[xv], %[rs], s44 offen offset:2560\n" \
    "buffer_load_dwordx4 v[120:123], %[xv], %[rs], s44 offen offset:3072\n" \
    "buffer_load_dwordx4 v[124:127], %[xv], %[rs], s44 offen offset:3584\n" \
    "s_add_u32 s44, %[xo], 12288\n" \
    "buffer_load_dwordx4 v[128:131], %[xv], %[rs], s44 offen offset:0\n" \
    "buffer_load_dwordx4 v[132:135], %[xv], %[rs], s44 offen offset:512\n" \
    "buffer_load_dwordx4 v[136:139], %[xv], %[rs], s44 offen offset:1024\n" \
    "buffer_load_dwordx4 v[140:143], %[xv], %[rs], s44 offen offset:1536\n" \
    "v_accvgpr_write_b32 a0, 0\n" \
    "v_accvgpr_write_b32 a1, 0\n" \
    "v_accvgpr_write_b32 a2, 0\n" \
    "v_accvgpr_write_b32 a3, 0\n" \
    "v_accvgpr_write_b32 a4, 0\n" \
    "v_accvgpr_write_b32 a5, 0\n" \
    "v_accvgpr_write_b32 a6, 0\n" \
    "v_accvgpr_write_b32 a7, 0\n" \
    "v_accvgpr_write_b32 a8, 0\n" \
    "v_accvgpr_write_b32 a9, 0\n" \
    "v_accvgpr_write_b32 a10, 0\n" \
    "v_accvgpr_write_b32 a11, 0\n" \
    "v_accvgpr_write_b32 a12, 0\n" \
    "v_accvgpr_write_b32 a13, 0\n" \
    "v_accvgpr_write_b32 a14, 0\n" \
    "v_accvgpr_write_b32 a15, 0\n" \
    "v_accvgpr_write_b32 a16, 0\n" \
    "v_accvgpr_write_b32 a17, 0\n" \
    "v_accvgpr_write_b32 a18, 0\n" \
    "v_accvgpr_write_b32 a19, 0\n" \
    "v_accvgpr_write_b32 a20, 0\n" \
    "v_accvgpr_write_b32 a21, 0\n" \
    "v_accvgpr_write_b32 a22, 0\n" \
    "v_accvgpr_write_b32 a23, 0\n" \
    "v_accvgpr_write_b32 a24, 0\n" \
    "v_accvgpr_write_b32 a25, 0\n" \
    "v_accvgpr_write_b32 a26, 0\n" \
    "v_accvgpr_write_b32 a27, 0\n" \
    "v_accvgpr_write_b32 a28, 0\n" \
    "v_accvgpr_write_b32 a29, 0\n" \
    "v_accvgpr_write_b32 a30, 0\n" \
    "v_accvgpr_write_b32 a31, 0\n" \
    "v_accvgpr_write_b32 a32, 0\n" \
    "v_accvgpr_write_b32 a33, 0\n" \
    "v_accvgpr_write_b32 a34, 0\n" \
    "v_accvgpr_write_b32 a35, 0\n" \
    "v_accvgpr_write_b32 a36, 0\n" \
    "v_accvgpr_write_b32 a37, 0\n" \
    "v_accvgpr_write_b32 a38, 0\n" \
    "v_accvgpr_write_b32 a39, 0\n" \
    "v_accvgpr_write_b32 a40, 0\n" \
    "v_accvgpr_write_b32 a41, 0\n" \
    "v_accvgpr_write_b32 a42, 0\n" \
    "v_accvgpr_write_b32 a43, 0\n" \
    "v_accvgpr_write_b32 a44, 0\n" \
    "v_accvgpr_write_b32 a45, 0\n" \
    "v_accvgpr_write_b32 a46, 0\n" \
    "v_accvgpr_write_b32 a47, 0\n" \
    "v_accvgpr_write_b32 a48, 0\n" \
    "v_accvgpr_write_b32 a49, 0\n" \
    "v_accvgpr_write_b32 a50, 0\n" \
    "v_accvgpr_write_b32 a51, 0\n" \
    "v_accvgpr_write_b32 a52, 0\n" \
    "v_accvgpr_write_b32 a53, 0\n" \
    "v_accvgpr_write_b32 a54, 0\n" \
    "v_accvgpr_write_b32 a55, 0\n" \
    "v_accvgpr_write_b32 a56, 0\n" \
    "v_accvgpr_write_b32 a57, 0\n" \
    "v_accvgpr_write_b32 a58, 0\n" \
    "v_accvgpr_write_b32 a59, 0\n" \
    "v_accvgpr_write_b32 a60, 0\n" \
    "v_accvgpr_write_b32 a61, 0\n" \
    "v_accvgpr_write_b32 a62, 0\n" \
    "v_accvgpr_write_b32 a63, 0\n" \
    "v_accvgpr_write_b32 a64, 0\n" \
    "v_accvgpr_write_b32 a65, 0\n" \
    "v_accvgpr_write_b32 a66, 0\n" \
    "v_accvgpr_write_b32 a67, 0\n" \
    "v_accvgpr_write_b32 a68, 0\n" \
    "v_accvgpr_write_b32 a69, 0\n" \
    "v_accvgpr_write_b32 a70, 0\n" \
    "v_accvgpr_write_b32 a71, 0\n" \
    "v_accvgpr_write_b32 a72, 0\n" \
    "v_accvgpr_write_b32 a73, 0\n" \
    "v_accvgpr_write_b32 a74, 0\n" \
    "v_accvgpr_write_b32 a75, 0\n" \
    "v_accvgpr_write_b32 a76, 0\n" \
    "v_accvgpr_write_b32 a77, 0\n" \
    "v_accvgpr_write_b32 a78, 0\n" \
    "v_accvgpr_write_b32 a79, 0\n" \
    "v_accvgpr_write_b32 a80, 0\n" \
    "v_accvgpr_write_b32 a81, 0\n" \
    "v_accvgpr_write_b32 a82, 0\n" \
    "v_accvgpr_write_b32 a83, 0\n" \
    "v_accvgpr_write_b32 a84, 0\n" \
    "v_accvgpr_write_b32 a85, 0\n" \
    "v_accvgpr_write_b32 a86, 0\n" \
    "v_accvgpr_write_b32 a87, 0\n" \
    "v_accvgpr_write_b32 a88, 0\n" \
    "v_accvgpr_write_b32 a89, 0\n" \
    "v_accvgpr_write_b32 a90, 0\n" \
    "v_accvgpr_write_b32 a91, 0\n" \
    "v_accvgpr_write_b32 a92, 0\n" \
    "v_accvgpr_write_b32 a93, 0\n" \
    "v_accvgpr_write_b32 a94, 0\n" \
    "v_accvgpr_write_b32 a95, 0\n" \
    "v_accvgpr_write_b32 a96, 0\n" \
    "v_accvgpr_write_b32 a97, 0\n" \
    "v_accvgpr_write_b32 a98, 0\n" \
    "v_accvgpr_write_b32 a99, 0\n" \
    "v_accvgpr_write_b32 a100, 0\n" \
    "v_accvgpr_write_b32 a101, 0\n" \
    "v_accvgpr_write_b32 a102, 0\n" \
    "v_accvgpr_write_b32 a103, 0\n" \
    "v_accvgpr_write_b32 a104, 0\n" \
    "v_accvgpr_write_b32 a105, 0\n" \
    "v_accvgpr_write_b32 a106, 0\n" \
    "v_accvgpr_write_b32 a107, 0\n" \
    "v_accvgpr_write_b32 a108, 0\n" \
    "v_accvgpr_write_b32 a109, 0\n" \
    "v_accvgpr_write_b32 a110, 0\n" \
    "v_accvgpr_write_b32 a111, 0\n" \
    "v_accvgpr_write_b32 a112, 0\n" \
    "v_accvgpr_write_b32 a113, 0\n" \
    "v_accvgpr_write_b32 a114, 0\n" \
    "v_accvgpr_write_b32 a115, 0\n" \
    "v_accvgpr_write_b32 a116, 0\n" \
    "v_accvgpr_write_b32 a117, 0\n" \
    "v_accvgpr_write_b32 a118, 0\n" \
    "v_accvgpr_write_b32 a119, 0\n" \
    "v_accvgpr_write_b32 a120, 0\n" \
    "v_accvgpr_write_b32 a121, 0\n" \
    "v_accvgpr_write_b32 a122, 0\n" \
    "v_accvgpr_write_b32 a123, 0\n" \
    "v_accvgpr_write_b32 a124, 0\n" \
    "v_accvgpr_write_b32 a125, 0\n" \
    "v_accvgpr_write_b32 a126, 0\n" \
    "v_accvgpr_write_b32 a127, 0\n" \
    "v_accvgpr_write_b32 a128, 0\n" \
    "v_accvgpr_write_b32 a129, 0\n" \
    "v_accvgpr_write_b32 a130, 0\n" \
    "v_accvgpr_write_b32 a131, 0\n" \
    "v_accvgpr_write_b32 a132, 0\n" \
    "v_accvgpr_write_b32 a133, 0\n" \
    "v_accvgpr_write_b32 a134, 0\n" \
    "v_accvgpr_write_b32 a135, 0\n" \
    "v_accvgpr_write_b32 a136, 0\n" \
    "v_accvgpr_write_b32 a137, 0\n" \
    "v_accvgpr_write_b32 a138, 0\n" \
    "v_accvgpr_write_b32 a139, 0\n" \
    "v_accvgpr_write_b32 a140, 0\n" \
    "v_accvgpr_write_b32 a141, 0\n" \
    "v_accvgpr_write_b32 a142, 0\n" \
    "v_accvgpr_write_b32 a143, 0\n" \
    "v_accvgpr_write_b32 a144, 0\n" \
    "v_accvgpr_write_b32 a145, 0\n" \
    "v_accvgpr_write_b32 a146, 0\n" \
    "v_accvgpr_write_b32 a147, 0\n" \
    "v_accvgpr_write_b32 a148, 0\n" \
    "v_accvgpr_write_b32 a149, 0\n" \
    "v_accvgpr_write_b32 a150, 0\n" \
    "v_accvgpr_write_b32 a151, 0\n" \
    "v_accvgpr_write_b32 a152, 0\n" \
    "v_accvgpr_write_b32 a153, 0\n" \
    "v_accvgpr_write_b32 a154, 0\n" \
    "v_accvgpr_write_b32 a155, 0\n" \
    "v_accvgpr_write_b32 a156, 0\n" \
    "v_accvgpr_write_b32 a157, 0\n" \
    "v_accvgpr_write_b32 a158, 0\n" \
    "v_accvgpr_write_b32 a159, 0\n" \
    "v_accvgpr_write_b32 a160, 0\n" \
    "v_accvgpr_write_b32 a161, 0\n" \
    "v_accvgpr_write_b32 a162, 0\n" \
    "v_accvgpr_write_b32 a163, 0\n" \
    "v_accvgpr_write_b32 a164, 0\n" \
    "v_accvgpr_write_b32 a165, 0\n" \
    "v_accvgpr_write_b32 a166, 0\n" \
    "v_accvgpr_write_b32 a167, 0\n" \
    "v_accvgpr_write_b32 a168, 0\n" \
    "v_accvgpr_write_b32 a169, 0\n" \
    "v_accvgpr_write_b32 a170, 0\n" \
    "v_accvgpr_write_b32 a171, 0\n" \
    "v_accvgpr_write_b32 a172, 0\n" \
    "v_accvgpr_write_b32 a173, 0\n" \
    "v_accvgpr_write_b32 a174, 0\n" \
    "v_accvgpr_write_b32 a175, 0\n" \
    "v_accvgpr_write_b32 a176, 0\n" \
    "v_accvgpr_write_b32 a177, 0\n" \
    "v_accvgpr_write_b32 a178, 0\n" \
    "v_accvgpr_write_b32 a179, 0\n" \
    "v_accvgpr_write_b32 a180, 0\n" \
    "v_accvgpr_write_b32 a181, 0\n" \
    "v_accvgpr_write_b32 a182, 0\n" \
    "v_accvgpr_write_b32 a183, 0\n" \
    "v_accvgpr_write_b32 a184, 0\n" \
    "v_accvgpr_write_b32 a185, 0\n" \
    "v_accvgpr_write_b32 a186, 0\n" \
    "v_accvgpr_write_b32 a187, 0\n" \
    "v_accvgpr_write_b32 a188, 0\n" \
    "v_accvgpr_write_b32 a189, 0\n" \
    "v_accvgpr_write_b32 a190, 0\n" \
    "v_accvgpr_write_b32 a191, 0\n" \
    "v_accvgpr_write_b32 a192, 0\n" \
    "v_accvgpr_write_b32 a193, 0\n" \
    "v_accvgpr_write_b32 a194, 0\n" \
    "v_accvgpr_write_b32 a195, 0\n" \
    "v_accvgpr_write_b32 a196, 0\n" \
    "v_accvgpr_write_b32 a197, 0\n" \
    "v_accvgpr_write_b32 a198, 0\n" \
    "v_accvgpr_write_b32 a199, 0\n" \
    "v_accvgpr_write_b32 a200, 0\n" \
    "v_accvgpr_write_b32 a201, 0\n" \
    "v_accvgpr_write_b32 a202, 0\n" \
    "v_accvgpr_write_b32 a203, 0\n" \
    "v_accvgpr_write_b32 a204, 0\n" \
    "v_accvgpr_write_b32 a205, 0\n" \
    "v_accvgpr_write_b32 a206, 0\n" \
    "v_accvgpr_write_b32 a207, 0\n" \
    "v_accvgpr_write_b32 a208, 0\n" \
    "v_accvgpr_write_b32 a209, 0\n" \
    "v_accvgpr_write_b32 a210, 0\n" \
    "v_accvgpr_write_b32 a211, 0\n" \
    "v_accvgpr_write_b32 a212, 0\n" \
    "v_accvgpr_write_b32 a213, 0\n" \
    "v_accvgpr_write_b32 a214, 0\n" \
    "v_accvgpr_write_b32 a215, 0\n" \
    "v_accvgpr_write_b32 a216, 0\n" \
    "v_accvgpr_write_b32 a217, 0\n" \
    "v_accvgpr_write_b32 a218, 0\n" \
    "v_accvgpr_write_b32 a219, 0\n" \
    "v_accvgpr_write_b32 a220, 0\n" \
    "v_accvgpr_write_b32 a221, 0\n" \
    "v_accvgpr_write_b32 a222, 0\n" \
    "v_accvgpr_write_b32 a223, 0\n" \
    "s_mov_b32 s40, 0\n" \
    "s_add_u32 s41, %[w1o], 64\n" \
    "s_mov_b32 s42, %[w2o]\n" \
    "s_add_u32 s43, %[ldw], 57344\n" \
    "v_mov_b32 v216, %[t1]\n" \
    "v_mov_b32 v217, %[t2]\n" \
    "v_mov_b32 v218, %[ba]\n" \
    "s_add_u32 s44, %[w7], 0\n" \
    "s_and_b32 s45, s44, 1\n" \
    "s_mul_i32 s45, s45, 14336\n" \
    "s_lshr_b32 s44, s44, 1\n" \
    "s_lshl_b32 s44, s44, 6\n" \
    "s_add_u32 s46, s44, s45\n" \
    "s_add_u32 s44, %[w7], 1\n" \
    "s_and_b32 s45, s44, 1\n" \
    "s_mul_i32 s45, s45, 14336\n" \
    "s_lshr_b32 s44, s44, 1\n" \
    "s_lshl_b32 s44, s44, 6\n" \
    "s_add_u32 s47, s44, s45\n" \
    "s_add_u32 s44, %[w7], 2\n" \
    "s_and_b32 s45, s44, 1\n" \
    "s_mul_i32 s45, s45, 14336\n" \
    "s_lshr_b32 s44, s44, 1\n" \
    "s_lshl_b32 s44, s44, 6\n" \
    "s_add_u32 s48, s44, s45\n" \
    "s_add_u32 s44, %[w7], 3\n" \
    "s_and_b32 s45, s44, 1\n" \
    "s_mul_i32 s45, s45, 14336\n" \
    "s_lshr_b32 s44, s44, 1\n" \
    "s_lshl_b32 s44, s44, 6\n" \
    "s_add_u32 s49, s44, s45\n" \
    "s_add_u32 s44, %[w7], 4\n" \
    "s_and_b32 s45, s44, 1\n" \
    "s_mul_i32 s45, s45, 14336\n" \
    "s_lshr_b32 s44, s44, 1\n" \
    "s_lshl_b32 s44, s44, 6\n" \
    "s_add_u32 s50, s44, s45\n" \
    "s_add_u32 s44, %[w7], 5\n" \
    "s_and_b32 s45, s44, 1\n" \
    "s_mul_i32 s45, s45, 14336\n" \
    "s_lshr_b32 s44, s44, 1\n" \
    "s_lshl_b32 s44, s44, 6\n" \
    "s_add_u32 s51, s44, s45\n" \
    "s_add_u32 s44, %[w7], 6\n" \
    "s_and_b32 s45, s44, 1\n" \
    "s_mul_i32 s45, s45, 14336\n" \
    "s_lshr_b32 s44, s44, 1\n" \
    "s_lshl_b32 s44, s44, 6\n" \
    "s_add_u32 s52, s44, s45\n" \
    "s_waitcnt lgkmcnt(0)\n" \
    "s_waitcnt vmcnt(0)\n" \
    "s_barrier\n" \
    "ds_read_b128 v[200:203], v218 offset:0\n" \
    "ds_read_b128 v[204:207], v218 offset:16\n" \
    "ds_read_b128 v[208:211], v218 offset:32\n" \
    "ds_read_b128 v[212:215], v218 offset:48\n" \
    "ds_read_b64_tr_b16 v[176:177], v216 offset:0\n" \
    "ds_read_b64_tr_b16 v[178:179], v216 offset:256\n" \
    "ds_read_b64_tr_b16 v[180:181], v216 offset:1024\n" \
    "ds_read_b64_tr_b16 v[182:183], v216 offset:1280\n" \
    "ds_read_b64_tr_b16 v[184:185], v216 offset:2048\n" \
    "ds_read_b64_tr_b16 v[186:187], v216 offset:2304\n" \
    "ds_read_b64_tr_b16 v[188:189], v216 offset:3072\n" \
    "ds_read_b64_tr_b16 v[190:191], v216 offset:3328\n" \
    "s_waitcnt lgkmcnt(6)\n" \
    "v_mfma_f32_32x32x16_bf16 v[144:159], v[176:179], v[32:35], v[200:215]\n" \
    "ds_read_b64_tr_b16 v[176:177], v216 offset:4096\n" \
    "ds_read_b64_tr_b16 v[178:179], v216 offset:4352\n" \
    "s_add_u32 s44, s43, 0\n" \
    "s_add_u32 s45, s41, 0\n" \
    "s_mov_b32 m0, s44\n" \
    "s_nop 0\n" \
    "buffer_load_dwordx4 %[v1], %[rs1], s45 offen lds\n" \
    "s_waitcnt lgkmcnt(6)\n" \
    "v_mfma_f32_32x32x16_bf16 v[144:159], v[180:183], v[36:39], v[144:159]\n" \
    "ds_read_b64_tr_b16 v[180:181], v216 offset:5120\n" \
    "ds_read_b64_tr_b16 v[182:183], v216 offset:5376\n" \
    "s_waitcnt lgkmcnt(6)\n" \
    "v_mfma_f32_32x32x16_bf16 v[144:159], v[184:187], v[40:43], v[144:159]\n" \
    "ds_read_b64_tr_b16 v[184:185], v216 offset:6144\n" \
    "ds_read_b64_tr_b16 v[186:187], v216 offset:6400\n" \
    "s_add_u32 s44, s43, 1024\n" \
    "s_add_u32 s45, s41, 57344\n" \
    "s_mov_b32 m0, s44\n" \
    "s_nop 0\n" \
    "buffer_load_dwordx4 %[v1], %[rs1], s45 offen lds\n" \
    "s_waitcnt lgkmcnt(6)\n" \
    "v_mfma_f32_32x32x16_bf16 v[144:159], v[188:191], v[44:47], v[144:159]\n" \
    "ds_read_b64_tr_b16 v[188:189], v216 offset:7168\n" \
    "ds_read_b64_tr_b16 v[190:191], v216 offset:7424\n" \
    "s_waitcnt lgkmcnt(6)\n" \
    "v_mfma_f32_32x32x16_bf16 v[144:159], v[176:179], v[48:51], v[144:159]\n" \
    "ds_read_b64_tr_b16 v[176:177], v216 offset:8192\n" \
    "ds_read_b64_tr_b16 v[178:179], v216 offset:8448\n" \
    "s_add_u32 s44, s43, 2048\n" \
    "s_add_u32 s45, s41, 114688\n" \
    "s_mov_b32 m0, s44\n" \
    "s_nop 0\n" \
    "buffer_load_dwordx4 %[v1], %[rs1], s45 offen lds\n" \
    "s_waitcnt lgkmcnt(6)\n" \
    "v_mfma_f32_32x32x16_bf16 v[144:159], v[180:183], v[52:55], v[144:159]\n" \
    "ds_read_b64_tr_b16 v[180:181], v216 offset:9216\n" \
    "ds_read_b64_tr_b16 v[182:183], v216 offset:9472\n" \
    "s_waitcnt lgkmcnt(6)\n" \
    "v_mfma_f32_32x32x16_bf16 v[144:159], v[184:187], v[56:59], v[144:159]\n" \
    "ds_read_b64_tr_b16 v[184:185], v216 offset:10240\n" \
    "ds_read_b64_tr_b16 v[186:187], v216 offset:10496\n" \
    "s_add_u32 s44, s43, 3072\n" \
    "s_add_u32 s45, s41, 172032\n" \
    "s_mov_b32 m0, s44\n" \
    "s_nop 0\n" \
    "buffer_load_dwordx4 %[v1], %[rs1], s45 offen lds\n" \
    "s_waitcnt lgkmcnt(6)\n" \
    "v_mfma_f32_32x32x16_bf16 v[144:159], v[188:191], v[60:63], v[144:159]\n" \
    "ds_read_b64_tr_b16 v[188:189], v216 offset:11264\n" \
    "ds_read_b64_tr_b16 v[190:191], v216 offset:11520\n" \
    "s_waitcnt lgkmcnt(6)\n" \
    "v_mfma_f32_32x32x16_bf16 v[144:159], v[176:179], v[64:67], v[144:159]\n" \
    "ds_read_b64_tr_b16 v[176:177], v216 offset:12288\n" \
    "ds_read_b64_tr_b16 v[178:179], v216 offset:12544\n" \
    "s_add_u32 s44, s43, 4096\n" \
    "s_add_u32 s45, s41, 229376\n" \
    "s_mov_b32 m0, s44\n" \
    "s_nop 0\n" \
    "buffer_load_dwordx4 %[v1], %[rs1], s45 offen lds\n" \
    "s_waitcnt lgkmcnt(6)\n" \
    "v_mfma_f32_32x32x16_bf16 v[144:159], v[180:183], v[68:71], v[144:159]\n" \
    "ds_read_b64_tr_b16 v[180:181], v216 offset:13312\n" \
    "ds_read_b64_tr_b16 v[182:183], v216 offset:13568\n" \
    "s_waitcnt lgkmcnt(6)\n" \
    "v_mfma_f32_32x32x16_bf16 v[144:159], v[184:187], v[72:75], v[144:159]\n" \
    "ds_read_b64_tr_b16 v[184:185], v216 offset:14336\n" \
    "ds_read_b64_tr_b16 v[186:187], v216 offset:14592\n" \
    "s_add_u32 s44, s43, 5120\n" \
    "s_add_u32 s45, s41, 286720\n" \
    "s_mov_b32 m0, s44\n" \
    "s_nop 0\n" \
    "buffer_load_dwordx4 %[v1], %[rs1], s45 offen lds\n" \
    "s_waitcnt lgkmcnt(6)\n" \
    "v_mfma_f32_32x32x16_bf16 v[144:159], v[188:191], v[76:79], v[144:159]\n" \
    "ds_read_b64_tr_b16 v[188:189], v216 offset:15360\n" \
    "ds_read_b64_tr_b16 v[190:191], v216 offset:15616\n" \
    "s_waitcnt lgkmcnt(6)\n" \
    "v_mfma_f32_32x32x16_bf16 v[144:159], v[176:179], v[80:83], v[144:159]\n" \
    "ds_read_b64_tr_b16 v[176:177], v216 offset:16384\n" \
    "ds_read_b64_tr_b16 v[178:179], v216 offset:16640\n" \
    "s_add_u32 s44, s43, 6144\n" \
    "s_add_u32 s45, s41, 344064\n" \
    "s_mov_b32 m0, s44\n" \
    "s_nop 0\n" \
    "buffer_load_dwordx4 %[v1], %[rs1], s45 offen lds\n" \
    "s_waitcnt lgkmcnt(6)\n" \
    "v_mfma_f32_32x32x16_bf16 v[144:159], v[180:183], v[84:87], v[144:159]\n" \
    "ds_read_b64_tr_b16 v[180:181], v216 offset:17408\n" \
    "ds_read_b64_tr_b16 v[182:183], v216 offset:17664\n" \
    "s_waitcnt lgkmcnt(6)\n" \
    "v_mfma_f32_32x32x16_bf16 v[144:159], v[184:187], v[88:91], v[144:159]\n" \
    "ds_read_b64_tr_b16 v[184:185], v216 offset:18432\n" \
    "ds_read_b64_tr_b16 v[186:187], v216 offset:18688\n" \
    "s_add_u32 s44, s43, 28672\n" \
    "s_add_u32 s45, s42, s46\n" \
    "s_mov_b32 m0, s44\n" \
    "s_nop 0\n" \
    "buffer_load_dwordx4 %[v2], %[rs2], s45 offen lds\n" \
    "s_waitcnt lgkmcnt(6)\n" \
    "v_mfma_f32_32x32x16_bf16 v[144:159], v[188:191], v[92:95], v[144:159]\n" \
    "ds_read_b64_tr_b16 v[188:189], v216 offset:19456\n" \
    "ds_read_b64_tr_b16 v[190:191], v216 offset:19712\n" \
    "s_waitcnt lgkmcnt(6)\n" \
    "v_mfma_f32_32x32x16_bf16 v[144:159], v[176:179], v[96:99], v[144:159]\n" \
    "ds_read_b64_tr_b16 v[176:177], v216 offset:20480\n" \
    "ds_read_b64_tr_b16 v[178:179], v216 offset:20736\n" \
    "s_add_u32 s44, s43, 29696\n" \
    "s_add_u32 s45, s42, s47\n" \
    "s_mov_b32 m0, s44\n" \
    "s_nop 0\n" \
    "buffer_load_dwordx4 %[v2], %[rs2], s45 offen lds\n" \
    "s_waitcnt lgkmcnt(6)\n" \
    "v_mfma_f32_32x32x16_bf16 v[144:159], v[180:183], v[100:103], v[144:159]\n" \
    "ds_read_b64_tr_b16 v[180:181], v216 offset:21504\n" \
    "ds_read_b64_tr_b16 v[182:183], v216 offset:21760\n" \
    "s_waitcnt lgkmcnt(6)\n" \
    "v_mfma_f32_32x32x16_bf16 v[144:159], v[184:187], v[104:107], v[144:159]\n" \
    "ds_read_b64_tr_b16 v[184:185], v216 offset:22528\n" \
    "ds_read_b64_tr_b16 v[186:187], v216 offset:22784\n" \
    "s_add_u32 s44, s43, 30720\n" \
    "s_add_u32 s45, s42, s48\n" \
    "s_mov_b32 m0, s44\n" \
    "s_nop 0\n" \
    "buffer_load_dwordx4 %[v2], %[rs2], s45 offen lds\n" \
    "s_waitcnt lgkmcnt(6)\n" \
    "v_mfma_f32_32x32x16_bf16 v[144:159], v[188:191], v[108:111], v[144:159]\n" \
    "ds_read_b64_tr_b16 v[188:189], v216 offset:23552\n" \
    "ds_read_b64_tr_b16 v[190:191], v216 offset:23808\n" \
    "s_waitcnt lgkmcnt(6)\n" \
    "v_mfma_f32_32x32x16_bf16 v[144:159], v[176:179], v[112:115], v[144:159]\n" \
    "ds_read_b64_tr_b16 v[176:177], v216 offset:24576\n" \
    "ds_read_b64_tr_b16 v[178:179], v216 offset:24832\n" \
    "s_add_u32 s44, s43, 31744\n" \
    "s_add_u32 s45, s42, s49\n" \
    "s_mov_b32 m0, s44\n" \
    "s_nop 0\n" \
    "buffer_load_dwordx4 %[v2], %[rs2], s45 offen lds\n" \
    "s_waitcnt lgkmcnt(6)\n" \
    "v_mfma_f32_32x32x16_bf16 v[144:159], v[180:183], v[116:119], v[144:159]\n" \
    "ds_read_b64_tr_b16 v[180:181], v216 offset:25600\n" \
    "ds_read_b64_tr_b16 v[182:183], v216 offset:25856\n" \
    "s_waitcnt lgkmcnt(6)\n" \
    "v_mfma_f32_32x32x16_bf16 v[144:159], v[184:187], v[120:123], v[144:159]\n" \
    "ds_read_b64_tr_b16 v[184:185], v216 offset:26624\n" \
    "ds_read_b64_tr_b16 v[186:187], v216 offset:26880\n" \
    "s_add_u32 s44, s43, 32768\n" \
    "s_add_u32 s45, s42, s50\n" \
    "s_mov_b32 m0, s44\n" \
    "s_nop 0\n" \
    "buffer_load_dwordx4 %[v2], %[rs2], s45 offen lds\n" \
    "s_waitcnt lgkmcnt(6)\n" \
    "v_mfma_f32_32x32x16_bf16 v[144:159], v[188:191], v[124:127], v[144:159]\n" \
    "ds_read_b64_tr_b16 v[188:189], v216 offset:27648\n" \
    "ds_read_b64_tr_b16 v[190:191], v216 offset:27904\n" \
    "s_waitcnt lgkmcnt(6)\n" \
    "v_mfma_f32_32x32x16_bf16 v[144:159], v[176:179], v[128:131], v[144:159]\n" \
    "s_add_u32 s44, s43, 33792\n" \
    "s_add_u32 s45, s42, s51\n" \
    "s_mov_b32 m0, s44\n" \
    "s_nop 0\n" \
    "buffer_load_dwordx4 %[v2], %[rs2], s45 offen lds\n" \
    "s_waitcnt lgkmcnt(4)\n" \
    "v_mfma_f32_32x32x16_bf16 v[144:159], v[180:183], v[132:135], v[144:159]\n" \
    "s_add_u32 s44, s43, 34816\n" \
    "s_add_u32 s45, s42, s52\n" \
    "s_mov_b32 m0, s44\n" \
    "s_nop 0\n" \
    "buffer_load_dwordx4 %[v2], %[rs2], s45 offen lds\n" \
    "s_waitcnt lgkmcnt(2)\n" \
    "v_mfma_f32_32x32x16_bf16 v[144:159], v[184:187], v[136:139], v[144:159]\n" \
    "s_waitcnt lgkmcnt(0)\n" \
    "v_mfma_f32_32x32x16_bf16 v[144:159], v[188:191], v[140:143], v[144:159]\n" \
    "s_add_u32 s40, s40, 1\n" \
    "s_cmp_lt_u32 s40, 13\n" \
    "s_cselect_b32 s44, 64, 0\n" \
    "s_add_u32 s41, s41, s44\n" \
    "s_add_u32 s42, s42, 28672\n" \
    "v_add_u32 v216, 57344, v216\n" \
    "v_add_u32 v217, 57344, v217\n" \
    "v_add_u32 v218, 128, v218\n" \
    "s_sub_u32 s43, s43, 57344\n" \
    "Floop_%=:\n" \
    "s_waitcnt vmcnt(0)\n" \
    "s_barrier\n" \
    "ds_read_b128 v[200:203], v218 offset:0\n" \
    "ds_read_b128 v[204:207], v218 offset:16\n" \
    "ds_read_b128 v[208:211], v218 offset:32\n" \
    "ds_read_b128 v[212:215], v218 offset:48\n" \
    "ds_read_b64_tr_b16 v[176:177], v216 offset:0\n" \
    "ds_read_b64_tr_b16 v[178:179], v216 offset:256\n" \
    "ds_read_b64_tr_b16 v[180:181], v216 offset:1024\n" \
    "ds_read_b64_tr_b16 v[182:183], v216 offset:1280\n" \
    "ds_read_b64_tr_b16 v[184:185], v216 offset:2048\n" \
    "ds_read_b64_tr_b16 v[186:187], v216 offset:2304\n" \
    "ds_read_b64_tr_b16 v[188:189], v216 offset:3072\n" \
    "ds_read_b64_tr_b16 v[190:191], v216 offset:3328\n" \
    "s_waitcnt lgkmcnt(6)\n" \
    "v_mfma_f32_32x32x16_bf16 v[160:175], v[176:179], v[32:35], v[200:215]\n" \
    "ds_read_b64_tr_b16 v[176:177], v216 offset:4096\n" \
    "ds_read_b64_tr_b16 v[178:179], v216 offset:4352\n" \
    "s_add_u32 s44, s43, 0\n" \
    "s_add_u32 s45, s41, 0\n" \
    "s_mov_b32 m0, s44\n" \
    "v_max_f32 v144, 0, v144\n" \
    "s_waitcnt lgkmcnt(6)\n" \
    "v_mfma_f32_32x32x16_bf16 v[160:175], v[180:183], v[36:39], v[160:175]\n" \
    "ds_read_b64_tr_b16 v[180:181], v216 offset:5120\n" \
    "ds_read_b64_tr_b16 v[182:183], v216 offset:5376\n" \
    "buffer_load_dwordx4 %[v1], %[rs1], s45 offen lds\n" \
    "v_max_f32 v145, 0, v145\n" \
    "s_waitcnt lgkmcnt(6)\n" \
    "v_mfma_f32_32x32x16_bf16 v[160:175], v[184:187], v[40:43], v[160:175]\n" \
    "ds_read_b64_tr_b16 v[184:185], v216 offset:6144\n" \
    "ds_read_b64_tr_b16 v[186:187], v216 offset:6400\n" \
    "v_mul_f32 v144, v144, v144\n" \
    "v_mul_f32 v145, v145, v145\n" \
    "v_cvt_pk_bf16_f32 v192, v144, v145\n" \
    "s_waitcnt lgkmcnt(6)\n" \
    "v_mfma_f32_32x32x16_bf16 v[160:175], v[188:191], v[44:47], v[160:175]\n" \
    "ds_read_b64_tr_b16 v[188:189], v216 offset:7168\n" \
    "ds_read_b64_tr_b16 v[190:191], v216 offset:7424\n" \
    "s_add_u32 s44, s43, 1024\n" \
    "s_add_u32 s45, s41, 57344\n" \
    "s_mov_b32 m0, s44\n" \
    "v_max_f32 v146, 0, v146\n" \
    "s_waitcnt lgkmcnt(6)\n" \
    "v_mfma_f32_32x32x16_bf16 v[160:175], v[176:179], v[48:51], v[160:175]\n" \
    "ds_read_b64_tr_b16 v[176:177], v216 offset:8192\n" \
    "ds_read_b64_tr_b16 v[178:179], v216 offset:8448\n" \
    "buffer_load_dwordx4 %[v1], %[rs1], s45 offen lds\n" \
    "v_max_f32 v147, 0, v147\n" \
    "s_waitcnt lgkmcnt(6)\n" \
    "v_mfma_f32_32x32x16_bf16 v[160:175], v[180:183], v[52:55], v[160:175]\n" \
    "ds_read_b64_tr_b16 v[180:181], v216 offset:9216\n" \
    "ds_read_b64_tr_b16 v[182:183], v216 offset:9472\n" \
    "v_mul_f32 v146, v146, v146\n" \
    "v_mul_f32 v147, v147, v147\n" \
    "v_cvt_pk_bf16_f32 v193, v146, v147\n" \
    "s_waitcnt lgkmcnt(6)\n" \
    "v_mfma_f32_32x32x16_bf16 v[160:175], v[184:187], v[56:59], v[160:175]\n" \
    "ds_read_b64_tr_b16 v[184:185], v216 offset:10240\n" \
    "ds_read_b64_tr_b16 v[186:187], v216 offset:10496\n" \
    "s_add_u32 s44, s43, 2048\n" \
    "s_add_u32 s45, s41, 114688\n" \
    "s_mov_b32 m0, s44\n" \
    "v_max_f32 v148, 0, v148\n" \
    "s_waitcnt lgkmcnt(6)\n" \
    "v_mfma_f32_32x32x16_bf16 v[160:175], v[188:191], v[60:63], v[160:175]\n" \
    "ds_read_b64_tr_b16 v[188:189], v216 offset:11264\n" \
    "ds_read_b64_tr_b16 v[190:191], v216 offset:11520\n" \
    "buffer_load_dwordx4 %[v1], %[rs1], s45 offen lds\n" \
    "v_max_f32 v149, 0, v149\n" \
    "s_waitcnt lgkmcnt(6)\n" \
    "v_mfma_f32_32x32x16_bf16 v[160:175], v[176:179], v[64:67], v[160:175]\n" \
    "ds_read_b64_tr_b16 v[176:177], v216 offset:12288\n" \
    "ds_read_b64_tr_b16 v[178:179], v216 offset:12544\n" \
    "v_mul_f32 v148, v148, v148\n" \
    "v_mul_f32 v149, v149, v149\n" \
    "v_cvt_pk_bf16_f32 v194, v148, v149\n" \
    "s_waitcnt lgkmcnt(6)\n" \
    "v_mfma_f32_32x32x16_bf16 v[160:175], v[180:183], v[68:71], v[160:175]\n" \
    "ds_read_b64_tr_b16 v[180:181], v216 offset:13312\n" \
    "ds_read_b64_tr_b16 v[182:183], v216 offset:13568\n" \
    "s_add_u32 s44, s43, 3072\n" \
    "s_add_u32 s45, s41, 172032\n" \
    "s_mov_b32 m0, s44\n" \
    "v_max_f32 v150, 0, v150\n" \
    "s_waitcnt lgkmcnt(6)\n" \
    "v_mfma_f32_32x32x16_bf16 v[160:175], v[184:187], v[72:75], v[160:175]\n" \
    "ds_read_b64_tr_b16 v[184:185], v216 offset:14336\n" \
    "ds_read_b64_tr_b16 v[186:187], v216 offset:14592\n" \
    "buffer_load_dwordx4 %[v1], %[rs1], s45 offen lds\n" \
    "v_max_f32 v151, 0, v151\n" \
    "s_waitcnt lgkmcnt(6)\n" \
    "v_mfma_f32_32x32x16_bf16 v[160:175], v[188:191], v[76:79], v[160:175]\n" \
    "ds_read_b64_tr_b16 v[188:189], v216 offset:15360\n" \
    "ds_read_b64_tr_b16 v[190:191], v216 offset:15616\n" \
    "v_mul_f32 v150, v150, v150\n" \
    "v_mul_f32 v151, v151, v151\n" \
    "v_cvt_pk_bf16_f32 v195, v150, v151\n" \
    "s_waitcnt lgkmcnt(6)\n" \
    "v_mfma_f32_32x32x16_bf16 v[160:175], v[176:179], v[80:83], v[160:175]\n" \
    "ds_read_b64_tr_b16 v[176:177], v216 offset:16384\n" \
    "ds_read_b64_tr_b16 v[178:179], v216 offset:16640\n" \
    "s_add_u32 s44, s43, 4096\n" \
    "s_add_u32 s45, s41, 229376\n" \
    "s_mov_b32 m0, s44\n" \
    "v_max_f32 v152, 0, v152\n" \
    "s_waitcnt lgkmcnt(6)\n" \
    "v_mfma_f32_32x32x16_bf16 v[160:175], v[180:183], v[84:87], v[160:175]\n" \
    "ds_read_b64_tr_b16 v[180:181], v216 offset:17408\n" \
    "ds_read_b64_tr_b16 v[182:183], v216 offset:17664\n" \
    "buffer_load_dwordx4 %[v1], %[rs1], s45 offen lds\n" \
    "v_max_f32 v153, 0, v153\n" \
    "s_waitcnt lgkmcnt(6)\n" \
    "v_mfma_f32_32x32x16_bf16 v[160:175], v[184:187], v[88:91], v[160:175]\n" \
    "ds_read_b64_tr_b16 v[184:185], v216 offset:18432\n" \
    "ds_read_b64_tr_b16 v[186:187], v216 offset:18688\n" \
    "v_mul_f32 v152, v152, v152\n" \
    "v_mul_f32 v153, v153, v153\n" \
    "v_cvt_pk_bf16_f32 v196, v152, v153\n" \
    "s_waitcnt lgkmcnt(6)\n" \
    "v_mfma_f32_32x32x16_bf16 v[160:175], v[188:191], v[92:95], v[160:175]\n" \
    "ds_read_b64_tr_b16 v[188:189], v216 offset:19456\n" \
    "ds_read_b64_tr_b16 v[190:191], v216 offset:19712\n" \
    "s_add_u32 s44, s43, 5120\n" \
    "s_add_u32 s45, s41, 286720\n" \
    "s_mov_b32 m0, s44\n" \
    "v_max_f32 v154, 0, v154\n" \
    "s_waitcnt lgkmcnt(6)\n" \
    "v_mfma_f32_32x32x16_bf16 v[160:175], v[176:179], v[96:99], v[160:175]\n" \
    "ds_read_b64_tr_b16 v[176:177], v216 offset:20480\n" \
    "ds_read_b64_tr_b16 v[178:179], v216 offset:20736\n" \
    "buffer_load_dwordx4 %[v1], %[rs1], s45 offen lds\n" \
    "v_max_f32 v155, 0, v155\n" \
    "s_waitcnt lgkmcnt(6)\n" \
    "v_mfma_f32_32x32x16_bf16 v[160:175], v[180:183], v[100:103], v[160:175]\n" \
    "ds_read_b64_tr_b16 v[180:181], v216 offset:21504\n" \
    "ds_read_b64_tr_b16 v[182:183], v216 offset:21760\n" \
    "v_mul_f32 v154, v154, v154\n" \
    "v_mul_f32 v155, v155, v155\n" \
    "v_cvt_pk_bf16_f32 v197, v154, v155\n" \
    "s_waitcnt lgkmcnt(6)\n" \
    "v_mfma_f32_32x32x16_bf16 v[160:175], v[184:187], v[104:107], v[160:175]\n" \
    "ds_read_b64_tr_b16 v[184:185], v216 offset:22528\n" \
    "ds_read_b64_tr_b16 v[186:187], v216 offset:22784\n" \
    "s_add_u32 s44, s43, 6144\n" \
    "s_add_u32 s45, s41, 344064\n" \
    "s_mov_b32 m0, s44\n" \
    "v_max_f32 v156, 0, v156\n" \
    "s_waitcnt lgkmcnt(6)\n" \
    "v_mfma_f32_32x32x16_bf16 v[160:175], v[188:191], v[108:111], v[160:175]\n" \
    "ds_read_b64_tr_b16 v[188:189], v216 offset:23552\n" \
    "ds_read_b64_tr_b16 v[190:191], v216 offset:23808\n" \
    "buffer_load_dwordx4 %[v1], %[rs1], s45 offen lds\n" \
    "v_max_f32 v157, 0, v157\n" \
    "s_waitcnt lgkmcnt(6)\n" \
    "v_mfma_f32_32x32x16_bf16 v[160:175], v[176:179], v[112:115], v[160:175]\n" \
    "ds_read_b64_tr_b16 v[176:177], v216 offset:24576\n" \
    "ds_read_b64_tr_b16 v[178:179], v216 offset:24832\n" \
    "v_mul_f32 v156, v156, v156\n" \
    "v_mul_f32 v157, v157, v157\n" \
    "v_cvt_pk_bf16_f32 v198, v156, v157\n" \
    "s_waitcnt lgkmcnt(6)\n" \
    "v_mfma_f32_32x32x16_bf16 v[160:175], v[180:183], v[116:119], v[160:175]\n" \
    "ds_read_b64_tr_b16 v[180:181], v216 offset:25600\n" \
    "ds_read_b64_tr_b16 v[182:183], v216 offset:25856\n" \
    "s_add_u32 s44, s43, 28672\n" \
    "s_add_u32 s45, s42, s46\n" \
    "s_mov_b32 m0, s44\n" \
    "v_max_f32 v158, 0, v158\n" \
    "s_waitcnt lgkmcnt(6)\n" \
    "v_mfma_f32_32x32x16_bf16 v[160:175], v[184:187], v[120:123], v[160:175]\n" \
    "ds_read_b64_tr_b16 v[184:185], v216 offset:26624\n" \
    "ds_read_b64_tr_b16 v[186:187], v216 offset:26880\n" \
    "buffer_load_dwordx4 %[v2], %[rs2], s45 offen lds\n" \
    "v_max_f32 v159, 0, v159\n" \
    "s_waitcnt lgkmcnt(6)\n" \
    "v_mfma_f32_32x32x16_bf16 v[160:175], v[188:191], v[124:127], v[160:175]\n" \
    "ds_read_b64_tr_b16 v[188:189], v216 offset:27648\n" \
    "ds_read_b64_tr_b16 v[190:191], v216 offset:27904\n" \
    "v_mul_f32 v158, v158, v158\n" \
    "v_mul_f32 v159, v159, v159\n" \
    "v_cvt_pk_bf16_f32 v199, v158, v159\n" \
    "s_waitcnt lgkmcnt(6)\n" \
    "v_mfma_f32_32x32x16_bf16 v[160:175], v[176:179], v[128:131], v[160:175]\n" \
    "ds_read_b64_tr_b16 v[176:177], v217 offset:0\n" \
    "ds_read_b64_tr_b16 v[178:179], v217 offset:512\n" \
    "s_add_u32 s44, s43, 29696\n" \
    "s_add_u32 s45, s42, s47\n" \
    "s_mov_b32 m0, s44\n" \
    "s_waitcnt lgkmcnt(6)\n" \
    "v_mfma_f32_32x32x16_bf16 v[160:175], v[180:183], v[132:135], v[160:175]\n" \
    "ds_read_b64_tr_b16 v[180:181], v217 offset:1024\n" \
    "ds_read_b64_tr_b16 v[182:183], v217 offset:1536\n" \
    "buffer_load_dwordx4 %[v2], %[rs2], s45 offen lds\n" \
    "s_waitcnt lgkmcnt(6)\n" \
    "v_mfma_f32_32x32x16_bf16 v[160:175], v[184:187], v[136:139], v[160:175]\n" \
    "ds_read_b64_tr_b16 v[184:185], v217 offset:2048\n" \
    "ds_read_b64_tr_b16 v[186:187], v217 offset:2560\n" \
    "s_waitcnt lgkmcnt(6)\n" \
    "v_mfma_f32_32x32x16_bf16 v[160:175], v[188:191], v[140:143], v[160:175]\n" \
    "ds_read_b64_tr_b16 v[188:189], v217 offset:3072\n" \
    "ds_read_b64_tr_b16 v[190:191], v217 offset:3584\n" \
    "s_add_u32 s44, s43, 30720\n" \
    "s_add_u32 s45, s42, s48\n" \
    "s_mov_b32 m0, s44\n" \
    "s_waitcnt lgkmcnt(6)\n" \
    "v_mfma_f32_32x32x16_bf16 a[0:15], v[176:179], v[192:195], a[0:15]\n" \
    "ds_read_b64_tr_b16 v[176:177], v217 offset:4096\n" \
    "ds_read_b64_tr_b16 v[178:179], v217 offset:4608\n" \
    "buffer_load_dwordx4 %[v2], %[rs2], s45 offen lds\n" \
    "s_waitcnt lgkmcnt(6)\n" \
    "v_mfma_f32_32x32x16_bf16 a[0:15], v[180:183], v[196:199], a[0:15]\n" \
    "ds_read_b64_tr_b16 v[180:181], v217 offset:5120\n" \
    "ds_read_b64_tr_b16 v[182:183], v217 offset:5632\n" \
    "s_waitcnt lgkmcnt(6)\n" \
    "v_mfma_f32_32x32x16_bf16 a[16:31], v[184:187], v[192:195], a[16:31]\n" \
    "ds_read_b64_tr_b16 v[184:185], v217 offset:6144\n" \
    "ds_read_b64_tr_b16 v[186:187], v217 offset:6656\n" \
    "s_add_u32 s44, s43, 31744\n" \
    "s_add_u32 s45, s42, s49\n" \
    "s_mov_b32 m0, s44\n" \
    "s_waitcnt lgkmcnt(6)\n" \
    "v_mfma_f32_32x32x16_bf16 a[16:31], v[188:191], v[196:199], a[16:31]\n" \
    "ds_read_b64_tr_b16 v[188:189], v217 offset:7168\n" \
    "ds_read_b64_tr_b16 v[190:191], v217 offset:7680\n" \
    "buffer_load_dwordx4 %[v2], %[rs2], s45 offen lds\n" \
    "s_waitcnt lgkmcnt(6)\n" \
    "v_mfma_f32_32x32x16_bf16 a[32:47], v[176:179], v[192:195], a[32:47]\n" \
    "ds_read_b64_tr_b16 v[176:177], v217 offset:8192\n" \
    "ds_read_b64_tr_b16 v[178:179], v217 offset:8704\n" \
    "s_waitcnt lgkmcnt(6)\n" \
    "v_mfma_f32_32x32x16_bf16 a[32:47], v[180:183], v[196:199], a[32:47]\n" \
    "ds_read_b64_tr_b16 v[180:181], v217 offset:9216\n" \
    "ds_read_b64_tr_b16 v[182:183], v217 offset:9728\n" \
    "s_add_u32 s44, s43, 32768\n" \
    "s_add_u32 s45, s42, s50\n" \
    "s_mov_b32 m0, s44\n" \
    "s_waitcnt lgkmcnt(6)\n" \
    "v_mfma_f32_32x32x16_bf16 a[48:63], v[184:187], v[192:195], a[48:63]\n" \
    "ds_read_b64_tr_b16 v[184:185], v217 offset:10240\n" \
    "ds_read_b64_tr_b16 v[186:187], v217 offset:10752\n" \
    "buffer_load_dwordx4 %[v2], %[rs2], s45 offen lds\n" \
    "s_waitcnt lgkmcnt(6)\n" \
    "v_mfma_f32_32x32x16_bf16 a[48:63], v[188:191], v[196:199], a[48:63]\n" \
    "ds_read_b64_tr_b16 v[188:189], v217 offset:11264\n" \
    "ds_read_b64_tr_b16 v[190:191], v217 offset:11776\n" \
    "s_waitcnt lgkmcnt(6)\n" \
    "v_mfma_f32_32x32x16_bf16 a[64:79], v[176:179], v[192:195], a[64:79]\n" \
    "ds_read_b64_tr_b16 v[176:177], v217 offset:12288\n" \
    "ds_read_b64_tr_b16 v[178:179], v217 offset:12800\n" \
    "s_add_u32 s44, s43, 33792\n" \
    "s_add_u32 s45, s42, s51\n" \
    "s_mov_b32 m0, s44\n" \
    "s_waitcnt lgkmcnt(6)\n" \
    "v_mfma_f32_32x32x16_bf16 a[64:79], v[180:183], v[196:199], a[64:79]\n" \
    "ds_read_b64_tr_b16 v[180:181], v217 offset:13312\n" \
    "ds_read_b64_tr_b16 v[182:183], v217 offset:13824\n" \
    "buffer_load_dwordx4 %[v2], %[rs2], s45 offen lds\n" \
    "s_waitcnt lgkmcnt(6)\n" \
    "v_mfma_f32_32x32x16_bf16 a[80:95], v[184:187], v[192:195], a[80:95]\n" \
    "ds_read_b64_tr_b16 v[184:185], v217 offset:14336\n" \
    "ds_read_b64_tr_b16 v[186:187], v217 offset:14848\n" \
    "s_waitcnt lgkmcnt(6)\n" \
    "v_mfma_f32_32x32x16_bf16 a[80:95], v[188:191], v[196:199], a[80:95]\n" \
    "ds_read_b64_tr_b16 v[188:189], v217 offset:15360\n" \
    "ds_read_b64_tr_b16 v[190:191], v217 offset:15872\n" \
    "s_add_u32 s44, s43, 34816\n" \
    "s_add_u32 s45, s42, s52\n" \
    "s_mov_b32 m0, s44\n" \
    "s_waitcnt lgkmcnt(6)\n" \
    "v_mfma_f32_32x32x16_bf16 a[96:111], v[176:179], v[192:195], a[96:111]\n" \
    "ds_read_b64_tr_b16 v[176:177], v217 offset:16384\n" \
    "ds_read_b64_tr_b16 v[178:179], v217 offset:16896\n" \
    "buffer_load_dwordx4 %[v2], %[rs2], s45 offen lds\n" \
    "s_waitcnt lgkmcnt(6)\n" \
    "v_mfma_f32_32x32x16_bf16 a[96:111], v[180:183], v[196:199], a[96:111]\n" \
    "ds_read_b64_tr_b16 v[180:181], v217 offset:17408\n" \
    "ds_read_b64_tr_b16 v[182:183], v217 offset:17920\n" \
    "s_waitcnt lgkmcnt(6)\n" \
    "v_mfma_f32_32x32x16_bf16 a[112:127], v[184:187], v[192:195], a[112:127]\n" \
    "ds_read_b64_tr_b16 v[184:185], v217 offset:18432\n" \
    "ds_read_b64_tr_b16 v[186:187], v217 offset:18944\n" \
    "s_waitcnt lgkmcnt(6)\n" \
    "v_mfma_f32_32x32x16_bf16 a[112:127], v[188:191], v[196:199], a[112:127]\n" \
    "ds_read_b64_tr_b16 v[188:189], v217 offset:19456\n" \
    "ds_read_b64_tr_b16 v[190:191], v217 offset:19968\n" \
    "s_waitcnt lgkmcnt(6)\n" \
    "v_mfma_f32_32x32x16_bf16 a[128:143], v[176:179], v[192:195], a[128:143]\n" \
    "ds_read_b64_tr_b16 v[176:177], v217 offset:20480\n" \
    "ds_read_b64_tr_b16 v[178:179], v217 offset:20992\n" \
    "s_waitcnt lgkmcnt(6)\n" \
    "v_mfma_f32_32x32x16_bf16 a[128:143], v[180:183], v[196:199], a[128:143]\n" \
    "ds_read_b64_tr_b16 v[180:181], v217 offset:21504\n" \
    "ds_read_b64_tr_b16 v[182:183], v217 offset:22016\n" \
    "s_waitcnt lgkmcnt(6)\n" \
    "v_mfma_f32_32x32x16_bf16 a[144:159], v[184:187], v[192:195], a[144:159]\n" \
    "ds_read_b64_tr_b16 v[184:185], v217 offset:22528\n" \
    "ds_read_b64_tr_b16 v[186:187], v217 offset:23040\n" \
    "s_waitcnt lgkmcnt(6)\n" \
    "v_mfma_f32_32x32x16_bf16 a[144:159], v[188:191], v[196:199], a[144:159]\n" \
    "ds_read_b64_tr_b16 v[188:189], v217 offset:23552\n" \
    "ds_read_b64_tr_b16 v[190:191], v217 offset:24064\n" \
    "s_waitcnt lgkmcnt(6)\n" \
    "v_mfma_f32_32x32x16_bf16 a[160:175], v[176:179], v[192:195], a[160:175]\n" \
    "ds_read_b64_tr_b16 v[176:177], v217 offset:24576\n" \
    "ds_read_b64_tr_b16 v[178:179], v217 offset:25088\n" \
    "s_waitcnt lgkmcnt(6)\n" \
    "v_mfma_f32_32x32x16_bf16 a[160:175], v[180:183], v[196:199], a[160:175]\n" \
    "ds_read_b64_tr_b16 v[180:181], v217 offset:25600\n" \
    "ds_read_b64_tr_b16 v[182:183], v217 offset:26112\n" \
    "s_waitcnt lgkmcnt(6)\n" \
    "v_mfma_f32_32x32x16_bf16 a[176:191], v[184:187], v[192:195], a[176:191]\n" \
    "ds_read_b64_tr_b16 v[184:185], v217 offset:26624\n" \
    "ds_read_b64_tr_b16 v[186:187], v217 offset:27136\n" \
    "s_waitcnt lgkmcnt(6)\n" \
    "v_mfma_f32_32x32x16_bf16 a[176:191], v[188:191], v[196:199], a[176:191]\n" \
    "ds_read_b64_tr_b16 v[188:189], v217 offset:27648\n" \
    "ds_read_b64_tr_b16 v[190:191], v217 offset:28160\n" \
    "s_waitcnt lgkmcnt(6)\n" \
    "v_mfma_f32_32x32x16_bf16 a[192:207], v[176:179], v[192:195], a[192:207]\n" \
    "s_waitcnt lgkmcnt(4)\n" \
    "v_mfma_f32_32x32x16_bf16 a[192:207], v[180:183], v[196:199], a[192:207]\n" \
    "s_waitcnt lgkmcnt(2)\n" \
    "v_mfma_f32_32x32x16_bf16 a[208:223], v[184:187], v[192:195], a[208:223]\n" \
    "s_waitcnt lgkmcnt(0)\n" \
    "v_mfma_f32_32x32x16_bf16 a[208:223], v[188:191], v[196:199], a[208:223]\n" \
    "s_add_u32 s40, s40, 1\n" \
    "s_cmp_lt_u32 s40, 13\n" \
    "s_cselect_b32 s44, 64, 0\n" \
    "s_add_u32 s41, s41, s44\n" \
    "s_add_u32 s42, s42, 28672\n" \
    "v_subrev_u32 v216, 57344, v216\n" \
    "v_subrev_u32 v217, 57344, v217\n" \
    "v_add_u32 v218, 128, v218\n" \
    "s_add_u32 s43, s43, 57344\n" \
    "s_cmp_eq_u32 s40, 14\n" \
    "s_cbranch_scc1 Fend_%=\n" \
    "s_waitcnt vmcnt(0)\n" \
    "s_barrier\n" \
    "ds_read_b128 v[200:203], v218 offset:0\n" \
    "ds_read_b128 v[204:207], v218 offset:16\n" \
    "ds_read_b128 v[208:211], v218 offset:32\n" \
    "ds_read_b128 v[212:215], v218 offset:48\n" \
    "ds_read_b64_tr_b16 v[176:177], v216 offset:0\n" \
    "ds_read_b64_tr_b16 v[178:179], v216 offset:256\n" \
    "ds_read_b64_tr_b16 v[180:181], v216 offset:1024\n" \
    "ds_read_b64_tr_b16 v[182:183], v216 offset:1280\n" \
    "ds_read_b64_tr_b16 v[184:185], v216 offset:2048\n" \
    "ds_read_b64_tr_b16 v[186:187], v216 offset:2304\n" \
    "ds_read_b64_tr_b16 v[188:189], v216 offset:3072\n" \
    "ds_read_b64_tr_b16 v[190:191], v216 offset:3328\n" \
    "s_waitcnt lgkmcnt(6)\n" \
    "v_mfma_f32_32x32x16_bf16 v[144:159], v[176:179], v[32:35], v[200:215]\n" \
    "ds_read_b64_tr_b16 v[176:177], v216 offset:4096\n" \
    "ds_read_b64_tr_b16 v[178:179], v216 offset:4352\n" \
    "s_add_u32 s44, s43, 0\n" \
    "s_add_u32 s45, s41, 0\n" \
    "s_mov_b32 m0, s44\n" \
    "v_max_f32 v160, 0, v160\n" \
    "s_waitcnt lgkmcnt(6)\n" \
    "v_mfma_f32_32x32x16_bf16 v[144:159], v[180:183], v[36:39], v[144:159]\n" \
    "ds_read_b64_tr_b16 v[180:181], v216 offset:5120\n" \
    "ds_read_b64_tr_b16 v[182:183], v216 offset:5376\n" \
    "buffer_load_dwordx4 %[v1], %[rs1], s45 offen lds\n" \
    "v_max_f32 v161, 0, v161\n" \
    "s_waitcnt lgkmcnt(6)\n" \
    "v_mfma_f32_32x32x16_bf16 v[144:159], v[184:187], v[40:43], v[144:159]\n" \
    "ds_read_b64_tr_b16 v[184:185], v216 offset:6144\n" \
    "ds_read_b64_tr_b16 v[186:187], v216 offset:6400\n" \
    "v_mul_f32 v160, v160, v160\n" \
    "v_mul_f32 v161, v161, v161\n" \
    "v_cvt_pk_bf16_f32 v192, v160, v161\n" \
    "s_waitcnt lgkmcnt(6)\n" \
    "v_mfma_f32_32x32x16_bf16 v[144:159], v[188:191], v[44:47], v[144:159]\n" \
    "ds_read_b64_tr_b16 v[188:189], v216 offset:7168\n" \
    "ds_read_b64_tr_b16 v[190:191], v216 offset:7424\n" \
    "s_add_u32 s44, s43, 1024\n" \
    "s_add_u32 s45, s41, 57344\n" \
    "s_mov_b32 m0, s44\n" \
    "v_max_f32 v162, 0, v162\n" \
    "s_waitcnt lgkmcnt(6)\n" \
    "v_mfma_f32_32x32x16_bf16 v[144:159], v[176:179], v[48:51], v[144:159]\n" \
    "ds_read_b64_tr_b16 v[176:177], v216 offset:8192\n" \
    "ds_read_b64_tr_b16 v[178:179], v216 offset:8448\n" \
    "buffer_load_dwordx4 %[v1], %[rs1], s45 offen lds\n" \
    "v_max_f32 v163, 0, v163\n" \
    "s_waitcnt lgkmcnt(6)\n" \
    "v_mfma_f32_32x32x16_bf16 v[144:159], v[180:183], v[52:55], v[144:159]\n" \
    "ds_read_b64_tr_b16 v[180:181], v216 offset:9216\n" \
    "ds_read_b64_tr_b16 v[182:183], v216 offset:9472\n" \
    "v_mul_f32 v162, v162, v162\n" \
    "v_mul_f32 v163, v163, v163\n" \
    "v_cvt_pk_bf16_f32 v193, v162, v163\n" \
    "s_waitcnt lgkmcnt(6)\n" \
    "v_mfma_f32_32x32x16_bf16 v[144:159], v[184:187], v[56:59], v[144:159]\n" \
    "ds_read_b64_tr_b16 v[184:185], v216 offset:10240\n" \
    "ds_read_b64_tr_b16 v[186:187], v216 offset:10496\n" \
    "s_add_u32 s44, s43, 2048\n" \
    "s_add_u32 s45, s41, 114688\n" \
    "s_mov_b32 m0, s44\n" \
    "v_max_f32 v164, 0, v164\n" \
    "s_waitcnt lgkmcnt(6)\n" \
    "v_mfma_f32_32x32x16_bf16 v[144:159], v[188:191], v[60:63], v[144:159]\n" \
    "ds_read_b64_tr_b16 v[188:189], v216 offset:11264\n" \
    "ds_read_b64_tr_b16 v[190:191], v216 offset:11520\n" \
    "buffer_load_dwordx4 %[v1], %[rs1], s45 offen lds\n" \
    "v_max_f32 v165, 0, v165\n" \
    "s_waitcnt lgkmcnt(6)\n" \
    "v_mfma_f32_32x32x16_bf16 v[144:159], v[176:179], v[64:67], v[144:159]\n" \
    "ds_read_b64_tr_b16 v[176:177], v216 offset:12288\n" \
    "ds_read_b64_tr_b16 v[178:179], v216 offset:12544\n" \
    "v_mul_f32 v164, v164, v164\n" \
    "v_mul_f32 v165, v165, v165\n" \
    "v_cvt_pk_bf16_f32 v194, v164, v165\n" \
    "s_waitcnt lgkmcnt(6)\n" \
    "v_mfma_f32_32x32x16_bf16 v[144:159], v[180:183], v[68:71], v[144:159]\n" \
    "ds_read_b64_tr_b16 v[180:181], v216 offset:13312\n" \
    "ds_read_b64_tr_b16 v[182:183], v216 offset:13568\n" \
    "s_add_u32 s44, s43, 3072\n" \
    "s_add_u32 s45, s41, 172032\n" \
    "s_mov_b32 m0, s44\n" \
    "v_max_f32 v166, 0, v166\n" \
    "s_waitcnt lgkmcnt(6)\n" \
    "v_mfma_f32_32x32x16_bf16 v[144:159], v[184:187], v[72:75], v[144:159]\n" \
    "ds_read_b64_tr_b16 v[184:185], v216 offset:14336\n" \
    "ds_read_b64_tr_b16 v[186:187], v216 offset:14592\n" \
    "buffer_load_dwordx4 %[v1], %[rs1], s45 offen lds\n" \
    "v_max_f32 v167, 0, v167\n" \
    "s_waitcnt lgkmcnt(6)\n" \
    "v_mfma_f32_32x32x16_bf16 v[144:159], v[188:191], v[76:79], v[144:159]\n" \
    "ds_read_b64_tr_b16 v[188:189], v216 offset:15360\n" \
    "ds_read_b64_tr_b16 v[190:191], v216 offset:15616\n" \
    "v_mul_f32 v166, v166, v166\n" \
    "v_mul_f32 v167, v167, v167\n" \
    "v_cvt_pk_bf16_f32 v195, v166, v167\n" \
    "s_waitcnt lgkmcnt(6)\n" \
    "v_mfma_f32_32x32x16_bf16 v[144:159], v[176:179], v[80:83], v[144:159]\n" \
    "ds_read_b64_tr_b16 v[176:177], v216 offset:16384\n" \
    "ds_read_b64_tr_b16 v[178:179], v216 offset:16640\n" \
    "s_add_u32 s44, s43, 4096\n" \
    "s_add_u32 s45, s41, 229376\n" \
    "s_mov_b32 m0, s44\n" \
    "v_max_f32 v168, 0, v168\n" \
    "s_waitcnt lgkmcnt(6)\n" \
    "v_mfma_f32_32x32x16_bf16 v[144:159], v[180:183], v[84:87], v[144:159]\n" \
    "ds_read_b64_tr_b16 v[180:181], v216 offset:17408\n" \
    "ds_read_b64_tr_b16 v[182:183], v216 offset:17664\n" \
    "buffer_load_dwordx4 %[v1], %[rs1], s45 offen lds\n" \
    "v_max_f32 v169, 0, v169\n" \
    "s_waitcnt lgkmcnt(6)\n" \
    "v_mfma_f32_32x32x16_bf16 v[144:159], v[184:187], v[88:91], v[144:159]\n" \
    "ds_read_b64_tr_b16 v[184:185], v216 offset:18432\n" \
    "ds_read_b64_tr_b16 v[186:187], v216 offset:18688\n" \
    "v_mul_f32 v168, v168, v168\n" \
    "v_mul_f32 v169, v169, v169\n" \
    "v_cvt_pk_bf16_f32 v196, v168, v169\n" \
    "s_waitcnt lgkmcnt(6)\n" \
    "v_mfma_f32_32x32x16_bf16 v[144:159], v[188:191], v[92:95], v[144:159]\n" \
    "ds_read_b64_tr_b16 v[188:189], v216 offset:19456\n" \
    "ds_read_b64_tr_b16 v[190:191], v216 offset:19712\n" \
    "s_add_u32 s44, s43, 5120\n" \
    "s_add_u32 s45, s41, 286720\n" \
    "s_mov_b32 m0, s44\n" \
    "v_max_f32 v170, 0, v170\n" \
    "s_waitcnt lgkmcnt(6)\n" \
    "v_mfma_f32_32x32x16_bf16 v[144:159], v[176:179], v[96:99], v[144:159]\n" \
    "ds_read_b64_tr_b16 v[176:177], v216 offset:20480\n" \
    "ds_read_b64_tr_b16 v[178:179], v216 offset:20736\n" \
    "buffer_load_dwordx4 %[v1], %[rs1], s45 offen lds\n" \
    "v_max_f32 v171, 0, v171\n" \
    "s_waitcnt lgkmcnt(6)\n" \
    "v_mfma_f32_32x32x16_bf16 v[144:159], v[180:183], v[100:103], v[144:159]\n" \
    "ds_read_b64_tr_b16 v[180:181], v216 offset:21504\n" \
    "ds_read_b64_tr_b16 v[182:183], v216 offset:21760\n" \
    "v_mul_f32 v170, v170, v170\n" \
    "v_mul_f32 v171, v171, v171\n" \
    "v_cvt_pk_bf16_f32 v197, v170, v171\n" \
    "s_waitcnt lgkmcnt(6)\n" \
    "v_mfma_f32_32x32x16_bf16 v[144:159], v[184:187], v[104:107], v[144:159]\n" \
    "ds_read_b64_tr_b16 v[184:185], v216 offset:22528\n" \
    "ds_read_b64_tr_b16 v[186:187], v216 offset:22784\n" \
    "s_add_u32 s44, s43, 6144\n" \
    "s_add_u32 s45, s41, 344064\n" \
    "s_mov_b32 m0, s44\n" \
    "v_max_f32 v172, 0, v172\n" \
    "s_waitcnt lgkmcnt(6)\n" \
    "v_mfma_f32_32x32x16_bf16 v[144:159], v[188:191], v[108:111], v[144:159]\n" \
    "ds_read_b64_tr_b16 v[188:189], v216 offset:23552\n" \
    "ds_read_b64_tr_b16 v[190:191], v216 offset:23808\n" \
    "buffer_load_dwordx4 %[v1], %[rs1], s45 offen lds\n" \
    "v_max_f32 v173, 0, v173\n" \
    "s_waitcnt lgkmcnt(6)\n" \
    "v_mfma_f32_32x32x16_bf16 v[144:159], v[176:179], v[112:115], v[144:159]\n" \
    "ds_read_b64_tr_b16 v[176:177], v216 offset:24576\n" \
    "ds_read_b64_tr_b16 v[178:179], v216 offset:24832\n" \
    "v_mul_f32 v172, v172, v172\n" \
    "v_mul_f32 v173, v173, v173\n" \
    "v_cvt_pk_bf16_f32 v198, v172, v173\n" \
    "s_waitcnt lgkmcnt(6)\n" \
    "v_mfma_f32_32x32x16_bf16 v[144:159], v[180:183], v[116:119], v[144:159]\n" \
    "ds_read_b64_tr_b16 v[180:181], v216 offset:25600\n" \
    "ds_read_b64_tr_b16 v[182:183], v216 offset:25856\n" \
    "s_add_u32 s44, s43, 28672\n" \
    "s_add_u32 s45, s42, s46\n" \
    "s_mov_b32 m0, s44\n" \
    "v_max_f32 v174, 0, v174\n" \
    "s_waitcnt lgkmcnt(6)\n" \
    "v_mfma_f32_32x32x16_bf16 v[144:159], v[184:187], v[120:123], v[144:159]\n" \
    "ds_read_b64_tr_b16 v[184:185], v216 offset:26624\n" \
    "ds_read_b64_tr_b16 v[186:187], v216 offset:26880\n" \
    "buffer_load_dwordx4 %[v2], %[rs2], s45 offen lds\n" \
    "v_max_f32 v175, 0, v175\n" \
    "s_waitcnt lgkmcnt(6)\n" \
    "v_mfma_f32_32x32x16_bf16 v[144:159], v[188:191], v[124:127], v[144:159]\n" \
    "ds_read_b64_tr_b16 v[188:189], v216 offset:27648\n" \
    "ds_read_b64_tr_b16 v[190:191], v216 offset:27904\n" \
    "v_mul_f32 v174, v174, v174\n" \
    "v_mul_f32 v175, v175, v175\n" \
    "v_cvt_pk_bf16_f32 v199, v174, v175\n" \
    "s_waitcnt lgkmcnt(6)\n" \
    "v_mfma_f32_32x32x16_bf16 v[144:159], v[176:179], v[128:131], v[144:159]\n" \
    "ds_read_b64_tr_b16 v[176:177], v217 offset:0\n" \
    "ds_read_b64_tr_b16 v[178:179], v217 offset:512\n" \
    "s_add_u32 s44, s43, 29696\n" \
    "s_add_u32 s45, s42, s47\n" \
    "s_mov_b32 m0, s44\n" \
    "s_waitcnt lgkmcnt(6)\n" \
    "v_mfma_f32_32x32x16_bf16 v[144:159], v[180:183], v[132:135], v[144:159]\n" \
    "ds_read_b64_tr_b16 v[180:181], v217 offset:1024\n" \
    "ds_read_b64_tr_b16 v[182:183], v217 offset:1536\n" \
    "buffer_load_dwordx4 %[v2], %[rs2], s45 offen lds\n" \
    "s_waitcnt lgkmcnt(6)\n" \
    "v_mfma_f32_32x32x16_bf16 v[144:159], v[184:187], v[136:139], v[144:159]\n" \
    "ds_read_b64_tr_b16 v[184:185], v217 offset:2048\n" \
    "ds_read_b64_tr_b16 v[186:187], v217 offset:2560\n" \
    "s_waitcnt lgkmcnt(6)\n" \
    "v_mfma_f32_32x32x16_bf16 v[144:159], v[188:191], v[140:143], v[144:159]\n" \
    "ds_read_b64_tr_b16 v[188:189], v217 offset:3072\n" \
    "ds_read_b64_tr_b16 v[190:191], v217 offset:3584\n" \
    "s_add_u32 s44, s43, 30720\n" \
    "s_add_u32 s45, s42, s48\n" \
    "s_mov_b32 m0, s44\n" \
    "s_waitcnt lgkmcnt(6)\n" \
    "v_mfma_f32_32x32x16_bf16 a[0:15], v[176:179], v[192:195], a[0:15]\n" \
    "ds_read_b64_tr_b16 v[176:177], v217 offset:4096\n" \
    "ds_read_b64_tr_b16 v[178:179], v217 offset:4608\n" \
    "buffer_load_dwordx4 %[v2], %[rs2], s45 offen lds\n" \
    "s_waitcnt lgkmcnt(6)\n" \
    "v_mfma_f32_32x32x16_bf16 a[0:15], v[180:183], v[196:199], a[0:15]\n" \
    "ds_read_b64_tr_b16 v[180:181], v217 offset:5120\n" \
    "ds_read_b64_tr_b16 v[182:183], v217 offset:5632\n" \
    "s_waitcnt lgkmcnt(6)\n" \
    "v_mfma_f32_32x32x16_bf16 a[16:31], v[184:187], v[192:195], a[16:31]\n" \
    "ds_read_b64_tr_b16 v[184:185], v217 offset:6144\n" \
    "ds_read_b64_tr_b16 v[186:187], v217 offset:6656\n" \
    "s_add_u32 s44, s43, 31744\n" \
    "s_add_u32 s45, s42, s49\n" \
    "s_mov_b32 m0, s44\n" \
    "s_waitcnt lgkmcnt(6)\n" \
    "v_mfma_f32_32x32x16_bf16 a[16:31], v[188:191], v[196:199], a[16:31]\n" \
    "ds_read_b64_tr_b16 v[188:189], v217 offset:7168\n" \
    "ds_read_b64_tr_b16 v[190:191], v217 offset:7680\n" \
    "buffer_load_dwordx4 %[v2], %[rs2], s45 offen lds\n" \
    "s_waitcnt lgkmcnt(6)\n" \
    "v_mfma_f32_32x32x16_bf16 a[32:47], v[176:179], v[192:195], a[32:47]\n" \
    "ds_read_b64_tr_b16 v[176:177], v217 offset:8192\n" \
    "ds_read_b64_tr_b16 v[178:179], v217 offset:8704\n" \
    "s_waitcnt lgkmcnt(6)\n" \
    "v_mfma_f32_32x32x16_bf16 a[32:47], v[180:183], v[196:199], a[32:47]\n" \
    "ds_read_b64_tr_b16 v[180:181], v217 offset:9216\n" \
    "ds_read_b64_tr_b16 v[182:183], v217 offset:9728\n" \
    "s_add_u32 s44, s43, 32768\n" \
    "s_add_u32 s45, s42, s50\n" \
    "s_mov_b32 m0, s44\n" \
    "s_waitcnt lgkmcnt(6)\n" \
    "v_mfma_f32_32x32x16_bf16 a[48:63], v[184:187], v[192:195], a[48:63]\n" \
    "ds_read_b64_tr_b16 v[184:185], v217 offset:10240\n" \
    "ds_read_b64_tr_b16 v[186:187], v217 offset:10752\n" \
    "buffer_load_dwordx4 %[v2], %[rs2], s45 offen lds\n" \
    "s_waitcnt lgkmcnt(6)\n" \
    "v_mfma_f32_32x32x16_bf16 a[48:63], v[188:191], v[196:199], a[48:63]\n" \
    "ds_read_b64_tr_b16 v[188:189], v217 offset:11264\n" \
    "ds_read_b64_tr_b16 v[190:191], v217 offset:11776\n" \
    "s_waitcnt lgkmcnt(6)\n" \
    "v_mfma_f32_32x32x16_bf16 a[64:79], v[176:179], v[192:195], a[64:79]\n" \
    "ds_read_b64_tr_b16 v[176:177], v217 offset:12288\n" \
    "ds_read_b64_tr_b16 v[178:179], v217 offset:12800\n" \
    "s_add_u32 s44, s43, 33792\n" \
    "s_add_u32 s45, s42, s51\n" \
    "s_mov_b32 m0, s44\n" \
    "s_waitcnt lgkmcnt(6)\n" \
    "v_mfma_f32_32x32x16_bf16 a[64:79], v[180:183], v[196:199], a[64:79]\n" \
    "ds_read_b64_tr_b16 v[180:181], v217 offset:13312\n" \
    "ds_read_b64_tr_b16 v[182:183], v217 offset:13824\n" \
    "buffer_load_dwordx4 %[v2], %[rs2], s45 offen lds\n" \
    "s_waitcnt lgkmcnt(6)\n" \
    "v_mfma_f32_32x32x16_bf16 a[80:95], v[184:187], v[192:195], a[80:95]\n" \
    "ds_read_b64_tr_b16 v[184:185], v217 offset:14336\n" \
    "ds_read_b64_tr_b16 v[186:187], v217 offset:14848\n" \
    "s_waitcnt lgkmcnt(6)\n" \
    "v_mfma_f32_32x32x16_bf16 a[80:95], v[188:191], v[196:199], a[80:95]\n" \
    "ds_read_b64_tr_b16 v[188:189], v217 offset:15360\n" \
    "ds_read_b64_tr_b16 v[190:191], v217 offset:15872\n" \
    "s_add_u32 s44, s43, 34816\n" \
    "s_add_u32 s45, s42, s52\n" \
    "s_mov_b32 m0, s44\n" \
    "s_waitcnt lgkmcnt(6)\n" \
    "v_mfma_f32_32x32x16_bf16 a[96:111], v[176:179], v[192:195], a[96:111]\n" \
    "ds_read_b64_tr_b16 v[176:177], v217 offset:16384\n" \
    "ds_read_b64_tr_b16 v[178:179], v217 offset:16896\n" \
    "buffer_load_dwordx4 %[v2], %[rs2], s45 offen lds\n" \
    "s_waitcnt lgkmcnt(6)\n" \
    "v_mfma_f32_32x32x16_bf16 a[96:111], v[180:183], v[196:199], a[96:111]\n" \
    "ds_read_b64_tr_b16 v[180:181], v217 offset:17408\n" \
    "ds_read_b64_tr_b16 v[182:183], v217 offset:17920\n" \
    "s_waitcnt lgkmcnt(6)\n" \
    "v_mfma_f32_32x32x16_bf16 a[112:127], v[184:187], v[192:195], a[112:127]\n" \
    "ds_read_b64_tr_b16 v[184:185], v217 offset:18432\n" \
    "ds_read_b64_tr_b16 v[186:187], v217 offset:18944\n" \
    "s_waitcnt lgkmcnt(6)\n" \
    "v_mfma_f32_32x32x16_bf16 a[112:127], v[188:191], v[196:199], a[112:127]\n" \
    "ds_read_b64_tr_b16 v[188:189], v217 offset:19456\n" \
    "ds_read_b64_tr_b16 v[190:191], v217 offset:19968\n" \
    "s_waitcnt lgkmcnt(6)\n" \
    "v_mfma_f32_32x32x16_bf16 a[128:143], v[176:179], v[192:195], a[128:143]\n" \
    "ds_read_b64_tr_b16 v[176:177], v217 offset:20480\n" \
    "ds_read_b64_tr_b16 v[178:179], v217 offset:20992\n" \
    "s_waitcnt lgkmcnt(6)\n" \
    "v_mfma_f32_32x32x16_bf16 a[128:143], v[180:183], v[196:199], a[128:143]\n" \
    "ds_read_b64_tr_b16 v[180:181], v217 offset:21504\n" \
    "ds_read_b64_tr_b16 v[182:183], v217 offset:22016\n" \
    "s_waitcnt lgkmcnt(6)\n" \
    "v_mfma_f32_32x32x16_bf16 a[144:159], v[184:187], v[192:195], a[144:159]\n" \
    "ds_read_b64_tr_b16 v[184:185], v217 offset:22528\n" \
    "ds_read_b64_tr_b16 v[186:187], v217 offset:23040\n" \
    "s_waitcnt lgkmcnt(6)\n" \
    "v_mfma_f32_32x32x16_bf16 a[144:159], v[188:191], v[196:199], a[144:159]\n" \
    "ds_read_b64_tr_b16 v[188:189], v217 offset:23552\n" \
    "ds_read_b64_tr_b16 v[190:191], v217 offset:24064\n" \
    "s_waitcnt lgkmcnt(6)\n" \
    "v_mfma_f32_32x32x16_bf16 a[160:175], v[176:179], v[192:195], a[160:175]\n" \
    "ds_read_b64_tr_b16 v[176:177], v217 offset:24576\n" \
    "ds_read_b64_tr_b16 v[178:179], v217 offset:25088\n" \
    "s_waitcnt lgkmcnt(6)\n" \
    "v_mfma_f32_32x32x16_bf16 a[160:175], v[180:183], v[196:199], a[160:175]\n" \
    "ds_read_b64_tr_b16 v[180:181], v217 offset:25600\n" \
    "ds_read_b64_tr_b16 v[182:183], v217 offset:26112\n" \
    "s_waitcnt lgkmcnt(6)\n" \
    "v_mfma_f32_32x32x16_bf16 a[176:191], v[184:187], v[192:195], a[176:191]\n" \
    "ds_read_b64_tr_b16 v[184:185], v217 offset:26624\n" \
    "ds_read_b64_tr_b16 v[186:187], v217 offset:27136\n" \
    "s_waitcnt lgkmcnt(6)\n" \
    "v_mfma_f32_32x32x16_bf16 a[176:191], v[188:191], v[196:199], a[176:191]\n" \
    "ds_read_b64_tr_b16 v[188:189], v217 offset:27648\n" \
    "ds_read_b64_tr_b16 v[190:191], v217 offset:28160\n" \
    "s_waitcnt lgkmcnt(6)\n" \
    "v_mfma_f32_32x32x16_bf16 a[192:207], v[176:179], v[192:195], a[192:207]\n" \
    "s_waitcnt lgkmcnt(4)\n" \
    "v_mfma_f32_32x32x16_bf16 a[192:207], v[180:183], v[196:199], a[192:207]\n" \
    "s_waitcnt lgkmcnt(2)\n" \
    "v_mfma_f32_32x32x16_bf16 a[208:223], v[184:187], v[192:195], a[208:223]\n" \
    "s_waitcnt lgkmcnt(0)\n" \
    "v_mfma_f32_32x32x16_bf16 a[208:223], v[188:191], v[196:199], a[208:223]\n" \
    "s_add_u32 s40, s40, 1\n" \
    "s_cmp_lt_u32 s40, 13\n" \
    "s_cselect_b32 s44, 64, 0\n" \
    "s_add_u32 s41, s41, s44\n" \
    "s_add_u32 s42, s42, 28672\n" \
    "v_add_u32 v216, 57344, v216\n" \
    "v_add_u32 v217, 57344, v217\n" \
    "v_add_u32 v218, 128, v218\n" \
    "s_sub_u32 s43, s43, 57344\n" \
    "s_branch Floop_%=\n" \
    "Fend_%=:\n" \
    "s_waitcnt vmcnt(0)\n" \
    "s_barrier\n" \
    "ds_read_b64_tr_b16 v[176:177], v217 offset:0\n" \
    "ds_read_b64_tr_b16 v[178:179], v217 offset:512\n" \
    "ds_read_b64_tr_b16 v[180:181], v217 offset:1024\n" \
    "ds_read_b64_tr_b16 v[182:183], v217 offset:1536\n" \
    "ds_read_b64_tr_b16 v[184:185], v217 offset:2048\n" \
    "ds_read_b64_tr_b16 v[186:187], v217 offset:2560\n" \
    "s_nop 7\n" \
    "v_max_f32 v160, 0, v160\n" \
    "v_max_f32 v161, 0, v161\n" \
    "v_mul_f32 v160, v160, v160\n" \
    "v_mul_f32 v161, v161, v161\n" \
    "v_cvt_pk_bf16_f32 v192, v160, v161\n" \
    "v_max_f32 v162, 0, v162\n" \
    "v_max_f32 v163, 0, v163\n" \
    "v_mul_f32 v162, v162, v162\n" \
    "v_mul_f32 v163, v163, v163\n" \
    "v_cvt_pk_bf16_f32 v193, v162, v163\n" \
    "v_max_f32 v164, 0, v164\n" \
    "v_max_f32 v165, 0, v165\n" \
    "v_mul_f32 v164, v164, v164\n" \
    "v_mul_f32 v165, v165, v165\n" \
    "v_cvt_pk_bf16_f32 v194, v164, v165\n" \
    "v_max_f32 v166, 0, v166\n" \
    "v_max_f32 v167, 0, v167\n" \
    "v_mul_f32 v166, v166, v166\n" \
    "v_mul_f32 v167, v167, v167\n" \
    "v_cvt_pk_bf16_f32 v195, v166, v167\n" \
    "v_max_f32 v168, 0, v168\n" \
    "v_max_f32 v169, 0, v169\n" \
    "v_mul_f32 v168, v168, v168\n" \
    "v_mul_f32 v169, v169, v169\n" \
    "v_cvt_pk_bf16_f32 v196, v168, v169\n" \
    "v_max_f32 v170, 0, v170\n" \
    "v_max_f32 v171, 0, v171\n" \
    "v_mul_f32 v170, v170, v170\n" \
    "v_mul_f32 v171, v171, v171\n" \
    "v_cvt_pk_bf16_f32 v197, v170, v171\n" \
    "v_max_f32 v172, 0, v172\n" \
    "v_max_f32 v173, 0, v173\n" \
    "v_mul_f32 v172, v172, v172\n" \
    "v_mul_f32 v173, v173, v173\n" \
    "v_cvt_pk_bf16_f32 v198, v172, v173\n" \
    "v_max_f32 v174, 0, v174\n" \
    "v_max_f32 v175, 0, v175\n" \
    "v_mul_f32 v174, v174, v174\n" \
    "v_mul_f32 v175, v175, v175\n" \
    "v_cvt_pk_bf16_f32 v199, v174, v175\n" \
    "ds_read_b64_tr_b16 v[188:189], v217 offset:3072\n" \
    "ds_read_b64_tr_b16 v[190:191], v217 offset:3584\n" \
    "s_waitcnt lgkmcnt(6)\n" \
    "v_mfma_f32_32x32x16_bf16 a[0:15], v[176:179], v[192:195], a[0:15]\n" \
    "ds_read_b64_tr_b16 v[176:177], v217 offset:4096\n" \
    "ds_read_b64_tr_b16 v[178:179], v217 offset:4608\n" \
    "s_waitcnt lgkmcnt(6)\n" \
    "v_mfma_f32_32x32x16_bf16 a[0:15], v[180:183], v[196:199], a[0:15]\n" \
    "ds_read_b64_tr_b16 v[180:181], v217 offset:5120\n" \
    "ds_read_b64_tr_b16 v[182:183], v217 offset:5632\n" \
    "s_waitcnt lgkmcnt(6)\n" \
    "v_mfma_f32_32x32x16_bf16 a[16:31], v[184:187], v[192:195], a[16:31]\n" \
    "ds_read_b64_tr_b16 v[184:185], v217 offset:6144\n" \
    "ds_read_b64_tr_b16 v[186:187], v217 offset:6656\n" \
    "s_waitcnt lgkmcnt(6)\n" \
    "v_mfma_f32_32x32x16_bf16 a[16:31], v[188:191], v[196:199], a[16:31]\n" \
    "ds_read_b64_tr_b16 v[188:189], v217 offset:7168\n" \
    "ds_read_b64_tr_b16 v[190:191], v217 offset:7680\n" \
    "s_waitcnt lgkmcnt(6)\n" \
    "v_mfma_f32_32x32x16_bf16 a[32:47], v[176:179], v[192:195], a[32:47]\n" \
    "ds_read_b64_tr_b16 v[176:177], v217 offset:8192\n" \
    "ds_read_b64_tr_b16 v[178:179], v217 offset:8704\n" \
    "s_waitcnt lgkmcnt(6)\n" \
    "v_mfma_f32_32x32x16_bf16 a[32:47], v[180:183], v[196:199], a[32:47]\n" \
    "ds_read_b64_tr_b16 v[180:181], v217 offset:9216\n" \
    "ds_read_b64_tr_b16 v[182:183], v217 offset:9728\n" \
    "s_waitcnt lgkmcnt(6)\n" \
    "v_mfma_f32_32x32x16_bf16 a[48:63], v[184:187], v[192:195], a[48:63]\n" \
    "ds_read_b64_tr_b16 v[184:185], v217 offset:10240\n" \
    "ds_read_b64_tr_b16 v[186:187], v217 offset:10752\n" \
    "s_waitcnt lgkmcnt(6)\n" \
    "v_mfma_f32_32x32x16_bf16 a[48:63], v[188:191], v[196:199], a[48:63]\n" \
    "ds_read_b64_tr_b16 v[188:189], v217 offset:11264\n" \
    "ds_read_b64_tr_b16 v[190:191], v217 offset:11776\n" \
    "s_waitcnt lgkmcnt(6)\n" \
    "v_mfma_f32_32x32x16_bf16 a[64:79], v[176:179], v[192:195], a[64:79]\n" \
    "ds_read_b64_tr_b16 v[176:177], v217 offset:12288\n" \
    "ds_read_b64_tr_b16 v[178:179], v217 offset:12800\n" \
    "s_waitcnt lgkmcnt(6)\n" \
    "v_mfma_f32_32x32x16_bf16 a[64:79], v[180:183], v[196:199], a[64:79]\n" \
    "ds_read_b64_tr_b16 v[180:181], v217 offset:13312\n" \
    "ds_read_b64_tr_b16 v[182:183], v217 offset:13824\n" \
    "s_waitcnt lgkmcnt(6)\n" \
    "v_mfma_f32_32x32x16_bf16 a[80:95], v[184:187], v[192:195], a[80:95]\n" \
    "ds_read_b64_tr_b16 v[184:185], v217 offset:14336\n" \
    "ds_read_b64_tr_b16 v[186:187], v217 offset:14848\n" \
    "s_waitcnt lgkmcnt(6)\n" \
    "v_mfma_f32_32x32x16_bf16 a[80:95], v[188:191], v[196:199], a[80:95]\n" \
    "ds_read_b64_tr_b16 v[188:189], v217 offset:15360\n" \
    "ds_read_b64_tr_b16 v[190:191], v217 offset:15872\n" \
    "s_waitcnt lgkmcnt(6)\n" \
    "v_mfma_f32_32x32x16_bf16 a[96:111], v[176:179], v[192:195], a[96:111]\n" \
    "ds_read_b64_tr_b16 v[176:177], v217 offset:16384\n" \
    "ds_read_b64_tr_b16 v[178:179], v217 offset:16896\n" \
    "s_waitcnt lgkmcnt(6)\n" \
    "v_mfma_f32_32x32x16_bf16 a[96:111], v[180:183], v[196:199], a[96:111]\n" \
    "ds_read_b64_tr_b16 v[180:181], v217 offset:17408\n" \
    "ds_read_b64_tr_b16 v[182:183], v217 offset:17920\n" \
    "s_waitcnt lgkmcnt(6)\n" \
    "v_mfma_f32_32x32x16_bf16 a[112:127], v[184:187], v[192:195], a[112:127]\n" \
    "ds_read_b64_tr_b16 v[184:185], v217 offset:18432\n" \
    "ds_read_b64_tr_b16 v[186:187], v217 offset:18944\n" \
    "s_waitcnt lgkmcnt(6)\n" \
    "v_mfma_f32_32x32x16_bf16 a[112:127], v[188:191], v[196:199], a[112:127]\n" \
    "ds_read_b64_tr_b16 v[188:189], v217 offset:19456\n" \
    "ds_read_b64_tr_b16 v[190:191], v217 offset:19968\n" \
    "s_waitcnt lgkmcnt(6)\n" \
    "v_mfma_f32_32x32x16_bf16 a[128:143], v[176:179], v[192:195], a[128:143]\n" \
    "ds_read_b64_tr_b16 v[176:177], v217 offset:20480\n" \
    "ds_read_b64_tr_b16 v[178:179], v217 offset:20992\n" \
    "s_waitcnt lgkmcnt(6)\n" \
    "v_mfma_f32_32x32x16_bf16 a[128:143], v[180:183], v[196:199], a[128:143]\n" \
    "ds_read_b64_tr_b16 v[180:181], v217 offset:21504\n" \
    "ds_read_b64_tr_b16 v[182:183], v217 offset:22016\n" \
    "s_waitcnt lgkmcnt(6)\n" \
    "v_mfma_f32_32x32x16_bf16 a[144:159], v[184:187], v[192:195], a[144:159]\n" \
    "ds_read_b64_tr_b16 v[184:185], v217 offset:22528\n" \
    "ds_read_b64_tr_b16 v[186:187], v217 offset:23040\n" \
    "s_waitcnt lgkmcnt(6)\n" \
    "v_mfma_f32_32x32x16_bf16 a[144:159], v[188:191], v[196:199], a[144:159]\n" \
    "ds_read_b64_tr_b16 v[188:189], v217 offset:23552\n" \
    "ds_read_b64_tr_b16 v[190:191], v217 offset:24064\n" \
    "s_waitcnt lgkmcnt(6)\n" \
    "v_mfma_f32_32x32x16_bf16 a[160:175], v[176:179], v[192:195], a[160:175]\n" \
    "ds_read_b64_tr_b16 v[176:177], v217 offset:24576\n" \
    "ds_read_b64_tr_b16 v[178:179], v217 offset:25088\n" \
    "s_waitcnt lgkmcnt(6)\n" \
    "v_mfma_f32_32x32x16_bf16 a[160:175], v[180:183], v[196:199], a[160:175]\n" \
    "ds_read_b64_tr_b16 v[180:181], v217 offset:25600\n" \
    "ds_read_b64_tr_b16 v[182:183], v217 offset:26112\n" \
    "s_waitcnt lgkmcnt(6)\n" \
    "v_mfma_f32_32x32x16_bf16 a[176:191], v[184:187], v[192:195], a[176:191]\n" \
    "ds_read_b64_tr_b16 v[184:185], v217 offset:26624\n" \
    "ds_read_b64_tr_b16 v[186:187], v217 offset:27136\n" \
    "s_waitcnt lgkmcnt(6)\n" \
    "v_mfma_f32_32x32x16_bf16 a[176:191], v[188:191], v[196:199], a[176:191]\n" \
    "ds_read_b64_tr_b16 v[188:189], v217 offset:27648\n" \
    "ds_read_b64_tr_b16 v[190:191], v217 offset:28160\n" \
    "s_waitcnt lgkmcnt(6)\n" \
    "v_mfma_f32_32x32x16_bf16 a[192:207], v[176:179], v[192:195], a[192:207]\n" \
    "s_waitcnt lgkmcnt(4)\n" \
    "v_mfma_f32_32x32x16_bf16 a[192:207], v[180:183], v[196:199], a[192:207]\n" \
    "s_waitcnt lgkmcnt(2)\n" \
    "v_mfma_f32_32x32x16_bf16 a[208:223], v[184:187], v[192:195], a[208:223]\n" \
    "s_waitcnt lgkmcnt(0)\n" \
    "v_mfma_f32_32x32x16_bf16 a[208:223], v[188:191], v[196:199], a[208:223]\n" \
    "s_nop 7\n" \
    "s_nop 7\n" \
    "s_add_u32 s44, %[fpo], 0\n" \
    "buffer_store_dwordx4 a[0:3], %[ov], %[rs], s44 offen offset:0 sc1\n" \
    "buffer_store_dwordx4 a[4:7], %[ov], %[rs], s44 offen offset:512 sc1\n" \
    "buffer_store_dwordx4 a[8:11], %[ov], %[rs], s44 offen offset:2048 sc1\n" \
    "buffer_store_dwordx4 a[12:15], %[ov], %[rs], s44 offen offset:2560 sc1\n" \
    "s_add_u32 s44, %[fpo], 4096\n" \
    "buffer_store_dwordx4 a[16:19], %[ov], %[rs], s44 offen offset:0 sc1\n" \
    "buffer_store_dwordx4 a[20:23], %[ov], %[rs], s44 offen offset:512 sc1\n" \
    "buffer_store_dwordx4 a[24:27], %[ov], %[rs], s44 offen offset:2048 sc1\n" \
    "buffer_store_dwordx4 a[28:31], %[ov], %[rs], s44 offen offset:2560 sc1\n" \
    "s_add_u32 s44, %[fpo], 16384\n" \
    "buffer_store_dwordx4 a[32:35], %[ov], %[rs], s44 offen offset:0 sc1\n" \
    "buffer_store_dwordx4 a[36:39], %[ov], %[rs], s44 offen offset:512 sc1\n" \
    "buffer_store_dwordx4 a[40:43], %[ov], %[rs], s44 offen offset:2048 sc1\n" \
    "buffer_store_dwordx4 a[44:47], %[ov], %[rs], s44 offen offset:2560 sc1\n" \
    "s_add_u32 s44, %[fpo], 20480\n" \
    "buffer_store_dwordx4 a[48:51], %[ov], %[rs], s44 offen offset:0 sc1\n" \
    "buffer_store_dwordx4 a[52:55], %[ov], %[rs], s44 offen offset:512 sc1\n" \
    "buffer_store_dwordx4 a[56:59], %[ov], %[rs], s44 offen offset:2048 sc1\n" \
    "buffer_store_dwordx4 a[60:63], %[ov], %[rs], s44 offen offset:2560 sc1\n" \
    "s_add_u32 s44, %[fpo], 32768\n" \
    "buffer_store_dwordx4 a[64:67], %[ov], %[rs], s44 offen offset:0 sc1\n" \
    "buffer_store_dwordx4 a[68:71], %[ov], %[rs], s44 offen offset:512 sc1\n" \
    "buffer_store_dwordx4 a[72:75], %[ov], %[rs], s44 offen offset:2048 sc1\n" \
    "buffer_store_dwordx4 a[76:79], %[ov], %[rs], s44 offen offset:2560 sc1\n" \
    "s_add_u32 s44, %[fpo], 36864\n" \
    "buffer_store_dwordx4 a[80:83], %[ov], %[rs], s44 offen offset:0 sc1\n" \
    "buffer_store_dwordx4 a[84:87], %[ov], %[rs], s44 offen offset:512 sc1\n" \
    "buffer_store_dwordx4 a[88:91], %[ov], %[rs], s44 offen offset:2048 sc1\n" \
    "buffer_store_dwordx4 a[92:95], %[ov], %[rs], s44 offen offset:2560 sc1\n" \
    "s_add_u32 s44, %[fpo], 49152\n" \
    "buffer_store_dwordx4 a[96:99], %[ov], %[rs], s44 offen offset:0 sc1\n" \
    "buffer_store_dwordx4 a[100:103], %[ov], %[rs], s44 offen offset:512 sc1\n" \
    "buffer_store_dwordx4 a[104:107], %[ov], %[rs], s44 offen offset:2048 sc1\n" \
    "buffer_store_dwordx4 a[108:111], %[ov], %[rs], s44 offen offset:2560 sc1\n" \
    "s_add_u32 s44, %[fpo], 53248\n" \
    "buffer_store_dwordx4 a[112:115], %[ov], %[rs], s44 offen offset:0 sc1\n" \
    "buffer_store_dwordx4 a[116:119], %[ov], %[rs], s44 offen offset:512 sc1\n" \
    "buffer_store_dwordx4 a[120:123], %[ov], %[rs], s44 offen offset:2048 sc1\n" \
    "buffer_store_dwordx4 a[124:127], %[ov], %[rs], s44 offen offset:2560 sc1\n" \
    "s_add_u32 s44, %[fpo], 65536\n" \
    "buffer_store_dwordx4 a[128:131], %[ov], %[rs], s44 offen offset:0 sc1\n" \
    "buffer_store_dwordx4 a[132:135], %[ov], %[rs], s44 offen offset:512 sc1\n" \
    "buffer_store_dwordx4 a[136:139], %[ov], %[rs], s44 offen offset:2048 sc1\n" \
    "buffer_store_dwordx4 a[140:143], %[ov], %[rs], s44 offen offset:2560 sc1\n" \
    "s_add_u32 s44, %[fpo], 69632\n" \
    "buffer_store_dwordx4 a[144:147], %[ov], %[rs], s44 offen offset:0 sc1\n" \
    "buffer_store_dwordx4 a[148:151], %[ov], %[rs], s44 offen offset:512 sc1\n" \
    "buffer_store_dwordx4 a[152:155], %[ov], %[rs], s44 offen offset:2048 sc1\n" \
    "buffer_store_dwordx4 a[156:159], %[ov], %[rs], s44 offen offset:2560 sc1\n" \
    "s_add_u32 s44, %[fpo], 81920\n" \
    "buffer_store_dwordx4 a[160:163], %[ov], %[rs], s44 offen offset:0 sc1\n" \
    "buffer_store_dwordx4 a[164:167], %[ov], %[rs], s44 offen offset:512 sc1\n" \
    "buffer_store_dwordx4 a[168:171], %[ov], %[rs], s44 offen offset:2048 sc1\n" \
    "buffer_store_dwordx4 a[172:175], %[ov], %[rs], s44 offen offset:2560 sc1\n" \
    "s_add_u32 s44, %[fpo], 86016\n" \
    "buffer_store_dwordx4 a[176:179], %[ov], %[rs], s44 offen offset:0 sc1\n" \
    "buffer_store_dwordx4 a[180:183], %[ov], %[rs], s44 offen offset:512 sc1\n" \
    "buffer_store_dwordx4 a[184:187], %[ov], %[rs], s44 offen offset:2048 sc1\n" \
    "buffer_store_dwordx4 a[188:191], %[ov], %[rs], s44 offen offset:2560 sc1\n" \
    "s_add_u32 s44, %[fpo], 98304\n" \
    "buffer_store_dwordx4 a[192:195], %[ov], %[rs], s44 offen offset:0 sc1\n" \
    "buffer_store_dwordx4 a[196:199], %[ov], %[rs], s44 offen offset:512 sc1\n" \
    "buffer_store_dwordx4 a[200:203], %[ov], %[rs], s44 offen offset:2048 sc1\n" \
    "buffer_store_dwordx4 a[204:207], %[ov], %[rs], s44 offen offset:2560 sc1\n" \
    "s_add_u32 s44, %[fpo], 102400\n" \
    "buffer_store_dwordx4 a[208:211], %[ov], %[rs], s44 offen offset:0 sc1\n" \
    "buffer_store_dwordx4 a[212:215], %[ov], %[rs], s44 offen offset:512 sc1\n" \
    "buffer_store_dwordx4 a[216:219], %[ov], %[rs], s44 offen offset:2048 sc1\n" \
    "buffer_store_dwordx4 a[220:223], %[ov], %[rs], s44 offen offset:2560 sc1\n" \
    "s_waitcnt vmcnt(0)\n"

#define CLOB_G \
    "v32", "v33", "v34", "v35", "v36", "v37", "v38", "v39", "v40", "v41", "v42", "v43", "v44", "v45", "v46", "v47", "v48", "v49", "v50", "v51", "v52", "v53", "v54", "v55", \
    "v56", "v57", "v58", "v59", "v60", "v61", "v62", "v63", "v64", "v65", "v66", "v67", "v68", "v69", "v70", "v71", "v72", "v73", "v74", "v75", "v76", "v77", "v78", "v79", \
    "v80", "v81", "v82", "v83", "v84", "v85", "v86", "v87", "v88", "v89", "v90", "v91", "v92", "v93", "v94", "v95", "v96", "v97", "v98", "v99", "v100", "v101", "v102", "v103", \
    "v104", "v105", "v106", "v107", "v108", "v109", "v110", "v111", "v112", "v113", "v114", "v115", "v116", "v117", "v118", "v119", "v120", "v121", "v122", "v123", "v124", "v125", "v126", "v127", \
    "a0", "a1", "a2", "a3", "a4", "a5", "a6", "a7", "a8", "a9", "a10", "a11", "a12", "a13", "a14", "a15", "a16", "a17", "a18", "a19", "a20", "a21", "a22", "a23", \
    "a24", "a25", "a26", "a27", "a28", "a29", "a30", "a31", "a32", "a33", "a34", "a35", "a36", "a37", "a38", "a39", "a40", "a41", "a42", "a43", "a44", "a45", "a46", "a47", \
    "a48", "a49", "a50", "a51", "a52", "a53", "a54", "a55", "a56", "a57", "a58", "a59", "a60", "a61", "a62", "a63", "a64", "a65", "a66", "a67", "a68", "a69", "a70", "a71", \
    "a72", "a73", "a74", "a75", "a76", "a77", "a78", "a79", "a80", "a81", "a82", "a83", "a84", "a85", "a86", "a87", "a88", "a89", "a90", "a91", "a92", "a93", "a94", "a95", \
    "a96", "a97", "a98", "a99", "a100", "a101", "a102", "a103", "a104", "a105", "a106", "a107", "a108", "a109", "a110", "a111", "a112", "a113", "a114", "a115", "a116", "a117", "a118", "a119", \
    "a120", "a121", "a122", "a123", "a124", "a125", "a126", "a127", "a128", "a129", "a130", "a131", "a132", "a133", "a134", "a135", "a136", "a137", "a138", "a139", "a140", "a141", "a142", "a143", \
    "a144", "a145", "a146", "a147", "a148", "a149", "a150", "a151", "a152", "a153", "a154", "a155", "a156", "a157", "a158", "a159", "a160", "a161", "a162", "a163", "a164", "a165", "a166", "a167", \
    "a168", "a169", "a170", "a171", "a172", "a173", "a174", "a175", "a176", "a177", "a178", "a179", "a180", "a181", "a182", "a183", "a184", "a185", "a186", "a187", "a188", "a189", "a190", "a191", \
    "a192", "a193", "a194", "a195", "a196", "a197", "a198", "a199", "a200", "a201", "a202", "a203", "a204", "a205", "a206", "a207", "a208", "a209", "a210", "a211", "a212", "a213", "a214", "a215", \
    "a216", "a217", "a218", "a219", "a220", "a221", "a222", "a223", "s40", "s41", "s42", "s43", "s44", "s45", "s46", "s47", "s48", "s49", "s50", "s51", "s52", "s53", "s54", "s55", \
    "vcc", "scc", "memory"

#define CLOB_F \
    "v32", "v33", "v34", "v35", "v36", "v37", "v38", "v39", "v40", "v41", "v42", "v43", "v44", "v45", "v46", "v47", "v48", "v49", "v50", "v51", "v52", "v53", "v54", "v55", \
    "v56", "v57", "v58", "v59", "v60", "v61", "v62", "v63", "v64", "v65", "v66", "v67", "v68", "v69", "v70", "v71", "v72", "v73", "v74", "v75", "v76", "v77", "v78", "v79", \
    "v80", "v81", "v82", "v83", "v84", "v85", "v86", "v87", "v88", "v89", "v90", "v91", "v92", "v93", "v94", "v95", "v96", "v97", "v98", "v99", "v100", "v101", "v102", "v103", \
    "v104", "v105", "v106", "v107", "v108", "v109", "v110", "v111", "v112", "v113", "v114", "v115", "v116", "v117", "v118", "v119", "v120", "v121", "v122", "v123", "v124", "v125", "v126", "v127", \
    "v128", "v129", "v130", "v131", "v132", "v133", "v134", "v135", "v136", "v137", "v138", "v139", "v140", "v141", "v142", "v143", "v144", "v145", "v146", "v147", "v148", "v149", "v150", "v151", \
    "v152", "v153", "v154", "v155", "v156", "v157", "v158", "v159", "v160", "v161", "v162", "v163", "v164", "v165", "v166", "v167", "v168", "v169", "v170", "v171", "v172", "v173", "v174", "v175", \
    "v176", "v177", "v178", "v179", "v180", "v181", "v182", "v183", "v184", "v185", "v186", "v187", "v188", "v189", "v190", "v191", "v192", "v193", "v194", "v195", "v196", "v197", "v198", "v199", \
    "v200", "v201", "v202", "v203", "v204", "v205", "v206", "v207", "v208", "v209", "v210", "v211", "v212", "v213", "v214", "v215", "v216", "v217", "v218", "v219", "a0", "a1", "a2", "a3", \
    "a4", "a5", "a6", "a7", "a8", "a9", "a10", "a11", "a12", "a13", "a14", "a15", "a16", "a17", "a18", "a19", "a20", "a21", "a22", "a23", "a24", "a25", "a26", "a27", \
    "a28", "a29", "a30", "a31", "a32", "a33", "a34", "a35", "a36", "a37", "a38", "a39", "a40", "a41", "a42", "a43", "a44", "a45", "a46", "a47", "a48", "a49", "a50", "a51", \
    "a52", "a53", "a54", "a55", "a56", "a57", "a58", "a59", "a60", "a61", "a62", "a63", "a64", "a65", "a66", "a67", "a68", "a69", "a70", "a71", "a72", "a73", "a74", "a75", \
    "a76", "a77", "a78", "a79", "a80", "a81", "a82", "a83", "a84", "a85", "a86", "a87", "a88", "a89", "a90", "a91", "a92", "a93", "a94", "a95", "a96", "a97", "a98", "a99", \
    "a100", "a101", "a102", "a103", "a104", "a105", "a106", "a107", "a108", "a109", "a110", "a111", "a112", "a113", "a114", "a115", "a116", "a117", "a118", "a119", "a120", "a121", "a122", "a123", \
    "a124", "a125", "a126", "a127", "a128", "a129", "a130", "a131", "a132", "a133", "a134", "a135", "a136", "a137", "a138", "a139", "a140", "a141", "a142", "a143", "a144", "a145", "a146", "a147", \
    "a148", "a149", "a150", "a151", "a152", "a153", "a154", "a155", "a156", "a157", "a158", "a159", "a160", "a161", "a162", "a163", "a164", "a165", "a166", "a167", "a168", "a169", "a170", "a171", \
    "a172", "a173", "a174", "a175", "a176", "a177", "a178", "a179", "a180", "a181", "a182", "a183", "a184", "a185", "a186", "a187", "a188", "a189", "a190", "a191", "a192", "a193", "a194", "a195", \
    "a196", "a197", "a198", "a199", "a200", "a201", "a202", "a203", "a204", "a205", "a206", "a207", "a208", "a209", "a210", "a211", "a212", "a213", "a214", "a215", "a216", "a217", "a218", "a219", \
    "a220", "a221", "a222", "a223", "s40", "s41", "s42", "s43", "s44", "s45", "s46", "s47", "s48", "s49", "s50", "s51", "s52", "s53", "s54", "s55", "vcc", "scc", "memory"

constexpr unsigned HOPSZ = 20u << 20;
constexpr unsigned SZ_W = 16u * 28 * 14 * 1024;
constexpr unsigned OFF_WA = HOPSZ, OFF_WB = OFF_WA + SZ_W;
constexpr unsigned OFF_XN = OFF_WB + SZ_W, SZ_X = 2048u * 896;
constexpr unsigned OFF_XQ = OFF_XN + SZ_X;
constexpr unsigned OFF_SXM = OFF_XQ + SZ_X;
constexpr unsigned OFF_EX = OFF_SXM + 64u * 32768;
constexpr unsigned OFF_ML = OFF_EX + 64u * 32768;
constexpr unsigned OFF_CTL = OFF_ML + 64u * 16 * 256;
constexpr unsigned OFF_KP = 44u << 20, OFF_UB = OFF_KP + (64u << 20), OFF_FP = OFF_UB + (64u << 20);
constexpr unsigned C_QUEUE = OFF_CTL + 2560, C_QFFN = OFF_CTL + 2816;
constexpr unsigned C_STAMP = OFF_CTL + 4096, C_XSF = OFF_CTL + 16384, C_FF = OFF_CTL + 20480;
constexpr unsigned C_FK = OFF_CTL + 24576, C_FU = C_FK + 128 * 128, C_PROG = C_FK + 256 * 128, CTLSZ = 24576 + 320 * 128;
constexpr unsigned C_SEL = OFF_CTL + CTLSZ, WS_END = OFF_FP + (32u << 20);
static_assert(C_SEL + 128 <= OFF_KP, "workspace map");
constexpr unsigned OOB = 0x7FFF0000u;
constexpr int SPIN = 1 << 16;
constexpr int LDS_PREP = 4 * (24576 + 14336), LDS_MAIN = 120 * 1024;

struct Params {
    const us *x, *mem0, *Wq, *Wk, *Wv, *Wo, *w1, *b1, *w2, *b2;
    us* out; char* ws;
    int cold, a0, b0, ga, gb, dV, dM, thr, nearffn, farffn, bulk, lead, dec, outl, bump, pad0;
};

DEVI float bf2f(unsigned h) { return __uint_as_float(h << 16); }
DEVI float rbf(float x) { return (float)(__bf16)x; }
DEVI unsigned cvtpk(float a, float b) { bf2 r = {(__bf16)a, (__bf16)b}; return __builtin_bit_cast(unsigned, r); }
DEVI u4 cvt8(f4 a, f4 b) { return u4{cvtpk(a[0], a[1]), cvtpk(a[2], a[3]), cvtpk(b[0], b[1]), cvtpk(b[2], b[3])}; }
DEVI f4 mfma(u4 a, u4 b, f4 c) { return __builtin_amdgcn_mfma_f32_16x16x32_bf16(__builtin_bit_cast(bf8, a), __builtin_bit_cast(bf8, b), c, 0, 0, 0); }
DEVI f16v mfma32(u4 a, u4 b, f16v c) { return __builtin_amdgcn_mfma_f32_32x32x16_bf16(__builtin_bit_cast(bf8, a), __builtin_bit_cast(bf8, b), c, 0, 0, 0); }
template <int A> DEVI f4 quad(f16v v) { return f4{v[4 * A], v[4 * A + 1], v[4 * A + 2], v[4 * A + 3]}; }
DEVI unsigned rfl(unsigned x) { return __builtin_amdgcn_readfirstlane(x); }
DEVI u4 mksrd(const void* p, unsigned bytes) { unsigned long long a = (unsigned long long)p; return u4{rfl((unsigned)a), rfl((unsigned)(a >> 32)) & 0xffffu, bytes, 0x00020000u}; }
DEVI __amdgpu_buffer_rsrc_t mkrs(const void* p, unsigned bytes) { return __builtin_amdgcn_make_buffer_rsrc((void*)p, 0, bytes, 0x00020000); }
DEVI u4 ld(__amdgpu_buffer_rsrc_t r, unsigned vo, unsigned so) { return __builtin_amdgcn_raw_buffer_load_b128(r, vo, so, 0); }
DEVI u4 ld1(__amdgpu_buffer_rsrc_t r, unsigned vo, unsigned so) { return __builtin_amdgcn_raw_buffer_load_b128(r, vo, so, 16); }
DEVI void st1(u4 v, __amdgpu_buffer_rsrc_t r, unsigned vo, unsigned so) {
    __builtin_amdgcn_raw_buffer_store_b128(v, r, vo, so, 16);
    asm volatile("s_nop 1" :: "v"(v) : "memory");
}
DEVI unsigned ldw1(__amdgpu_buffer_rsrc_t r, unsigned vo) { asm volatile("" ::: "memory"); return __builtin_amdgcn_raw_buffer_load_b32(r, vo, 0, 16); }
DEVI void stw1(unsigned v, __amdgpu_buffer_rsrc_t r, unsigned vo) { __builtin_amdgcn_raw_buffer_store_b32(v, r, vo, 0, 16); }
DEVI void dma(unsigned ldsdst, unsigned vo, u4 srd, unsigned so) {
    asm volatile("s_mov_b32 m0, %0\n\ts_nop 0\n\tbuffer_load_dwordx4 %1, %2, %3 offen lds" :: "s"(ldsdst), "v"(vo), "s"(srd), "s"(so) : "memory");
}
template <int OFF> DEVI void dma_at(unsigned vo, u4 srd, unsigned so) {
    asm volatile("buffer_load_dwordx4 %0, %1, %2 offen offset:%3 lds" :: "v"(vo), "s"(srd), "s"(so), "i"(OFF) : "memory");
}
DEVI void set_m0(unsigned x) { asm volatile("s_mov_b32 m0, %0" :: "s"(x) : "memory"); }
#define VMCNT(n) asm volatile("s_waitcnt vmcnt(" #n ")" ::: "memory")
DEVI void bar() { asm volatile("s_waitcnt lgkmcnt(0)\n\ts_barrier" ::: "memory"); }
DEVI u4 lds128(unsigned a) { return *(LAS u4*)(size_t)a; }
DEVI u2 ldstr(unsigned a) { return __builtin_bit_cast(u2, __builtin_amdgcn_ds_read_tr16_b64_v4i16((LAS s4*)(size_t)a)); }
DEVI float xrow_max(float x) {
    auto a = __builtin_amdgcn_permlane16_swap(__float_as_uint(x), __float_as_uint(x), false, false);
    x = fmaxf(__uint_as_float(a[0]), __uint_as_float(a[1]));
    auto b = __builtin_amdgcn_permlane32_swap(__float_as_uint(x), __float_as_uint(x), false, false);
    return fmaxf(__uint_as_float(b[0]), __uint_as_float(b[1]));
}
DEVI float xrow_sum(float x) {
    auto a = __builtin_amdgcn_permlane16_swap(__float_as_uint(x), __float_as_uint(x), false, false);
    x = __uint_as_float(a[0]) + __uint_as_float(a[1]);
    auto b = __builtin_amdgcn_permlane32_swap(__float_as_uint(x), __float_as_uint(x), false, false);
    return __uint_as_float(b[0]) + __uint_as_float(b[1]);
}
template <int C> DEVI float dppf(float x) { return __builtin_bit_cast(float, __builtin_amdgcn_update_dpp(0, __builtin_bit_cast(int, x), C, 0xf, 0xf, true)); }
template <int C> DEVI unsigned dppu(unsigned x) { return (unsigned)__builtin_amdgcn_update_dpp(0, (int)x, C, 0xf, 0xf, true); }
constexpr int XOR1 = 0xB1, XOR2 = 0x4E, HMIR = 0x141, ROR8 = 0x128;
DEVI float wave_sum(float x) { x += dppf<XOR1>(x); x += dppf<XOR2>(x); x += dppf<HMIR>(x); x += dppf<ROR8>(x); return xrow_sum(x); }
DEVI void wait_words(__amdgpu_buffer_rsrc_t r, unsigned vo, unsigned need) {
    for (int i = 0; i < SPIN; ++i) {
        if (__all(ldw1(r, vo) >= need)) break;
        __builtin_amdgcn_s_sleep(8);
    }
}
DEVI unsigned blk2(unsigned l) { return ((l >> 13) << 14) + (l & 8191u); }
DEVI unsigned seg_base(int s, unsigned pol) { return ((unsigned)s << 20) + ((((unsigned)s >> 1) ^ pol) & 1u) * 8192; }
DEVI unsigned fp_base(int i, unsigned pol) { return OFF_FP + ((unsigned)i << 17) + ((((unsigned)i >> 4) ^ pol) & 1u) * 8192; }
DEVI unsigned vm_base(int s, unsigned sel) { const unsigned r = 2 + s / 9; return (r << 21) + (s % 9) * 229376u + (((sel >> r) & 1u) << 13); }
DEVI unsigned get_sel(__amdgpu_buffer_rsrc_t r, int lane) {
    const unsigned w = __builtin_amdgcn_raw_buffer_load_b32(r, lane < 10 ? C_SEL + lane * 4 : OOB, 0, 0);
    const int l0 = w & 0xffffu, l1 = w >> 16, d = max(-300, min(300, l0 - l1));
    const float vote = wave_sum((float)((lane & 1) ? -d : d));
    const bool clean = min(l0, l1) < 520 && abs(d) > 130;
    return rfl((unsigned)__ballot(clean ? d > 0 : (vote > 0.0f) != (bool)(lane & 1)));
}

#define ISTAMP(item, k)
#define TSTAMP(k)
#define SB() __builtin_amdgcn_sched_barrier(0)
template <bool ISA>
DEVI void prep_body(const Params& p, unsigned lb, int b, int tid, int lane, int wave) {
    const int m = (b & 7) * 4 + (b >> 6), part = (b >> 3) & 7, rh = part >> 2, cq = part & 3, h = m & 15;
    const int n = lane & 15, g = lane >> 4;
    const u4 sA = mksrd(ISA ? p.Wk : p.Wv, 448u * 8192), sB = mksrd(ISA ? p.Wq : p.Wo, 448u * 8192), sX = mksrd(p.x, SZ_X);
    const __amdgpu_buffer_rsrc_t rW = mkrs(p.ws, WS_END);
    constexpr unsigned SL = 24576 + 14336;
    u4 xr[2];
    const int row0 = b * 8 + wave * 2;
    asm volatile("buffer_load_dwordx4 %0, %2, %3, 0 offen\n\tbuffer_load_dwordx4 %1, %2, %3, 0 offen offset:896" : "=&v"(xr[0]), "=&v"(xr[1]) : "v"(lane < 56 ? row0 * 896 + lane * 16 : OOB), "s"(sX) : "memory");
    const int lr = lane >> 3;
    unsigned vo[10];
#pragma unroll
    for (int i = 0; i < 6; ++i) {
        const int R = wave * 8 + lr, blk = i < 3 ? i : i + 1;
        const int dp = 224 * rh + 32 * blk + 8 * ((R & 15) >> 2) + 4 * (R >> 4) + (R & 3);
        vo[i] = (dp * 16 + h) * 512 + ((lane & 7) ^ lr) * 16;
    }
#pragma unroll
    for (int i = 0; i < 4; ++i) {
        const int pc = min(wave + 4 * i, 13);
        if (ISA) {
            const int C = pc * 8 + lr, t = 7 * cq + (C >> 4), ii = C & 15, jj = 4 * (t & 1) + (ii & 3);
            const int d = 64 * (t >> 2) + 16 * (ii >> 2) + 4 * (jj >> 1) + 2 * ((t >> 1) & 1) + (jj & 1);
            vo[6 + i] = (d * 16 + h) * 512 + ((lane & 7) ^ lr) * 16;
        } else {
            const int P = pc * 64 + lane, er = (P * 4682) >> 16;
            vo[6 + i] = (h * 256 + er) * 896 + cq * 224 + (P - er * 14) * 16;
        }
    }
#pragma unroll
    for (int kc = 0; kc < 4; ++kc) {
#pragma unroll
        for (int i = 0; i < 6; ++i) dma(lb + kc * SL + i * 4096 + wave * 1024, vo[i], sA, kc * 128);
#pragma unroll
        for (int i = 0; i < 4; ++i) dma(lb + kc * SL + 24576 + min(wave + 4 * i, 13) * 1024, vo[6 + i], sB, kc * (ISA ? 128u : 64u * 896));
    }
    u4 a3[16];
    if (wave == 3) {
        const unsigned v3 = ((224 * rh + 96 + 8 * (n >> 2) + (n & 3)) * 16 + h) * 512 + g * 16;
#pragma unroll
        for (int kc = 0; kc < 4; ++kc)
            asm volatile("buffer_load_dwordx4 %0, %4, %5, %6 offen\n\tbuffer_load_dwordx4 %1, %4, %5, %7 offen\n\tbuffer_load_dwordx4 %2, %4, %5, %6 offen offset:64\n\tbuffer_load_dwordx4 %3, %4, %5, %7 offen offset:64"
                         : "=&v"(a3[4 * kc]), "=&v"(a3[4 * kc + 1]), "=&v"(a3[4 * kc + 2]), "=&v"(a3[4 * kc + 3]) : "v"(v3), "s"(sA), "s"(kc * 128), "s"(kc * 128 + 4 * 8192) : "memory");
        asm volatile("s_waitcnt vmcnt(56)" ::: "memory");
    } else {
        asm volatile("s_waitcnt vmcnt(40)" ::: "memory");
    }
    asm volatile("" : "+v"(xr[0]), "+v"(xr[1]) :: "memory");
#pragma unroll
    for (int i = 0; i < 2; ++i) {
        const int row = row0 + i;
        float v[8];
#pragma unroll
        for (int j = 0; j < 4; ++j) { v[2 * j] = bf2f(xr[i][j] & 0xffffu); v[2 * j + 1] = __uint_as_float(xr[i][j] & 0xffff0000u); }
        const float mean = wave_sum(((v[0] + v[1]) + (v[2] + v[3])) + ((v[4] + v[5]) + (v[6] + v[7]))) * (1.0f / 448.0f);
        float q = 0;
#pragma unroll
        for (int j = 0; j < 8; ++j) q = __builtin_fmaf(v[j] - mean, v[j] - mean, q);
        const float var = wave_sum(lane < 56 ? q : 0.0f) * (1.0f / 448.0f);
        const float mb = rbf(mean), rd = 1.0f / rbf(__builtin_amdgcn_sqrtf(rbf(rbf(var) + 1e-5f)));
        u4 o;
#pragma unroll
        for (int j = 0; j < 4; ++j) {
            const unsigned dq = cvtpk(v[2 * j] - mb, v[2 * j + 1] - mb);
            o[j] = cvtpk(bf2f(dq & 0xffffu) * rd, __uint_as_float(dq & 0xffff0000u) * rd);
        }
        __builtin_amdgcn_raw_buffer_store_b128(o, rW, lane < 56 ? OFF_XN + (row >> 4) * 14336 + (lane >> 2) * 1024 + ((lane & 3) * 16 + (row & 15)) * 16 : OOB, 0, 0);
        const u4 nb = {dppu<XOR1>(o[0]), dppu<XOR1>(o[1]), dppu<XOR1>(o[2]), dppu<XOR1>(o[3])};
        const u4 oq = (lane & 1) ? u4{nb[1], nb[3], o[1], o[3]} : u4{o[0], o[2], nb[0], nb[2]};
        __builtin_amdgcn_raw_buffer_store_b128(oq, rW, lane < 56 ? OFF_XQ + (row >> 3) * 7168 + lane * 128 + (row & 7) * 16 : OOB, 0, 0);
    }
    st1(u4{0, 0, 0, 0}, rW, b < (int)(CTLSZ / 4096) ? OFF_CTL + b * 4096 + tid * 16 : OOB, 0);
    TSTAMP(2);
    const unsigned aA = lb + wave * 4096 + n * 128, aB = lb + 24576 + n * 128, aT = lb + 24576 + (8 * g + (n >> 2)) * 224 + (n & 3) * 8;
    auto colfrag = [&](int st, int t) {
        const unsigned so = (st >> 1) * SL;
        if (ISA) return lds128(aB + so + ((4 * (st & 1) + g) ^ (n & 7)) * 16 + t * 2048);
        const u2 lo = ldstr(aT + so + (st & 1) * 7168 + t * 32), hi = ldstr(aT + so + (st & 1) * 7168 + 896 + t * 32);
        return u4{lo[0], lo[1], hi[0], hi[1]};
    };
    const float sc = ISA ? 1.4426950408889634f * 0.0625f : 1.0f;
    const unsigned wo = (ISA ? OFF_WA : OFF_WB) + ((h * 28 + 7 * cq) * 14 + 7 * rh + wave) * 1024;
    if (wave < 3) {
        f4 acc[4][7];
#pragma unroll
        for (int i = 0; i < 28; ++i) acc[i / 7][i % 7] = f4{0, 0, 0, 0};
        u4 af[2][4], bf[2][7];
        auto rowfrag = [&](int st, int i) { return lds128(aA + (st >> 1) * SL + ((4 * (st & 1) + g) ^ (n & 7)) * 16 + (i >> 1) * 12288 + (i & 1) * 2048); };
        unsigned touch;
        asm volatile("buffer_load_dword %0, %1, %2, 0 offen\n\ts_waitcnt vmcnt(1)" : "=&v"(touch) : "v"(wave < 2 ? (unsigned)(b * 64 + lane) * 128 : OOB), "s"(mksrd(wave ? p.w2 : p.w1, 448u * 3584)) : "memory");
        bar();
        TSTAMP(3);
#pragma unroll
        for (int i = 0; i < 4; ++i) af[0][i] = rowfrag(0, i);
#pragma unroll
        for (int t = 0; t < 7; ++t) bf[0][t] = colfrag(0, t);
#pragma unroll
        for (int st = 0; st < 8; ++st)
#pragma unroll
            for (int t = 0; t < 7; ++t) {
                if (st < 7) { bf[(st + 1) & 1][t] = colfrag(st + 1, t); if (t < 4) af[(st + 1) & 1][t] = rowfrag(st + 1, t); }
                SB();
#pragma unroll
                for (int i = 0; i < 4; ++i) acc[i][t] = mfma(af[st & 1][i], bf[st & 1][t], acc[i][t]);
                if (st == 7) {
                    SB();
                    if (t > 0) { st1(cvt8(acc[0][t - 1] * sc, acc[1][t - 1] * sc), rW, lane * 16, wo + (t - 1) * 14336); st1(cvt8(acc[2][t - 1] * sc, acc[3][t - 1] * sc), rW, lane * 16, wo + (t - 1) * 14336 + 4096); }
                }
                SB();
            }
        st1(cvt8(acc[0][6] * sc, acc[1][6] * sc), rW, lane * 16, wo + 6 * 14336); st1(cvt8(acc[2][6] * sc, acc[3][6] * sc), rW, lane * 16, wo + 6 * 14336 + 4096);
        asm volatile("s_waitcnt vmcnt(14)" : "+v"(touch) :: "memory");
        TSTAMP(6);
        return;
    }
    const bool cal = ISA && (b & 7) == 0 && b < 80;
    const unsigned cb = (unsigned)(b >> 3) * (2u << 20) + 63 * 32768 + 7168;
    stw1(0, rW, cal && lane < 6 ? cb + (lane & 1) * 8192 + (lane >> 1) * 128 : OOB);
#pragma unroll
    for (int k = 0; k < 16; k += 8)
        asm volatile("s_waitcnt vmcnt(0)" : "+v"(a3[k]), "+v"(a3[k + 1]), "+v"(a3[k + 2]), "+v"(a3[k + 3]), "+v"(a3[k + 4]), "+v"(a3[k + 5]), "+v"(a3[k + 6]), "+v"(a3[k + 7]) :: "memory");
    bar();
    f4 acc[2][7];
#pragma unroll
    for (int i = 0; i < 14; ++i) acc[i / 7][i % 7] = f4{0, 0, 0, 0};
    u4 bf[2][7];
#pragma unroll
    for (int t = 0; t < 7; ++t) bf[0][t] = colfrag(0, t);
#pragma unroll
    for (int st = 0; st < 8; ++st)
#pragma unroll
        for (int t = 0; t < 7; ++t) {
            if (st < 7) bf[(st + 1) & 1][t] = colfrag(st + 1, t);
            SB();
            acc[0][t] = mfma(a3[2 * st], bf[st & 1][t], acc[0][t]);
            acc[1][t] = mfma(a3[2 * st + 1], bf[st & 1][t], acc[1][t]);
            SB();
        }
    if (cal) {
        unsigned best[2] = {~0u, ~0u};
        const u4 sW = mksrd(p.ws, WS_END);
#pragma unroll
        for (int i = 0; i < 6; ++i) {
            unsigned long long t0, t1; unsigned x;
            asm volatile("s_memtime %0\n\ts_waitcnt lgkmcnt(0)\n\tbuffer_load_dword %2, %3, %4, 0 offen sc1\n\ts_waitcnt vmcnt(0)\n\ts_memtime %1\n\ts_waitcnt lgkmcnt(0)"
                         : "=&s"(t0), "=&s"(t1), "=&v"(x) : "v"(cb + (i & 1) * 8192 + (i >> 1) * 128), "s"(sW) : "memory");
            best[i & 1] = min(best[i & 1], (unsigned)(t1 - t0));
        }
        stw1(min(best[0], 0xffffu) | (min(best[1], 0xffffu) << 16), rW, lane == 0 ? C_SEL + (b >> 3) * 4 : OOB);
    }
#pragma unroll
    for (int t = 0; t < 7; ++t) st1(cvt8(acc[0][t] * sc, acc[1][t] * sc), rW, lane * 16, wo + t * 14336);
}

__global__ void __launch_bounds__(256, 1) k_prep(Params p) {
    extern __shared__ __attribute__((aligned(16))) char lds[];
    const int b = blockIdx.x, tid = threadIdx.x, lane = tid & 63, wave = rfl(tid >> 6);
    const unsigned lb = (unsigned)(size_t)(LAS char*)lds;
    TSTAMP(0);
    if ((b & 7) < 4) prep_body<true>(p, lb, b, tid, lane, wave);
    else prep_body<false>(p, lb, b, tid, lane, wave);
    TSTAMP(1);
}

template <bool ISK>
DEVI void gemm_role(const Params& p, int u, unsigned pol, unsigned lb, int lane, int wave) {
    const u4 rs = mksrd(p.ws, WS_END);
    const int h = u / 7, j = u % 7, r = lane & 31, hh = lane >> 5;
    const unsigned l16 = lane * 16;
    const unsigned nat = (r >> 4) * 14336 + (hh * 16 + (r & 15)) * 16, prm = ((r >> 3) & 1) * 14336 + (hh * 16 + 8 * (r >> 4) + (r & 7)) * 16;
    const unsigned fo = lane == 0 ? (ISK ? C_FK + (h * 8 + j) * 128 : C_FU + (j * 16 + h) * 128) : OOB;
    const unsigned ov = (r >> 4) * (ISK ? 1024 : 32768) + (hh * 16 + (r & 15)) * 16;
    const unsigned wv = ISK ? prm : nat, xb = lb + (ISK ? nat : prm);
    const unsigned woff = (ISK ? OFF_WA : OFF_WB) + (h * 28 + 4 * j) * 14336, out0 = ISK ? OFF_KP + blk2((h * 28 + 4 * j) * 1024) : OFF_UB + blk2((4 * j * 16 + h) * 1024);
#define TSOP
#define GOPS :: [l16] "v"(l16), [wv] "v"(wv), [xb] "v"(xb), [fo] "v"(fo), [ov] "v"(ov), TSOP [rs] "s"(rs), [ld0] "s"(lb + wave * 7168), [so0] "s"(OFF_XN + wave * 7168), \
        [woff] "s"(woff), [out0] "s"(out0), [pol] "s"(pol) : CLOB_G
    if (ISK) asm volatile(ASM_GK GOPS); else asm volatile(ASM_GU GOPS);
}

DEVI void ffn_item(const Params& p, int item, unsigned pol, unsigned lb, int lane, int wave) {
    const __amdgpu_buffer_rsrc_t rW = mkrs(p.ws, WS_END);
    const u4 rs = mksrd(p.ws, WS_END), rs1 = mksrd(p.w1, 448u * 3584), rs2 = mksrd(p.w2, 1792u * 896);
    ISTAMP(item, 0);
    const int fq = item & 3, s = (item >> 2) * 4 + wave, r = lane & 31, hh = lane >> 5, blk = (lane >> 4) & 1, q = (lane & 15) >> 2;
    if (lane < 56) {
        const unsigned bw = *(const unsigned*)(p.b1 + fq * 448 + (wave * 56 + lane) * 2);
        const int f = (wave * 56 + lane) * 2, e = f & 31;
        *(LAS u2*)(size_t)(lb + 114688 + (f >> 5) * 128 + ((e >> 2) & 1) * 64 + ((e >> 3) * 4 + (e & 3)) * 4) = u2{bw << 16, bw & 0xffff0000u};
    }
    const unsigned v1 = (lane >> 2) * 3584 + (lane & 3) * 16, v2 = (lane >> 2) * 896 + (lane & 3) * 16;
    const unsigned xv = (r >> 4) * 14336 + (hh * 16 + (r & 15)) * 16, ov = (r >> 4) * 1024 + (hh * 16 + (r & 15)) * 16;
    const unsigned t1 = lb + (8 * hh + q) * 64 + blk * 32 + (lane & 3) * 8, t2 = lb + 28672 + (4 * hh + q) * 64 + blk * 32 + (lane & 3) * 8, ba = lb + 114688 + hh * 64;
    asm volatile(ASM_FFN :: [v1] "v"(v1), [v2] "v"(v2), [xv] "v"(xv), [ov] "v"(ov), [t1] "v"(t1), [t2] "v"(t2), [ba] "v"(ba), [rs] "s"(rs), [rs1] "s"(rs1), [rs2] "s"(rs2),
                 [ldw] "s"(lb + wave * 7168), [w1o] "s"(wave * 7 * 57344 + fq * 896), [w2o] "s"(fq * 448 * 896), [w7] "s"(wave * 7), [xo] "s"(OFF_XN + s * 28672),
                 [fpo] "s"(fp_base(fq * 64 + s, pol)) : CLOB_F);
    stw1(1, rW, lane == 0 ? C_FF + (s * 4 + fq) * 4 : OOB);
    ISTAMP(item, 1);
    ISTAMP(item, 2);
}

DEVI void xscore_item(const Params& p, int item, unsigned pol, int lane, int wave) {
    const __amdgpu_buffer_rsrc_t rW = mkrs(p.ws, WS_END);
    const int s = item >> 2, h = (item & 3) * 4 + wave, n = lane & 15, g = lane >> 4;
    ISTAMP(64 + item, 0);
    u4 xq[2][14], kp[28];
    const unsigned xo = (n >> 3) * 7168 + g * 256 + (n & 7) * 16;
#pragma unroll
    for (int t = 0; t < 2; ++t)
#pragma unroll
        for (int k = 0; k < 14; ++k) xq[t][k] = ld(rW, xo, OFF_XQ + (s * 4 + t * 2) * 7168 + (k >> 1) * 1024 + (k & 1) * 128);
    wait_words(rW, C_FK + (h * 8 + ((n & 7) < 7 ? n & 7 : 0)) * 128, s + 1);
    ISTAMP(64 + item, 1);
#pragma unroll
    for (int i = 0; i < 28; ++i) kp[i] = ld1(rW, lane * 16 + (i & 3) * 1024, OFF_KP + seg_base(s, pol) + blk2((h * 7 + (i >> 2)) * 4096));
    f4 a00 = {0, 0, 0, 0}, a01 = a00, a11 = a00;
#pragma unroll
    for (int k = 0; k < 14; ++k) {
        a00 = mfma(kp[2 * k], xq[0][k], a00);
        a01 = mfma(kp[2 * k], xq[1][k], a01);
        a11 = mfma(kp[2 * k + 1], xq[1][k], a11);
    }
    const float NEG = -__builtin_inff();
#pragma unroll
    for (int r = 0; r < 4; ++r) if (4 * g + r > n) { a00[r] = NEG; a11[r] = NEG; }
    const float m0 = xrow_max(fmaxf(fmaxf(a00[0], a00[1]), fmaxf(a00[2], a00[3])));
    const float m1 = xrow_max(fmaxf(fmaxf(fmaxf(a01[0], a01[1]), fmaxf(a01[2], a01[3])), fmaxf(fmaxf(a11[0], a11[1]), fmaxf(a11[2], a11[3]))));
#pragma unroll
    for (int r = 0; r < 4; ++r) { a00[r] = __builtin_amdgcn_exp2f(a00[r] - m0); a01[r] = __builtin_amdgcn_exp2f(a01[r] - m1); a11[r] = __builtin_amdgcn_exp2f(a11[r] - m1); }
    const float l0 = xrow_sum((a00[0] + a00[1]) + (a00[2] + a00[3]));
    const float l1 = xrow_sum(((a01[0] + a01[1]) + (a01[2] + a01[3])) + ((a11[0] + a11[1]) + (a11[2] + a11[3])));
    st1(cvt8(a00, f4{0, 0, 0, 0}), rW, lane * 16, OFF_EX + (s * 16 + h) * 2048);
    st1(cvt8(a01, a11), rW, lane * 16, OFF_EX + (s * 16 + h) * 2048 + 1024);
    const float mv = g == 0 ? m0 : g == 1 ? m1 : g == 2 ? l0 : l1;
    stw1(__float_as_uint(mv), rW, OFF_ML + (s * 16 + h) * 256 + (g >> 1) * 128 + (n & 7) * 16 + ((g & 1) * 2 + (n >> 3)) * 4);
    VMCNT(0);
    stw1(1, rW, lane == 0 ? C_XSF + (s * 16 + h) * 4 : OOB);
    ISTAMP(64 + item, 2);
}

DEVI void xout_item(const Params& p, int item, unsigned sel, unsigned pol, unsigned lb, int lane, int wave) {
    const __amdgpu_buffer_rsrc_t rW = mkrs(p.ws, WS_END);
    const int s = item / 7, cg = item % 7, ct = cg * 4 + wave, n = lane & 15, g = lane >> 4;
    const unsigned l16 = lane * 16;
    ISTAMP(320 + item, 0);
    wait_words(rW, lane < 32 ? C_FU + (cg * 16 + n) * 128 : C_XSF + (s * 16 + n) * 4, lane < 32 ? s + 1 : 1);
    wait_words(rW, C_FF + (s * 4 + (lane & 3)) * 4, 1);
    ISTAMP(320 + item, 1);
    u4 ub[16], ex[8];
#pragma unroll
    for (int i = 0; i < 8; ++i) ex[i] = ld1(rW, l16, OFF_EX + (s * 32 + wave * 8 + i) * 1024);
#pragma unroll
    for (int i = 0; i < 16; ++i) ub[i] = ld1(rW, l16, OFF_UB + seg_base(s, pol) + ct * 32768 + blk2(i * 1024));
    f4 res[2], mx[4], lx[4];
    const int qo = 4 * (g & 1) + 2 * (g >> 1) + (n >> 3);
#pragma unroll
    for (int i = 0; i < 4; ++i) {
        mx[i] = __builtin_bit_cast(f4, ld1(rW, qo * 16, OFF_ML + (s * 16 + wave * 4 + i) * 256));
        lx[i] = __builtin_bit_cast(f4, ld1(rW, qo * 16, OFF_ML + (s * 16 + wave * 4 + i) * 256 + 128));
    }
    const u2 bb = *(const u2*)(p.b2 + ct * 16 + 4 * g);
#pragma unroll
    for (int t = 0; t < 2; ++t) {
        const u2 xx = *(const u2*)(p.x + (s * 32 + t * 16 + n) * 448 + ct * 16 + 4 * g);
        res[t] = f4{bf2f(xx[0] & 0xffffu) + bf2f(bb[0] & 0xffffu), __uint_as_float(xx[0] & 0xffff0000u) + __uint_as_float(bb[0] & 0xffff0000u),
                    bf2f(xx[1] & 0xffffu) + bf2f(bb[1] & 0xffffu), __uint_as_float(xx[1] & 0xffff0000u) + __uint_as_float(bb[1] & 0xffff0000u)};
#pragma unroll
        for (int q = 0; q < 4; ++q) res[t] += __builtin_bit_cast(f4, ld1(rW, l16, fp_base(q * 64 + s, pol) + blk2((ct * 2 + t) * 1024)));
    }
#pragma unroll
    for (int i = 0; i < 8; ++i) *(LAS u4*)(size_t)(lb + (wave * 8 + i) * 1024 + l16) = ex[i];
    bar();
    f4 o[16][2];
#pragma unroll
    for (int i = 0; i < 16; ++i)
#pragma unroll
        for (int t = 0; t < 2; ++t) o[i][t] = mfma(ub[i], lds128(lb + (i * 2 + t) * 1024 + l16), f4{0, 0, 0, 0});
    asm volatile("" :: "v"(o[15][1]), "v"(res[0]), "v"(res[1]));
    ISTAMP(320 + item, 2);
    if (wave == 0) wait_words(rW, C_PROG + min(s + 1, 62) * 128, 1);
    bar();
    if (s == 63) __builtin_amdgcn_s_sleep(12);
    ISTAMP(320 + item, 3);
    u4 sx[8], av[4];
    const unsigned vb = vm_base(s, sel) + (ct >> 1) * 16384 + (ct & 1) * 4096;
    for (int it = 0; it < SPIN; ++it) {
        asm volatile("" ::: "memory");
        unsigned mm = 0;
#pragma unroll
        for (int i = 0; i < 8; ++i) sx[i] = ld1(rW, l16, OFF_SXM + (s * 32 + wave * 8 + i) * 1024);
#pragma unroll
        for (int i = 0; i < 4; ++i) av[i] = ld1(rW, l16, vb + i * 1024);
#pragma unroll
        for (int i = 0; i < 8; ++i) mm = max(max(mm, max(sx[i][0], sx[i][1])), max(sx[i][2], sx[i][3]));
#pragma unroll
        for (int i = 0; i < 4; ++i) mm = max(max(mm, max(av[i][0], av[i][1])), max(av[i][2], av[i][3]));
        if (!__any(mm == ~0u)) break;
        __builtin_amdgcn_s_sleep(4);
    }
    ISTAMP(320 + item, 4);
    const unsigned pw = lb + 32768 + wave * 2048 + (((n >> 2) & 1) * 16 + qo) * 16 + (n & 3) * 2;
    const unsigned aw = lb + 40960 + wave * 512 + qo * 16;
#pragma unroll
    for (int i = 0; i < 4; ++i) {
        f4 al;
#pragma unroll
        for (int t = 0; t < 4; ++t) {
            const float v = __uint_as_float(sx[2 * i][t]) + __uint_as_float(sx[2 * i + 1][t]);
            float m = fmaxf(v, dppf<XOR1>(v)); m = fmaxf(m, dppf<XOR2>(m)); m = fmaxf(m, dppf<HMIR>(m));
            m = fmaxf(m, mx[i][t]);
            const float e = __builtin_amdgcn_exp2f(v - m), a = __builtin_amdgcn_exp2f(mx[i][t] - m);
            float l = e + dppf<XOR1>(e); l += dppf<XOR2>(l); l += dppf<HMIR>(l);
            const float rl = __builtin_amdgcn_rcpf(__builtin_fmaf(a, lx[i][t], l));
            *(LAS us*)(size_t)(pw + (t >> 1) * 1024 + ((i & 1) * 32 + (t & 1) * 8) * 16 + (i >> 1) * 8) = (us)(cvtpk(e * rl, 0.0f) & 0xffffu);
            al[(t & 1) * 2 + (t >> 1)] = a * rl;
        }
        if ((n & 7) == 0) *(LAS f4*)(size_t)(aw + i * 128) = al;
    }
    bar();
#pragma unroll
    for (int i = 0; i < 16; ++i) {
        const u2 a = *(LAS u2*)(size_t)(lb + 40960 + i * 128 + (n & 7) * 16 + (n >> 3) * 8);
        res[0] += __uint_as_float(a[0]) * o[i][0];
        res[1] += __uint_as_float(a[1]) * o[i][1];
    }
#pragma unroll
    for (int i = 0; i < 4; ++i)
#pragma unroll
        for (int t = 0; t < 2; ++t) res[t] = mfma(av[i], lds128(lb + 32768 + i * 2048 + t * 1024 + l16), res[t]);
#pragma unroll
    for (int t = 0; t < 2; ++t) *(u2*)(p.out + (s * 32 + t * 16 + n) * 448 + ct * 16 + 4 * g) = u2{cvtpk(res[t][0], res[t][1]), cvtpk(res[t][2], res[t][3])};
    ISTAMP(320 + item, 5);
}

DEVI void prefetch_role(const Params& p, int xc, int wv, unsigned pol, int lane) {
    const __amdgpu_buffer_rsrc_t rW = mkrs(p.ws, WS_END);
    const bool isK = !(xc & 1);
    const unsigned fw = isK ? C_FK + ((xc * 4 + (lane >> 3)) * 8 + min(lane & 7, 6)) * 128 : C_FU + (((7 * (xc - 1)) >> 2) * 16 + lane) * 128;
    unsigned vo[4];
#pragma unroll
    for (int i = 0; i < 4; ++i) {
        const int k = wv * 4 + i;
        vo[i] = k < 56 ? blk2(((isK ? xc * 4 * 7 : 7 * (xc - 1) * 4) + k) * 4096) + lane * 64 : OOB;
    }
    unsigned ready = 0;
    for (int s = 0; s < 63; ++s) {
        if (s >= p.lead && (wv & 3) == 0)
            for (int i = 0; i < SPIN; ++i) {
                if (ldw1(rW, C_PROG + (s - p.lead) * 128) != 0) break;
                __builtin_amdgcn_s_sleep(4);
            }
        for (int i = 0; i < SPIN && (wv & 3) == 0 && ready <= (unsigned)s; ++i) {
            unsigned w = ldw1(rW, fw);
            w = min(w, dppu<XOR1>(w)); w = min(w, dppu<XOR2>(w)); w = min(w, dppu<HMIR>(w)); w = min(w, dppu<ROR8>(w));
            ready = min(min(__builtin_amdgcn_readlane(w, 0), __builtin_amdgcn_readlane(w, 16)), min(__builtin_amdgcn_readlane(w, 32), __builtin_amdgcn_readlane(w, 48)));
            if (ready <= (unsigned)s) __builtin_amdgcn_s_sleep(12);
        }
        bar();
        const bool paced = s >= p.lead;
        if (paced) for (int i = 0; i < wv; ++i) asm volatile("s_nop 7" ::: "memory");
        unsigned x[4];
#pragma unroll
        for (int i = 0; i < 4; ++i) {
            x[i] = __builtin_amdgcn_raw_buffer_load_b32(rW, vo[i], (isK ? OFF_KP : OFF_UB) + seg_base(s, pol), 16);
            if (paced && i < 3) { asm volatile("" ::: "memory"); __builtin_amdgcn_s_sleep(6); asm volatile("" ::: "memory"); }
        }
        asm volatile("" :: "v"(x[0]), "v"(x[1]), "v"(x[2]), "v"(x[3]));
    }
}

DEVI int wave_maxi(int x) {
    x = max(x, (int)dppu<XOR1>(x)); x = max(x, (int)dppu<XOR2>(x)); x = max(x, (int)dppu<HMIR>(x)); x = max(x, (int)dppu<ROR8>(x));
    return x;
}
DEVI void pace_role(const Params& p, unsigned sel, int lane) {
    const __amdgpu_buffer_rsrc_t rW = mkrs(p.ws, WS_END);
    unsigned r0 = 0, r1 = 0;
    for (int i = 0; i < SPIN; ++i) {
        r0 = ldw1(rW, C_STAMP + lane * 128); r1 = ldw1(rW, C_STAMP + (lane < 24 ? 64 + lane : 0) * 128);
        if (__all(r0 != 0 && r1 != 0)) break;
        __builtin_amdgcn_s_sleep(6);
    }
    const unsigned ref = rfl(r0);
    int m = wave_maxi(max((int)(r0 - ref), (int)(r1 - ref)));
    unsigned T = ref + max(max(__builtin_amdgcn_readlane(m, 0), __builtin_amdgcn_readlane(m, 16)), max(__builtin_amdgcn_readlane(m, 32), __builtin_amdgcn_readlane(m, 48))) + K_START;
    unsigned Tp = T, so = (3u << 20) + (sel & 2u) * 4096;
    int a = p.a0, b = p.b0, dE = a + p.ga + p.cold, per = dE + b + p.gb + p.cold, dEp = dE, rec = K_REC, moved = 1;
    for (int s = 0; s < 63; ++s, so += 16384) {
        for (int i = 0; i < SPIN && (int)(((unsigned)__builtin_amdgcn_s_memrealtime() << 3) - (T + 200)) < 0; ++i) __builtin_amdgcn_s_sleep(1);
        unsigned w = 0;
        for (int i = 0; i < SPIN; ++i) {
            asm volatile("" ::: "memory");
            w = __builtin_amdgcn_raw_buffer_load_b32(rW, (lane >= 28 && lane < 32 ? 0 : lane) * 128, so, 16);
            if (!__any(w == ~0u)) break;
            __builtin_amdgcn_s_sleep(4);
        }
        m = wave_maxi((int)(w - Tp));
        int ob = max(__builtin_amdgcn_readlane(m, 0), __builtin_amdgcn_readlane(m, 16)), oa = max(__builtin_amdgcn_readlane(m, 32), __builtin_amdgcn_readlane(m, 48));
        const int late = (int)(Tp + ob - T);
        int pert = 0;
        if (late + p.thr > 0) { T += late <= K_PASS - K_VIS ? K_PASS + 20 : late <= 2 * K_PASS - K_VIS ? 2 * K_PASS + 25 : late + rec; pert = K_PERT; }
        ob -= dEp;
        if (s > 1 && !moved) {
            a -= p.dec; b -= p.dec;
            const bool fa = oa > a + p.outl, fb = ob > b + p.outl;
            a = fa ? a : max(a, oa); b = fb ? b : max(b, ob);
            if (fa) b += p.bump; else if (fb) a += p.bump;
        }
        const int dEn = a + p.ga, pern = dEn + b + p.gb;
        __builtin_amdgcn_raw_buffer_store_b64(u2{T & ~1u, (unsigned)pern | ((unsigned)dEn << 16)}, rW, lane == 0 ? 28 * 128 : OOB, so + 16384, 16);
        rec = pert ? min(rec + K_RINC, K_REC + 4 * K_RINC) : K_REC; moved = pert;
        Tp = T; dEp = dE + pert; T += per + pert; dE = dEn; per = pern;
    }
}

__global__ void __launch_bounds__(256, 1) k_main(Params p) {
    extern __shared__ __attribute__((aligned(16))) char lds[];
    const int b = blockIdx.x, tid = threadIdx.x, lane = tid & 63, wave = rfl(tid >> 6);
    const int xc = b & 7, ix = b >> 3;
    const unsigned lb = (unsigned)(size_t)(LAS char*)lds;
    const __amdgpu_buffer_rsrc_t rW = mkrs(p.ws, WS_END);
    TSTAMP(2);
    const bool isS = xc < 4 && !(xc & 1) && ix < 16, isV = xc < 4 && (xc & 1) && ix < 14, isM = xc < 4 && (xc & 1) && ix >= 14 && ix < 28;
    const unsigned sel = get_sel(rW, lane), pol = (sel ^ p.bulk) & 1u;
    if (!isS && !isV && !isM) {
        const int wi = ix * 4 + (xc - 4);
        if (ix == 31 && wave == 0) {
            const unsigned a = ldw1(rW, (unsigned)lane * (2u << 20) + 63 * 32768 + 7168), c = ldw1(rW, (unsigned)(lane + 64) * (2u << 20) + 63 * 32768 + 7168);
            asm volatile("" :: "v"(a), "v"(c));
        }
        if (b == 27 * 8) { if (wave == 0) pace_role(p, sel, lane); return; }
        if (xc < 4 && ix >= 28 && p.lead) { prefetch_role(p, xc, (ix - 28) * 4 + wave, pol, lane); return; }
        if (xc >= 4 && wi < 28) gemm_role<true>(p, wi * 4 + wave, pol, lb, lane, wave);
        else if (xc >= 4 && wi < 56) gemm_role<false>(p, (wi - 28) * 4 + wave, pol, lb, lane, wave);
        if (xc >= 4 && wi < 56) TSTAMP(3);
        bool ffn = xc < 4 ? p.nearffn != 0 : wi < p.farffn;
        if (xc < 4 && !ffn) return;
        if (ffn) wait_words(rW, C_FK, 1);
        for (;;) {
            bar();
            if (tid == 0) *(LAS unsigned*)(size_t)(lb + LDS_MAIN - 16) = __hip_atomic_fetch_add((unsigned*)(p.ws + (ffn ? C_QFFN : C_QUEUE)), 1u, __ATOMIC_RELAXED, __HIP_MEMORY_SCOPE_AGENT);
            bar();
            const int item = rfl(*(LAS unsigned*)(size_t)(lb + LDS_MAIN - 16));
            int ln = lane;
            asm volatile("" : "+v"(ln));
            if (ffn) {
                if (item < 64) { ffn_item(p, item, pol, lb, ln, wave); TSTAMP(4); }
                else if (xc < 4) break;
                else ffn = false;
            } else {
                constexpr int LEAD = 6;
                if (item >= 704) break;
                const int it = item - 4 * LEAD, blk = it / 11, r = it % 11;
                if (item < 4 * LEAD) { xscore_item(p, item, pol, ln, wave); TSTAMP(5); }
                else if (blk < 64 - LEAD && r < 4) { xscore_item(p, (blk + LEAD) * 4 + r, pol, ln, wave); TSTAMP(5); }
                else xout_item(p, blk < 64 - LEAD ? blk * 7 + r - 4 : it - (64 - LEAD) * 4, sel, pol, lb, ln, wave);
            }
        }
        TSTAMP(6);
        return;
    }
    const int rk = isS ? ix + 8 * xc : isV ? ix + 7 * (xc - 1) : ix - 14 + 7 * (3 - xc);
    for (int i = tid; i < 2048; i += 256) ((unsigned*)lds)[i] = 0;
    __syncthreads();
    const u4 rsH = mksrd(p.ws, HOPSZ), rsW = mksrd(p.ws, WS_END);
    const int n = lane & 15, g = lane >> 4, hf = n >> 3, q = n & 7;
    const unsigned idx = (lane & 8) ? 0xEEEEu : 0x4444u, l16 = lane * 16, one16 = lane < 16 ? 1u : 0u;
    const unsigned myslot = C_STAMP + (isS ? 28 + rk : isM ? 60 + rk : rk) * 128;
    const unsigned rdyw = tid == 0 ? myslot : OOB;
    const unsigned rdy0 = C_STAMP + lane * 128, rdy0b = C_STAMP + (lane < 24 ? 64 + lane : 0) * 128, stpo = (lane < 29 ? lane : 0) * 128;
    if (isS) {
        const int h = rk >> 1, j = rk & 1;
        const unsigned cnt = lb + 1024 + lane * 4, rdS = lb + ((n >> 1) * 8 + (n & 1) * 4 + g) * 4;
        const unsigned pubS = lane < 16 ? (unsigned)((h * 2 + j) * 256 + lane * 16) : OOB;
        const unsigned flg = C_FK + (h * 8 + (q < 7 ? q : 0)) * 128, rdys = lane == 0 ? (unsigned)((32 + rk) * 128) : OOB;
        if (j == 0 || wave < 3) {
            const int f = j * 4 + wave;
            const unsigned aS = lb + wave * 256 + ((4 * (g & 1) + 2 * (g >> 1) + hf) * 8 + q) * 4, aX = lb + 2048 + wave * 1024 + l16;
            u4 selv;
#pragma unroll
            for (int d = 0; d < 4; ++d) {
                const unsigned lo = ((d >> 1) == hf && ((4 * g + ((2 * d) & 3)) & 7) == q) ? 0x3F80u : 0u;
                const unsigned hi = ((d >> 1) == hf && ((4 * g + ((2 * d + 1) & 3)) & 7) == q) ? 0x3F800000u : 0u;
                selv[d] = lo | hi;
            }
            asm volatile(ASM_R :: [l16] "v"(l16), [idx] "v"(idx), [sel] "v"(selv), [aS] "v"(aS), [aX] "v"(aX), [cnt] "v"(cnt), [rdS] "v"(rdS), [pubS] "v"(pubS), [rdys] "v"(rdys),
                         [one16] "v"(one16), [rdyw] "v"(rdyw), [rdy0] "v"(rdy0), [rdy0b] "v"(rdy0b), [stpo] "v"(stpo), [selm] "s"((sel & 0xffffu) | (pol << 16)), [thr] "s"(p.thr), DBGOP [rsH] "s"(rsH), [rsW] "s"(rsW),
                         [f] "s"(f), [woff] "s"(OFF_WA + (h * 7 + f) * 4 * 14336), [nw] "s"(j ? 3 : 4), [xmoff] "s"(OFF_SXM + (h * 2 + j) * 1024),
                         [xqoff] "s"(OFF_XQ + f * 1024) : ASM_CLOB);
        } else {
            const unsigned aS = lb + wave * 256 + (q * 8 + 2 * g + hf) * 4;
            const unsigned pubE = hf ? OOB : (unsigned)(h * 512 + (g * 8 + q) * 16);
            asm volatile(ASM_X :: [l16] "v"(l16), [cnt] "v"(cnt), [rdS] "v"(rdS), [pubS] "v"(pubS), [rdys] "v"(rdys), [aS] "v"(aS), [pubE] "v"(pubE), [flg] "v"(flg),
                         [one16] "v"(one16), [rdyw] "v"(rdyw), [rdy0] "v"(rdy0), [rdy0b] "v"(rdy0b), [stpo] "v"(stpo), [selm] "s"((sel & 0xffffu) | (pol << 16)), [thr] "s"(p.thr), DBGOP [rsH] "s"(rsH), [rsW] "s"(rsW),
                         [kpoff] "s"(h * 28 * 1024), [kpb] "s"(OFF_KP) : ASM_CLOB);
        }
    } else {
        const int ct = rk;
        const unsigned flg = C_FU + ((ct >> 2) * 16 + wave * 4 + (lane & 3)) * 128;
        const unsigned vmoff = (ct >> 1) * 16384 + (ct & 1) * 4096 + wave * 1024;
        if (isM) {
            asm volatile(ASM_M :: [l16] "v"(l16), [idx] "v"(idx), [rdyw] "v"(rdyw), [rdy0] "v"(rdy0), [rdy0b] "v"(rdy0b), [stpo] "v"(stpo), [selm] "s"((sel & 0xffffu) | (pol << 16)), [thr] "s"(p.thr), DBGOP
                         [rsH] "s"(rsH), [rsW] "s"(rsW), [woff] "s"(OFF_WB + (4 * wave * 28 + ct) * 14336), [vmoff] "s"(vmoff), [d1] "s"(p.dM) : ASM_CLOB);
        } else {
            const u4 sent = {~0u, ~0u, ~0u, ~0u};
            const unsigned per0 = (p.a0 + p.ga + p.b0 + p.gb + 2 * p.cold) | ((p.a0 + p.ga + p.cold) << 16);
            for (int k = ct * 4 + wave; k < 264; k += 112) {
                const unsigned o = k >= 256 ? OFF_SXM + 62 * 32768 + (k - 256) * 8192 : k < 128 ? k * 16384 + (sel & 1u) * 8192 : (2u << 20) + (k - 128) * 16384 + (sel & 2u) * 4096;
                if (k == 0) {
#pragma unroll
                    for (int f = 0; f < 7; ++f) {
                        const unsigned* mp = (const unsigned*)(p.mem0 + q * 448 + 64 * f + 16 * g + 2 * hf);
                        u4 mv = {mp[0], mp[2], mp[4], mp[6]};
#pragma unroll
                        for (int d = 0; d < 4; ++d) mv[d] = min(mv[d], 0xFFFFFFFEu);
                        st1(mv, rW, l16, o + f * 1024);
                    }
                } else if (k == 192) {
                    st1(u4{((unsigned)__builtin_amdgcn_s_memrealtime() << 3) & ~1u, per0, 0, 0}, rW, lane * 128, o);
                } else {
                    const int np = (k < 128 && !(k & 1)) ? 7 : 8;
                    for (int f = 0; f < np; ++f) st1(sent, rW, l16, o + f * 1024);
                }
            }
            TSTAMP(14);
            const unsigned eo = (4 * wave + 2 * hf) * 512 + (g * 8 + q) * 16, so = (4 * wave + 2 * hf + (g >> 1)) * 512 + q * 32 + (g & 1) * 16;
            const unsigned aO = lb + wave * 512 + lane * 8, rdO = lb + lane * 8, cnt = lb + 2048 + lane * 4;
            const unsigned pubM = lane < 16 ? (unsigned)(ct * 256 + lane * 16) : OOB, mlo = hf ? 0u : ~0u;
            const unsigned rdyv = lane == 0 ? (unsigned)(ct * 128) : OOB, prog = (lane == 0 && ct == 0) ? C_PROG : OOB;
            asm volatile(ASM_V :: [l16] "v"(l16), [eo] "v"(eo), [so] "v"(so), [aO] "v"(aO), [rdO] "v"(rdO), [cnt] "v"(cnt), [pubM] "v"(pubM), [mlo] "v"(mlo), [flg] "v"(flg),
                         [rdyv] "v"(rdyv), [paro] "v"(28u * 128 + 4), [prog] "v"(prog), [one16] "v"(one16), [rdyw] "v"(rdyw), [rdy0] "v"(rdy0), [rdy0b] "v"(rdy0b), [stpo] "v"(stpo), [selm] "s"((sel & 0xffffu) | (pol << 16)), [thr] "s"(p.thr), DBGOP
                         [rsH] "s"(rsH), [rsW] "s"(rsW), [vmoff] "s"(vmoff), [uboff] "s"(OFF_UB + blk2((ct * 16 + 4 * wave) * 1024)),
                         [d1] "s"(p.dV) : ASM_CLOB);
        }
    }
    TSTAMP(7);
}

#define T_A0 520
#define T_B0 480
#define T_GA 0
#define T_GB 70
#define T_COLD 150
#define T_DV 150
#define T_DM 35
#define T_THR 5
#define T_NEAR 0
#define T_BULK 1
#define T_LEAD 4
#define T_DEC 2
#define T_OUTL 40
#define T_BUMP 6
#define T_FAR 120

extern "C" void kernel_launch(void* const* d_in, const int* in_sizes, int n_in, void* d_out, int out_size, void* d_ws, size_t ws_size, hipStream_t stream) {
    Params p;
    p.x = (const us*)d_in[0]; p.mem0 = (const us*)d_in[1]; p.Wq = (const us*)d_in[2]; p.Wk = (const us*)d_in[3]; p.Wv = (const us*)d_in[4]; p.Wo = (const us*)d_in[5];
    p.w1 = (const us*)d_in[8]; p.b1 = (const us*)d_in[9]; p.w2 = (const us*)d_in[10]; p.b2 = (const us*)d_in[11];
    p.out = (us*)d_out;
    p.ws = (char*)(((size_t)d_ws + (2u << 20) - 1) & ~(size_t)((2u << 20) - 1));
    p.cold = T_COLD; p.a0 = T_A0; p.b0 = T_B0; p.ga = T_GA; p.gb = T_GB; p.dV = T_DV; p.dM = T_DM; p.thr = T_THR; p.nearffn = T_NEAR; p.farffn = T_FAR; p.bulk = T_BULK; p.lead = T_LEAD; p.dec = T_DEC; p.outl = T_OUTL; p.bump = T_BUMP; p.pad0 = 0;
    (void)hipFuncSetAttribute((const void*)k_prep, hipFuncAttributeMaxDynamicSharedMemorySize, LDS_PREP);
    (void)hipFuncSetAttribute((const void*)k_main, hipFuncAttributeMaxDynamicSharedMemorySize, LDS_MAIN);
    k_prep<<<256, 256, LDS_PREP, stream>>>(p);
    k_main<<<256, 256, LDS_MAIN, stream>>>(p);
}
```
